```python
import torch

def _softmax(x, axis=-1):
    return torch.softmax(x, dim=axis)
def _gelu(x):
    return torch.nn.functional.gelu(x, approximate="tanh")
def _silu(x):
    return torch.nn.functional.silu(x)
def _sigmoid(x):
    return torch.sigmoid(x)
def _layernorm(x, axis=-1, eps=1e-5):
    m = x.mean(dim=axis, keepdim=True)
    v = x.var(dim=axis, keepdim=True, unbiased=False)
    return (x - m) / torch.sqrt(v + eps)
def _scatter_add(tgt, idx, src, axis=0):
    return tgt.clone().index_add_(axis, idx, src)
def _segment_sum(src, seg, n):
    out = torch.zeros((n,) + src.shape[1:], dtype=src.dtype)
    return out.index_add_(0, seg, src)
def _pad(x, pw):
    flat = tuple(v for pair in reversed(pw) for v in pair)
    return torch.nn.functional.pad(x, flat)

kernel_name = 'parallel_sum__softmax_full_mha__glu_gated_glu__prompt'

def setup_inputs():

    _w = (lambda a: (lambda t: t.to(torch.bfloat16) if t.is_floating_point() else t)(torch.as_tensor(a)))
    def _A_setup():
        B, S, D = 1, 2048, 576
        return {
            "x": _w((lambda *s: torch.randn(*s, dtype=torch.bfloat16) * 0.05)(B, S, D)),
            "wqkv": _w((lambda *s: torch.randn(*s, dtype=torch.bfloat16) * 0.2)(D, 3 * D)),
            "wo": _w((lambda *s: torch.randn(*s, dtype=torch.bfloat16) * 0.1)(D, D)),
        }
    _A_in = _A_setup()

    def _B_setup():
        B, S, D, F = 1, 2048, 576, 4608
        return {
            "x": _w((lambda *s: torch.randn(*s, dtype=torch.bfloat16) * 0.05)(B, S, D)),
            "w_gate": _w((lambda *s: torch.randn(*s, dtype=torch.bfloat16) * 0.8)(D, F)),
            "w_up": _w((lambda *s: torch.randn(*s, dtype=torch.bfloat16) * 0.1)(D, F)),
            "w_down": _w((lambda *s: torch.randn(*s, dtype=torch.bfloat16) * 0.1)(F, D)),
        }
    _B_in = _B_setup()

    return {
        "A_x": _A_in["x"],
        "A_wqkv": _A_in["wqkv"],
        "A_wo": _A_in["wo"],
        "B_x": _B_in["x"],
        "B_w_gate": _B_in["w_gate"],
        "B_w_up": _B_in["w_up"],
        "B_w_down": _B_in["w_down"],
    }

def reference(A_x, A_wqkv, A_wo, B_x, B_w_gate, B_w_up, B_w_down):
    def _A_ref(x, wqkv, wo):
        B, S, D = x.shape
        H = 32; Dh = D // H
        qkv = (lambda x, *d: x.permute(*d))((x @ wqkv).reshape(B, S, 3, H, Dh), 2, 0, 3, 1, 4)
        q, k, v = qkv[0], qkv[1], qkv[2]
        scale = Dh ** -0.5
        scores = torch.einsum("bhqd,bhkd->bhqk", q, k) * scale
        p = _softmax(scores, axis=-1)
        o = torch.einsum("bhqk,bhkd->bhqd", p, v)
        return (lambda x, *d: x.permute(*d))(o, 0, 2, 1, 3).reshape(B, S, D) @ wo

    def _B_ref(x, w_gate, w_up, w_down):
        g = torch.einsum("bsd,df->bsf", x, w_gate)
        u = torch.einsum("bsd,df->bsf", x, w_up)
        h = _sigmoid(g) * u
        return torch.einsum("bsf,fd->bsd", h, w_down)

    x = A_x
    xn = _layernorm(x, axis=-1)
    return x + _A_ref(xn, A_wqkv, A_wo) + _B_ref(xn, B_w_gate, B_w_up, B_w_down)
```

```cpp
#include <hip/hip_runtime.h>
#include <hip/hip_bf16.h>
typedef unsigned short u16;
static __device__ __forceinline__ float b2f(u16 x){ return __uint_as_float(((unsigned)x)<<16); }
static __device__ __forceinline__ u16 f2b(float f){ unsigned u=__float_as_uint(f); u += 0x7fffu + ((u>>16)&1u); return (u16)(u>>16); }
static __device__ __forceinline__ float rb(float f){ return b2f(f2b(f)); }
constexpr int S=2048, D=576, H=32, DH=18, F=4608;

__global__ void ln_k(const u16* x, u16* xn){
  int row=blockIdx.x; int t=threadIdx.x;
  float v[9]; float s=0;
  for(int i=0;i<9;i++){ v[i]=b2f(x[row*D+t+64*i]); s+=v[i]; }
  for(int o=32;o;o>>=1) s+=__shfl_xor(s,o);
  float m=s/D; float q=0;
  for(int i=0;i<9;i++){ float d=v[i]-m; q+=d*d; }
  for(int o=32;o;o>>=1) q+=__shfl_xor(q,o);
  float var=rb(q/D); float mb=rb(m);
  float sd=rb(sqrtf(rb(var+1e-5f)));
  for(int i=0;i<9;i++){ xn[row*D+t+64*i]=f2b(rb(v[i]-mb)/sd); }
}
__global__ void gemm_k(const u16* A, const u16* B, float* C, int M, int N, int K){
  __shared__ float As[16][17], Bs[16][17];
  int tx=threadIdx.x, ty=threadIdx.y; int r=blockIdx.y*16+ty, c=blockIdx.x*16+tx;
  float acc=0;
  for(int k0=0;k0<K;k0+=16){
    As[ty][tx]=b2f(A[(size_t)r*K+k0+tx]); Bs[ty][tx]=b2f(B[(size_t)(k0+ty)*N+c]);
    __syncthreads();
    for(int k=0;k<16;k++) acc+=As[ty][k]*Bs[k][tx];
    __syncthreads();
  }
  C[(size_t)r*N+c]=acc;
}
__global__ void attn_k(const float* qkv, u16* o){
  int h=blockIdx.y; int q=blockIdx.x*64+threadIdx.x;
  float qv[DH]; for(int d=0;d<DH;d++) qv[d]=rb(qkv[(size_t)q*3*D+h*DH+d]);
  float m=-1e30f,l=0,acc[DH]; for(int d=0;d<DH;d++)acc[d]=0;
  float scale=rsqrtf(18.f);
  for(int k=0;k<S;k++){
    float s=0; for(int d=0;d<DH;d++) s+=qv[d]*rb(qkv[(size_t)k*3*D+D+h*DH+d]);
    s*=scale;
    float mn=fmaxf(m,s); float a=__expf(m-mn); float p=__expf(s-mn);
    l=l*a+p; for(int d=0;d<DH;d++) acc[d]=acc[d]*a+p*rb(qkv[(size_t)k*3*D+2*D+h*DH+d]);
    m=mn;
  }
  for(int d=0;d<DH;d++) o[(size_t)q*D+h*DH+d]=f2b(acc[d]/l);
}
__global__ void glu_k(const float* g, const float* u, u16* h){
  size_t i=(size_t)blockIdx.x*256+threadIdx.x;
  float gg=rb(g[i]), uu=rb(u[i]);
  h[i]=f2b(rb(1.f/(1.f+__expf(-gg)))*uu);
}
__global__ void fin_k(const u16* x, const float* a, const float* b, u16* out){
  size_t i=(size_t)blockIdx.x*256+threadIdx.x;
  out[i]=f2b(rb(b2f(x[i])+rb(a[i]))+rb(b[i]));
}
extern "C" void kernel_launch(void* const* d_in, const int* in_sizes, int n_in,
                              void* d_out, int out_size, void* d_ws, size_t ws_size,
                              hipStream_t stream) {
  const u16* x=(const u16*)d_in[0]; const u16* wqkv=(const u16*)d_in[1]; const u16* wo=(const u16*)d_in[2];
  const u16* wg=(const u16*)d_in[4]; const u16* wu=(const u16*)d_in[5]; const u16* wd=(const u16*)d_in[6];
  char* ws=(char*)d_ws;
  u16* xn=(u16*)ws; ws+= (size_t)S*D*2;
  float* qkv=(float*)ws; ws+=(size_t)S*3*D*4;
  u16* o=(u16*)ws; ws+=(size_t)S*D*2;
  float* g=(float*)ws; ws+=(size_t)S*F*4;
  float* u=(float*)ws; ws+=(size_t)S*F*4;
  u16* h=(u16*)ws; ws+=(size_t)S*F*2;
  float* a=(float*)ws; ws+=(size_t)S*D*4;
  float* b=(float*)ws; ws+=(size_t)S*D*4;
  ln_k<<<S,64,0,stream>>>(x,xn);
  gemm_k<<<dim3(3*D/16,S/16),dim3(16,16),0,stream>>>(xn,wqkv,qkv,S,3*D,D);
  attn_k<<<dim3(S/64,H),64,0,stream>>>(qkv,o);
  gemm_k<<<dim3(F/16,S/16),dim3(16,16),0,stream>>>(xn,wg,g,S,F,D);
  gemm_k<<<dim3(F/16,S/16),dim3(16,16),0,stream>>>(xn,wu,u,S,F,D);
  glu_k<<<S*F/256,256,0,stream>>>(g,u,h);
  gemm_k<<<dim3(D/16,S/16),dim3(16,16),0,stream>>>(o,wo,a,S,D,D);
  gemm_k<<<dim3(D/16,S/16),dim3(16,16),0,stream>>>(h,wd,b,S,D,F);
  fin_k<<<S*D/256,256,0,stream>>>(x,a,b,(u16*)d_out);
}
```

```cpp
#include <hip/hip_runtime.h>
#include <stdint.h>

typedef unsigned short u16;
typedef __bf16 bf16x8 __attribute__((ext_vector_type(8)));
typedef __bf16 bf16x2 __attribute__((ext_vector_type(2)));
typedef short s16x4 __attribute__((ext_vector_type(4)));
typedef short s16x8 __attribute__((ext_vector_type(8)));
typedef float f32x2 __attribute__((ext_vector_type(2)));
typedef float f32x4 __attribute__((ext_vector_type(4)));
typedef float f32x16 __attribute__((ext_vector_type(16)));
typedef unsigned u32x2 __attribute__((ext_vector_type(2)));
typedef unsigned u32x4 __attribute__((ext_vector_type(4)));
#define DI __device__ __forceinline__
#define LAS __attribute__((address_space(3)))
typedef __amdgpu_buffer_rsrc_t rsrc_t;

constexpr int SEQ = 2048, DM = 576, FF = 4608;
constexpr unsigned QT = 1280, KVT = 2560;
constexpr size_t Q_OFF = 0;
constexpr size_t KV_OFF = Q_OFF + (size_t)32 * 64 * QT;
constexpr size_t H_OFF = KV_OFF + (size_t)32 * 64 * KVT;
constexpr size_t P_OFF = H_OFF + (size_t)128 * 152 * 1024;
constexpr size_t O_OFF = P_OFF + (size_t)5 * 64 * 18 * 2048;
constexpr size_t FL_OFF = O_OFF + (size_t)64 * 36 * 1024;
constexpr size_t WS_END = FL_OFF + 49152;

DI unsigned rfl(unsigned x) { return __builtin_amdgcn_readfirstlane(x); }
DI unsigned cvtpk(float lo, float hi) { f32x2 v = {lo, hi}; bf16x2 b = __builtin_convertvector(v, bf16x2); return __builtin_bit_cast(unsigned, b); }
DI float rbf(float f) { return __uint_as_float(cvtpk(f, f) << 16); }
DI rsrc_t mkrsrc(const void* p, unsigned bytes) { return __builtin_amdgcn_make_buffer_rsrc((void*)p, 0, bytes, 0x00020000); }
DI f32x4 mfma16(u32x4 a, u32x4 b, f32x4 c) { return __builtin_amdgcn_mfma_f32_16x16x32_bf16(__builtin_bit_cast(bf16x8, a), __builtin_bit_cast(bf16x8, b), c, 0, 0, 0); }
DI f32x16 mfma32(u32x4 a, u32x4 b, f32x16 c) { return __builtin_amdgcn_mfma_f32_32x32x16_bf16(__builtin_bit_cast(bf16x8, a), __builtin_bit_cast(bf16x8, b), c, 0, 0, 0); }
DI f32x16 mfma32x8(u32x2 a, u32x2 b, f32x16 c) { return __builtin_amdgcn_mfma_f32_32x32x8bf16_1k(__builtin_bit_cast(s16x4, a), __builtin_bit_cast(s16x4, b), c, 0, 0, 0); }
DI u32x4 mksrd(const void* p, unsigned bytes) {
  const unsigned long long a = (unsigned long long)p;
  return (u32x4){rfl((unsigned)a), rfl((unsigned)(a >> 32) & 0xffffu), rfl(bytes), 0x00020000u};
}
DI void dma16(u32x4 r, unsigned voff, unsigned soff, unsigned ldsdst) {
  asm volatile("s_mov_b32 m0, %0\n\ts_nop 0\n\tbuffer_load_dwordx4 %1, %2, %3 offen lds" :: "s"(ldsdst), "v"(voff), "s"(r), "s"(soff) : "memory");
}
DI void ld128(u32x4& d, u32x4 r, unsigned voff, unsigned soff) { asm volatile("buffer_load_dwordx4 %0, %1, %2, %3 offen" : "=v"(d) : "v"(voff), "s"(r), "s"(soff) : "memory"); }
DI void ld32(unsigned& d, u32x4 r, unsigned voff, unsigned soff) { asm volatile("buffer_load_dword %0, %1, %2, %3 offen" : "=v"(d) : "v"(voff), "s"(r), "s"(soff) : "memory"); }
DI unsigned ssel(bool c, unsigned a, unsigned b) { return rfl(c ? a : b); }
template <int N> DI void waitvm2(u32x4& a, u32x4& b) { asm volatile("s_waitcnt vmcnt(%2)" : "+v"(a), "+v"(b) : "n"(N) : "memory"); }
DI void ldsw128(unsigned a, u32x4 v) { *(LAS u32x4*)(uintptr_t)a = v; }
DI void st128(u32x4 v, rsrc_t r, unsigned voff, unsigned soff) {
  __builtin_amdgcn_raw_buffer_store_b128(v, r, voff, soff, 0);
  asm volatile("s_nop 1" :: "v"(v) : "memory");
}
DI u32x2 trread(unsigned a) { return __builtin_bit_cast(u32x2, __builtin_amdgcn_ds_read_tr16_b64_v4i16((LAS s16x4*)(uintptr_t)a)); }
DI u32x4 ldsr128(unsigned a) { return *(const LAS u32x4*)(uintptr_t)a; }
DI unsigned ldsr32(unsigned a) { return *(const LAS unsigned*)(uintptr_t)a; }
template <int N> DI void waitvm() { asm volatile("s_waitcnt vmcnt(%0)" :: "n"(N) : "memory"); }
DI void barrier() { asm volatile("s_waitcnt lgkmcnt(0)\n\ts_barrier" ::: "memory"); }
#define SB() __builtin_amdgcn_sched_barrier(0)
#define STAMP(i) do {} while (0)
#define STAMPC(i) do {} while (0)
#define STAMPSEL 0xFFFFFFFFu
#define STK 1
#define ST(i) do { if (KID == STK && ((STAMPSEL >> (i)) & 1)) { STAMP(i); STAMPC(i); } } while (0)
#define HEAD_ASM_TEXT \
    "v_mbcnt_lo_u32_b32 v47, -1, 0\n" \
    "s_mov_b64 s[36:37], %1\n" \
    "v_mbcnt_hi_u32_b32 v47, -1, v47\n" \
    "s_mov_b32 s38, 2359296\n" \
    "s_mov_b32 s39, 0x20000\n" \
    "s_mov_b64 s[48:49], %2\n" \
    "s_mul_i32 s54, s48, 36864\n" \
    "s_mov_b32 s66, s48\n" \
    "s_mov_b32 s55, s49\n" \
    "v_lshrrev_b32 v41, 3, v47\n" \
    "v_and_b32 v42, 7, v47\n" \
    "v_lshrrev_b32 v43, 1, v41\n" \
    "v_xor_b32 v42, v42, v43\n" \
    "v_lshlrev_b32 v42, 4, v42\n" \
    "s_movk_i32 s57, 1152\n" \
    "v_mad_u32_u24 v9, v41, s57, v42\n" \
    "v_xor_b32 v42, 64, v42\n" \
    "v_mad_u32_u24 v10, v41, s57, v42\n" \
    "v_add_u32 v10, 8192, v10\n" \
    "s_add_u32 m0, s55, 0\n" \
    "s_add_u32 s57, s54, 0\n" \
    "buffer_load_dwordx4 v9, s[36:39], s57 offen lds\n" \
    "buffer_load_dwordx4 v10, s[36:39], s57 offen offset:1024 lds\n" \
    "s_add_u32 m0, s55, 2048\n" \
    "s_add_u32 s57, s54, 128\n" \
    "buffer_load_dwordx4 v9, s[36:39], s57 offen lds\n" \
    "buffer_load_dwordx4 v10, s[36:39], s57 offen offset:1024 lds\n" \
    "s_add_u32 m0, s55, 4096\n" \
    "s_add_u32 s57, s54, 256\n" \
    "buffer_load_dwordx4 v9, s[36:39], s57 offen lds\n" \
    "buffer_load_dwordx4 v10, s[36:39], s57 offen offset:1024 lds\n" \
    "s_add_u32 m0, s55, 6144\n" \
    "s_add_u32 s57, s54, 384\n" \
    "buffer_load_dwordx4 v9, s[36:39], s57 offen lds\n" \
    "buffer_load_dwordx4 v10, s[36:39], s57 offen offset:1024 lds\n" \
    "s_add_u32 m0, s55, 8192\n" \
    "s_add_u32 s57, s54, 512\n" \
    "buffer_load_dwordx4 v9, s[36:39], s57 offen lds\n" \
    "buffer_load_dwordx4 v10, s[36:39], s57 offen offset:1024 lds\n" \
    "s_add_u32 m0, s55, 10240\n" \
    "s_add_u32 s57, s54, 640\n" \
    "buffer_load_dwordx4 v9, s[36:39], s57 offen lds\n" \
    "buffer_load_dwordx4 v10, s[36:39], s57 offen offset:1024 lds\n" \
    "s_add_u32 m0, s55, 12288\n" \
    "s_add_u32 s57, s54, 768\n" \
    "buffer_load_dwordx4 v9, s[36:39], s57 offen lds\n" \
    "buffer_load_dwordx4 v10, s[36:39], s57 offen offset:1024 lds\n" \
    "s_add_u32 m0, s55, 14336\n" \
    "s_add_u32 s57, s54, 896\n" \
    "buffer_load_dwordx4 v9, s[36:39], s57 offen lds\n" \
    "buffer_load_dwordx4 v10, s[36:39], s57 offen offset:1024 lds\n" \
    "s_add_u32 m0, s55, 16384\n" \
    "s_add_u32 s57, s54, 1024\n" \
    "buffer_load_dwordx4 v9, s[36:39], s57 offen lds\n" \
    "buffer_load_dwordx4 v10, s[36:39], s57 offen offset:1024 lds\n" \
    "v_mul_u32_u24 v41, 36, v47\n" \
    "s_mul_i32 s48, %3, 2304\n" \
    "buffer_load_dword a251, v41, s[36:39], s48 offen\n" \
    "s_getpc_b64 s[48:49]\n" \
    "FUSED_CODE_BEGIN:\n" \
    "s_lshr_b32 s57, %3, 5\n" \
    "s_lshl_b32 s57, s57, 2\n" \
    "s_and_b32 s66, %3, 3\n" \
    "s_add_u32 s57, s57, s66\n" \
    "s_min_u32 s57, s57, (FUSED_CODE_END-FUSED_CODE_BEGIN)/2304-1\n" \
    "s_mul_i32 s57, s57, 2304\n" \
    "s_add_u32 s48, s48, s57\n" \
    "s_addc_u32 s49, s49, 0\n" \
    "global_load_dword a251, v41, s[48:49]\n" \
    "s_mov_b64 s[48:49], %4\n" \
    "s_add_u32 s48, s48, 41943040\n" \
    "s_addc_u32 s49, s49, 0\n" \
    "v_mov_b32 v42, 0\n" \
    "global_load_dword a250, v42, s[48:49] sc1\n" \
    "global_load_dword a251, v41, %5\n" \
    "global_load_dword a251, v41, %6\n" \
    "global_load_dword a251, v41, %7\n"
#define HEAD_ASM_CLOB "v9", "v10", "v41", "v42", "v43", "v47", "a250", "a251", "s36", "s37", "s38", "s39", "s48", "s49", "s54", "s55", "s57", "s66", "scc", "vcc", "memory"
#define HSTRIDE 147456u
#define MAIN_ASM_TEXT \
    "v_mbcnt_lo_u32_b32 v47, -1, 0\n" \
    "s_mov_b64 s[36:37], %2\n" \
    "v_mbcnt_hi_u32_b32 v47, -1, v47\n" \
    "s_mov_b32 s38, 2359296\n" \
    "s_mov_b32 s39, 0x20000\n" \
    "s_mov_b64 s[48:49], %8\n" \
    "s_mul_i32 s54, s48, 36864\n" \
    "s_mov_b32 s66, s48\n" \
    "s_mov_b32 s55, s49\n" \
    "v_lshrrev_b32 v41, 3, v47\n" \
    "v_and_b32 v42, 7, v47\n" \
    "v_lshrrev_b32 v43, 1, v41\n" \
    "v_xor_b32 v42, v42, v43\n" \
    "v_lshlrev_b32 v42, 4, v42\n" \
    "s_movk_i32 s57, 1152\n" \
    "v_mad_u32_u24 v9, v41, s57, v42\n" \
    "v_xor_b32 v42, 64, v42\n" \
    "v_mad_u32_u24 v10, v41, s57, v42\n" \
    "v_add_u32 v10, 8192, v10\n" \
    "s_mov_b64 s[40:41], %3\n" \
    "s_mov_b32 s42, 41992192\n" \
    "s_mov_b32 s43, 0x20000\n" \
    "s_mov_b32 s47, 0x20000\n" \
    "v_lshlrev_b32 v8, 4, v47\n" \
    "v_mov_b64 v[22:23], %0\n" \
    "v_mov_b32 v21, v22\n" \
    "v_mov_b32 v22, v23\n" \
    "s_mov_b32 s87, 19\n" \
    "v_mov_b64 v[24:25], %16\n" \
    "v_mov_b32 v23, v24\n" \
    "v_mov_b32 v24, v25\n" \
    "v_mov_b64 v[26:27], %17\n" \
    "v_mov_b32 v25, v26\n" \
    "v_mov_b32 v26, v27\n" \
    "v_and_b32 v27, 15, v47\n" \
    "v_lshrrev_b32 v28, 4, v47\n" \
    "s_mov_b64 s[48:49], %9\n" \
    "s_mov_b32 s67, s48\n" \
    "s_mov_b32 s68, s49\n" \
    "s_mul_i32 s56, %6, 144\n" \
    "s_mul_i32 s69, %6, 9216\n" \
    "s_add_u32 s69, s69, %7\n" \
    "s_mov_b64 s[48:49], %10\n" \
    "s_mov_b32 s53, s48\n" \
    "s_mov_b32 s52, s48\n" \
    "s_mov_b32 s73, s49\n" \
    "s_mov_b32 s51, 0\n" \
    "s_mov_b32 s50, 0\n" \
    "s_nop 0\n" \
    "v_lshrrev_b32 v41, 1, v27\n" \
    "v_xor_b32 v41, v41, v28\n" \
    "v_lshlrev_b32 v41, 4, v41\n" \
    "v_lshl_add_u32 v11, v27, 7, v41\n" \
    "v_add_u32 v11, s55, v11\n" \
    "v_xor_b32 v12, 64, v11\n" \
    "v_lshrrev_b32 v41, 2, v27\n" \
    "v_lshl_add_u32 v13, v41, 4, v27\n" \
    "v_lshlrev_b32 v13, 2, v13\n" \
    "v_lshrrev_b32 v14, 2, v47\n" \
    "v_and_b32 v41, 1, v28\n" \
    "v_lshlrev_b32 v42, 1, v41\n" \
    "v_and_b32 v43, 3, v47\n" \
    "v_xor_b32 v15, v42, v43\n" \
    "v_lshlrev_b32 v15, 4, v15\n" \
    "v_lshlrev_b32 v19, 6, v14\n" \
    "v_lshl_add_u32 v19, v41, 5, v19\n" \
    "v_lshl_add_u32 v19, v43, 3, v19\n" \
    "v_add_u32 v19, %7, v19\n" \
    "v_add_u32 v16, s52, v19\n" \
    "v_xor_b32 v17, 32, v16\n" \
    "s_add_u32 s52, s52, 36864\n" \
    "s_cmp_eq_u32 s52, 147456\n" \
    "s_cselect_b32 s52, 0, s52\n" \
    "v_add_u32 v18, s52, v19\n" \
    "v_and_b32 v41, 3, v27\n" \
    "v_cmp_eq_u32 s[60:61], 1, v41\n" \
    "v_cmp_eq_u32 s[62:63], 2, v41\n" \
    "v_cmp_eq_u32 s[64:65], 3, v41\n" \
    "v_and_b32 v41, 1, v27\n" \
    "v_lshlrev_b32 v41, 4, v41\n" \
    "v_mov_b32 v42, 0x3f80\n" \
    "v_lshlrev_b32 v42, v41, v42\n" \
    "v_lshrrev_b32 v41, 1, v27\n" \
    "v_lshlrev_b32 v43, 2, v28\n" \
    "v_sub_u32 v41, v41, v43\n" \
    "v_cmp_eq_u32 vcc, 0, v41\n" \
    "v_cmp_eq_u32 s[70:71], -8, v41\n" \
    "v_mov_b32 v56, 0x3f803f80\n" \
    "v_cndmask_b32 v48, 0, v42, vcc\n" \
    "v_cndmask_b32 v52, 0, v42, s[70:71]\n" \
    "v_cmp_eq_u32 vcc, 1, v41\n" \
    "v_cmp_eq_u32 s[70:71], -7, v41\n" \
    "v_mov_b32 v57, 0x3f803f80\n" \
    "v_cndmask_b32 v49, 0, v42, vcc\n" \
    "v_cndmask_b32 v53, 0, v42, s[70:71]\n" \
    "v_cmp_eq_u32 vcc, 2, v41\n" \
    "v_cmp_eq_u32 s[70:71], -6, v41\n" \
    "v_mov_b32 v58, 0x3f803f80\n" \
    "v_cndmask_b32 v50, 0, v42, vcc\n" \
    "v_cndmask_b32 v54, 0, v42, s[70:71]\n" \
    "v_cmp_eq_u32 vcc, 3, v41\n" \
    "v_cmp_eq_u32 s[70:71], -5, v41\n" \
    "v_mov_b32 v59, 0x3f803f80\n" \
    "v_cndmask_b32 v51, 0, v42, vcc\n" \
    "v_cndmask_b32 v55, 0, v42, s[70:71]\n" \
    "v_lshrrev_b32 v200, 4, v8\n" \
    "s_movk_i32 s84, 5462\n" \
    "s_movk_i32 s85, 1152\n" \
    "v_add_u32 v202, 0, v200\n" \
    "v_mul_u32_u24 v203, s84, v202\n" \
    "v_lshrrev_b32 v203, 16, v203\n" \
    "v_mul_u32_u24 v204, 12, v203\n" \
    "v_sub_u32 v202, v202, v204\n" \
    "v_lshrrev_b32 v204, 1, v203\n" \
    "v_and_b32 v204, 2, v204\n" \
    "v_xor_b32 v202, v202, v204\n" \
    "v_lshlrev_b32 v202, 4, v202\n" \
    "v_mad_u32_u24 v204, v203, s85, v202\n" \
    "v_add_u32 v204, 2048, v204\n" \
    "v_accvgpr_write_b32 a232, v204\n" \
    "v_add_u32 v202, 64, v200\n" \
    "v_mul_u32_u24 v203, s84, v202\n" \
    "v_lshrrev_b32 v203, 16, v203\n" \
    "v_mul_u32_u24 v204, 12, v203\n" \
    "v_sub_u32 v202, v202, v204\n" \
    "v_lshrrev_b32 v204, 1, v203\n" \
    "v_and_b32 v204, 2, v204\n" \
    "v_xor_b32 v202, v202, v204\n" \
    "v_lshlrev_b32 v202, 4, v202\n" \
    "v_mad_u32_u24 v204, v203, s85, v202\n" \
    "v_add_u32 v204, 1024, v204\n" \
    "v_accvgpr_write_b32 a233, v204\n" \
    "v_add_u32 v202, 128, v200\n" \
    "v_mul_u32_u24 v203, s84, v202\n" \
    "v_lshrrev_b32 v203, 16, v203\n" \
    "v_mul_u32_u24 v204, 12, v203\n" \
    "v_sub_u32 v202, v202, v204\n" \
    "v_lshrrev_b32 v204, 1, v203\n" \
    "v_and_b32 v204, 2, v204\n" \
    "v_xor_b32 v202, v202, v204\n" \
    "v_lshlrev_b32 v202, 4, v202\n" \
    "v_mad_u32_u24 v204, v203, s85, v202\n" \
    "v_add_u32 v204, 0, v204\n" \
    "v_accvgpr_write_b32 a234, v204\n" \
    "s_lshr_b32 s84, %6, 1\n" \
    "s_mul_i32 s84, s84, 10240\n" \
    "s_add_u32 s84, s84, %7\n" \
    "v_add_u32 v202, s84, v8\n" \
    "v_accvgpr_write_b32 a235, v202\n" \
    "v_and_b32 v202, 31, v200\n" \
    "v_lshl_add_u32 v203, v202, 2, s84\n" \
    "v_add_u32 v203, 2304, v203\n" \
    "v_accvgpr_write_b32 a236, v203\n" \
    "v_and_b32 v203, 27, v200\n" \
    "v_cmp_eq_u32 vcc, 18, v203\n" \
    "v_mov_b32 v203, 19\n" \
    "v_lshrrev_b32 v204, 5, v200\n" \
    "v_cndmask_b32 v203, v203, 18, vcc\n" \
    "v_cmp_gt_u32 vcc, 18, v202\n" \
    "v_mul_u32_u24 v204, 320, v204\n" \
    "v_add_u32 v204, s84, v204\n" \
    "v_cndmask_b32 v202, v203, v202, vcc\n" \
    "v_lshl_add_u32 v202, v202, 4, v204\n" \
    "v_add_u32 v202, 1024, v202\n" \
    "v_accvgpr_write_b32 a237, v202\n" \
    "v_lshrrev_b32 v202, 2, v200\n" \
    "v_mul_u32_u24 v203, 192, v202\n" \
    "v_bfe_u32 v202, v200, 4, 1\n" \
    "v_lshl_add_u32 v203, v202, 5, v203\n" \
    "v_and_b32 v202, 3, v200\n" \
    "v_lshl_add_u32 v203, v202, 3, v203\n" \
    "v_add_u32 v203, %7, v203\n" \
    "v_add_u32 v203, 20480, v203\n" \
    "v_accvgpr_write_b32 a238, v203\n" \
    "v_mov_b64 v[128:129], %16\n" \
    "s_mul_i32 s58, %6, 6144\n" \
    "v_readlane_b32 s84, v128, 43\n" \
    "s_add_u32 s58, s58, %7\n" \
    "s_add_u32 s58, s58, 65536\n" \
    "v_add_u32 v130, s58, v8\n" \
    "s_xor_b32 s59, %6, 2\n" \
    "s_mul_i32 s59, s59, 6144\n" \
    "s_add_u32 s59, s59, %7\n" \
    "s_add_u32 s59, s59, 65536\n" \
    "v_add_u32 v131, s59, v8\n" \
    "v_lshrrev_b32 v132, 4, v8\n" \
    "v_bfe_u32 v133, v132, 4, 1\n" \
    "v_mul_u32_u24 v133, 18432, v133\n" \
    "v_and_b32 v134, 15, v132\n" \
    "v_lshl_add_u32 v133, v134, 4, v133\n" \
    "v_lshrrev_b32 v134, 5, v132\n" \
    "v_lshl_add_u32 v135, v134, 8, v133\n" \
    "v_add_u32 v133, s84, v133\n" \
    "v_lshlrev_b32 v134, 31, v134\n" \
    "v_or_b32 v133, v133, v134\n" \
    "v_and_b32 v136, 15, v132\n" \
    "v_mul_u32_u24 v136, 1152, v136\n" \
    "v_bfe_u32 v137, v132, 4, 1\n" \
    "v_lshl_add_u32 v136, v137, 5, v136\n" \
    "v_lshrrev_b32 v137, 5, v132\n" \
    "v_lshl_add_u32 v136, v137, 4, v136\n" \
    "v_bfe_u32 v140, v132, 2, 2\n" \
    "v_lshrrev_b32 v141, 4, v132\n" \
    "v_lshrrev_b32 v142, 1, v141\n" \
    "v_mul_u32_u24 v142, 18, v142\n" \
    "v_and_b32 v143, 1, v141\n" \
    "v_lshl_add_u32 v142, v143, 2, v142\n" \
    "v_add_u32 v142, v142, v140\n" \
    "v_lshrrev_b32 v143, 1, v140\n" \
    "v_lshl_add_u32 v143, v141, 2, v143\n" \
    "v_mul_u32_u24 v143, 18, v143\n" \
    "v_and_b32 v140, 1, v140\n" \
    "v_add3_u32 v143, v143, v140, 16\n" \
    "v_and_b32 v140, 3, v132\n" \
    "v_lshlrev_b32 v140, 3, v140\n" \
    "v_add_u32 v140, %7, v140\n" \
    "v_mul_u32_u24 v144, 192, v142\n" \
    "v_bfe_u32 v141, v142, 2, 1\n" \
    "v_lshl_add_u32 v144, v141, 5, v144\n" \
    "v_add_u32 v144, v144, v140\n" \
    "v_xor_b32 v145, 32, v144\n" \
    "v_mul_u32_u24 v146, 192, v143\n" \
    "v_bfe_u32 v141, v143, 2, 1\n" \
    "v_lshl_add_u32 v146, v141, 5, v146\n" \
    "v_add_u32 v146, v146, v140\n" \
    "v_xor_b32 v147, 32, v146\n" \
    "v_add_u32 v148, 90112, v144\n" \
    "v_add_u32 v149, 90112, v145\n" \
    "v_add_u32 v158, 90112, v146\n" \
    "v_add_u32 v159, 90112, v147\n" \
    "v_add_u32 v150, 36864, v148\n" \
    "v_add_u32 v151, 36864, v149\n" \
    "v_add_u32 v141, 360, v142\n" \
    "v_cmp_gt_u32 vcc, 0x180, v141\n" \
    "v_add_u32 v152, 159232, v144\n" \
    "v_subrev_u32 v141, 4608, v144\n" \
    "v_cndmask_b32 v152, v141, v152, vcc\n" \
    "v_xor_b32 v153, 32, v152\n" \
    "v_add_u32 v141, 368, v142\n" \
    "v_cmp_gt_u32 vcc, 0x180, v141\n" \
    "v_add_u32 v154, 160768, v144\n" \
    "v_subrev_u32 v141, 3072, v144\n" \
    "v_cndmask_b32 v154, v141, v154, vcc\n" \
    "v_xor_b32 v155, 32, v154\n" \
    "v_add_u32 v141, 288, v143\n" \
    "v_cmp_gt_u32 vcc, 0x180, v141\n" \
    "v_add_u32 v160, 145408, v146\n" \
    "v_subrev_u32 v141, 18432, v146\n" \
    "v_cndmask_b32 v160, v141, v160, vcc\n" \
    "v_xor_b32 v161, 32, v160\n" \
    "v_add_u32 v141, 324, v143\n" \
    "v_cmp_gt_u32 vcc, 0x180, v141\n" \
    "v_add_u32 v162, 152320, v147\n" \
    "v_subrev_u32 v141, 11520, v147\n" \
    "v_cndmask_b32 v162, v141, v162, vcc\n" \
    "v_xor_b32 v163, 32, v162\n" \
    "v_accvgpr_write_b32 a239, v130\n" \
    "v_accvgpr_write_b32 a240, v131\n" \
    "v_accvgpr_write_b32 a241, v133\n" \
    "v_accvgpr_write_b32 a242, v135\n" \
    "v_accvgpr_write_b32 a243, v136\n" \
    "v_accvgpr_write_b32 a244, v144\n" \
    "v_accvgpr_write_b32 a245, v148\n" \
    "v_accvgpr_write_b32 a246, v150\n" \
    "v_accvgpr_write_b32 a247, v152\n" \
    "v_accvgpr_write_b32 a248, v154\n" \
    "v_accvgpr_write_b32 a249, v158\n" \
    "v_accvgpr_write_b32 a252, v160\n" \
    "v_accvgpr_write_b32 a253, v162\n" \
    "s_waitcnt vmcnt(22)\n" \
    "ds_read_b128 a[0:3], v11 offset:0\n" \
    "ds_read_b128 a[4:7], v12 offset:0\n" \
    "s_waitcnt vmcnt(20)\n" \
    "ds_read_b128 a[8:11], v11 offset:2048\n" \
    "ds_read_b128 a[12:15], v12 offset:2048\n" \
    "s_waitcnt lgkmcnt(2)\n" \
    "s_add_u32 m0, s55, 0\n" \
    "s_add_u32 s57, s54, 18432\n" \
    "buffer_load_dwordx4 v9, s[36:39], s57 offen lds\n" \
    "buffer_load_dwordx4 v10, s[36:39], s57 offen offset:1024 lds\n" \
    "v_mfma_f32_16x16x32_bf16 v[64:67], v[56:59], a[0:3], 0\n" \
    "v_mfma_f32_16x16x32_bf16 v[68:71], a[0:3], a[0:3], 0\n" \
    "v_mfma_f32_16x16x32_bf16 v[64:67], v[56:59], a[4:7], v[64:67]\n" \
    "v_mfma_f32_16x16x32_bf16 v[68:71], a[4:7], a[4:7], v[68:71]\n" \
    "s_waitcnt vmcnt(20)\n" \
    "ds_read_b128 a[16:19], v11 offset:4096\n" \
    "ds_read_b128 a[20:23], v12 offset:4096\n" \
    "s_waitcnt lgkmcnt(2)\n" \
    "s_add_u32 m0, s55, 2048\n" \
    "s_add_u32 s57, s54, 18560\n" \
    "buffer_load_dwordx4 v9, s[36:39], s57 offen lds\n" \
    "buffer_load_dwordx4 v10, s[36:39], s57 offen offset:1024 lds\n" \
    "v_mfma_f32_16x16x32_bf16 v[64:67], v[56:59], a[8:11], v[64:67]\n" \
    "v_mfma_f32_16x16x32_bf16 v[68:71], a[8:11], a[8:11], v[68:71]\n" \
    "v_mfma_f32_16x16x32_bf16 v[64:67], v[56:59], a[12:15], v[64:67]\n" \
    "v_mfma_f32_16x16x32_bf16 v[68:71], a[12:15], a[12:15], v[68:71]\n" \
    "s_waitcnt vmcnt(20)\n" \
    "ds_read_b128 a[24:27], v11 offset:6144\n" \
    "ds_read_b128 a[28:31], v12 offset:6144\n" \
    "s_waitcnt lgkmcnt(2)\n" \
    "s_add_u32 m0, s55, 4096\n" \
    "s_add_u32 s57, s54, 18688\n" \
    "buffer_load_dwordx4 v9, s[36:39], s57 offen lds\n" \
    "buffer_load_dwordx4 v10, s[36:39], s57 offen offset:1024 lds\n" \
    "v_mfma_f32_16x16x32_bf16 v[64:67], v[56:59], a[16:19], v[64:67]\n" \
    "v_mfma_f32_16x16x32_bf16 v[68:71], a[16:19], a[16:19], v[68:71]\n" \
    "v_mfma_f32_16x16x32_bf16 v[64:67], v[56:59], a[20:23], v[64:67]\n" \
    "v_mfma_f32_16x16x32_bf16 v[68:71], a[20:23], a[20:23], v[68:71]\n" \
    "s_waitcnt vmcnt(20)\n" \
    "ds_read_b128 a[32:35], v11 offset:8192\n" \
    "ds_read_b128 a[36:39], v12 offset:8192\n" \
    "s_waitcnt lgkmcnt(2)\n" \
    "s_add_u32 m0, s55, 6144\n" \
    "s_add_u32 s57, s54, 18816\n" \
    "buffer_load_dwordx4 v9, s[36:39], s57 offen lds\n" \
    "buffer_load_dwordx4 v10, s[36:39], s57 offen offset:1024 lds\n" \
    "v_mfma_f32_16x16x32_bf16 v[64:67], v[56:59], a[24:27], v[64:67]\n" \
    "v_mfma_f32_16x16x32_bf16 v[68:71], a[24:27], a[24:27], v[68:71]\n" \
    "v_mfma_f32_16x16x32_bf16 v[64:67], v[56:59], a[28:31], v[64:67]\n" \
    "v_mfma_f32_16x16x32_bf16 v[68:71], a[28:31], a[28:31], v[68:71]\n" \
    "s_waitcnt vmcnt(20)\n" \
    "ds_read_b128 a[40:43], v11 offset:10240\n" \
    "ds_read_b128 a[44:47], v12 offset:10240\n" \
    "s_waitcnt lgkmcnt(2)\n" \
    "s_add_u32 m0, s55, 8192\n" \
    "s_add_u32 s57, s54, 18944\n" \
    "buffer_load_dwordx4 v9, s[36:39], s57 offen lds\n" \
    "buffer_load_dwordx4 v10, s[36:39], s57 offen offset:1024 lds\n" \
    "v_mfma_f32_16x16x32_bf16 v[64:67], v[56:59], a[32:35], v[64:67]\n" \
    "v_mfma_f32_16x16x32_bf16 v[68:71], a[32:35], a[32:35], v[68:71]\n" \
    "v_mfma_f32_16x16x32_bf16 v[64:67], v[56:59], a[36:39], v[64:67]\n" \
    "v_mfma_f32_16x16x32_bf16 v[68:71], a[36:39], a[36:39], v[68:71]\n" \
    "s_waitcnt vmcnt(20)\n" \
    "ds_read_b128 a[48:51], v11 offset:12288\n" \
    "ds_read_b128 a[52:55], v12 offset:12288\n" \
    "s_waitcnt lgkmcnt(2)\n" \
    "s_add_u32 m0, s55, 10240\n" \
    "s_add_u32 s57, s54, 19072\n" \
    "buffer_load_dwordx4 v9, s[36:39], s57 offen lds\n" \
    "buffer_load_dwordx4 v10, s[36:39], s57 offen offset:1024 lds\n" \
    "v_mfma_f32_16x16x32_bf16 v[64:67], v[56:59], a[40:43], v[64:67]\n" \
    "v_mfma_f32_16x16x32_bf16 v[68:71], a[40:43], a[40:43], v[68:71]\n" \
    "v_mfma_f32_16x16x32_bf16 v[64:67], v[56:59], a[44:47], v[64:67]\n" \
    "v_mfma_f32_16x16x32_bf16 v[68:71], a[44:47], a[44:47], v[68:71]\n" \
    "s_waitcnt vmcnt(20)\n" \
    "ds_read_b128 a[56:59], v11 offset:14336\n" \
    "ds_read_b128 a[60:63], v12 offset:14336\n" \
    "s_waitcnt lgkmcnt(2)\n" \
    "s_add_u32 m0, s55, 12288\n" \
    "s_add_u32 s57, s54, 19200\n" \
    "buffer_load_dwordx4 v9, s[36:39], s57 offen lds\n" \
    "buffer_load_dwordx4 v10, s[36:39], s57 offen offset:1024 lds\n" \
    "v_mfma_f32_16x16x32_bf16 v[64:67], v[56:59], a[48:51], v[64:67]\n" \
    "v_mfma_f32_16x16x32_bf16 v[68:71], a[48:51], a[48:51], v[68:71]\n" \
    "v_mfma_f32_16x16x32_bf16 v[64:67], v[56:59], a[52:55], v[64:67]\n" \
    "v_mfma_f32_16x16x32_bf16 v[68:71], a[52:55], a[52:55], v[68:71]\n" \
    "s_waitcnt vmcnt(20)\n" \
    "ds_read_b128 a[64:67], v11 offset:16384\n" \
    "ds_read_b128 a[68:71], v12 offset:16384\n" \
    "s_waitcnt lgkmcnt(2)\n" \
    "s_add_u32 m0, s55, 14336\n" \
    "s_add_u32 s57, s54, 19328\n" \
    "buffer_load_dwordx4 v9, s[36:39], s57 offen lds\n" \
    "buffer_load_dwordx4 v10, s[36:39], s57 offen offset:1024 lds\n" \
    "v_mfma_f32_16x16x32_bf16 v[64:67], v[56:59], a[56:59], v[64:67]\n" \
    "v_mfma_f32_16x16x32_bf16 v[68:71], a[56:59], a[56:59], v[68:71]\n" \
    "v_mfma_f32_16x16x32_bf16 v[64:67], v[56:59], a[60:63], v[64:67]\n" \
    "v_mfma_f32_16x16x32_bf16 v[68:71], a[60:63], a[60:63], v[68:71]\n" \
    "s_waitcnt vmcnt(14)\n" \
    "ds_read_b128 a[72:75], v11 offset:0\n" \
    "ds_read_b128 a[76:79], v12 offset:0\n" \
    "s_waitcnt lgkmcnt(2)\n" \
    "s_add_u32 m0, s55, 16384\n" \
    "s_add_u32 s57, s54, 19456\n" \
    "buffer_load_dwordx4 v9, s[36:39], s57 offen lds\n" \
    "buffer_load_dwordx4 v10, s[36:39], s57 offen offset:1024 lds\n" \
    "v_mfma_f32_16x16x32_bf16 v[64:67], v[56:59], a[64:67], v[64:67]\n" \
    "v_mfma_f32_16x16x32_bf16 v[68:71], a[64:67], a[64:67], v[68:71]\n" \
    "v_mfma_f32_16x16x32_bf16 v[64:67], v[56:59], a[68:71], v[64:67]\n" \
    "v_mfma_f32_16x16x32_bf16 v[68:71], a[68:71], a[68:71], v[68:71]\n" \
    "s_waitcnt vmcnt(14)\n" \
    "ds_read_b128 a[80:83], v11 offset:2048\n" \
    "ds_read_b128 a[84:87], v12 offset:2048\n" \
    "s_waitcnt lgkmcnt(2)\n" \
    "s_add_u32 m0, s55, 0\n" \
    "s_add_u32 s57, s54, 36864\n" \
    "buffer_load_dwordx4 v9, s[36:39], s57 offen lds\n" \
    "buffer_load_dwordx4 v10, s[36:39], s57 offen offset:1024 lds\n" \
    "v_mfma_f32_16x16x32_bf16 v[72:75], v[56:59], a[72:75], 0\n" \
    "v_mfma_f32_16x16x32_bf16 v[76:79], a[72:75], a[72:75], 0\n" \
    "v_mfma_f32_16x16x32_bf16 v[72:75], v[56:59], a[76:79], v[72:75]\n" \
    "v_mfma_f32_16x16x32_bf16 v[76:79], a[76:79], a[76:79], v[76:79]\n" \
    "v_accvgpr_read_b32 v108, a250\n" \
    "v_add_u32 v108, 1, v108\n" \
    "v_xor_b32 v109, 0x5bd1e995, v108\n" \
    "v_mov_b64 %0, v[108:109]\n" \
    "s_mov_b32 s82, 0\n" \
    "s_mov_b64 s[84:85], %15\n" \
    "s_sub_u32 s84, s84, 41951232\n" \
    "s_lshl_b32 s85, %6, 1\n" \
    "s_add_u32 s84, s84, s85\n" \
    "s_mul_i32 s84, s84, 2560\n" \
    "s_add_u32 s84, s84, 2622752\n" \
    "v_lshrrev_b32 v108, 7, v8\n" \
    "v_mul_u32_u24 v108, 2560, v108\n" \
    "v_bfe_u32 v109, v8, 5, 2\n" \
    "v_mul_u32_u24 v109, 320, v109\n" \
    "v_add_u32 v108, v108, v109\n" \
    "v_and_b32 v109, 16, v8\n" \
    "v_add_u32 v108, v108, v109\n" \
    "v_cmp_eq_u32 vcc, 0, v109\n" \
    "v_mov_b32 v109, 0x3f803f80\n" \
    "s_nop 0\n" \
    "v_cndmask_b32 v104, 0, v109, vcc\n" \
    "v_cndmask_b32 v105, 0, v109, vcc\n" \
    "v_cndmask_b32 v106, 0, v109, vcc\n" \
    "v_cndmask_b32 v107, 0, v109, vcc\n" \
    "s_mov_b64 exec, 0xffff\n" \
    "buffer_store_dwordx4 v[104:107], v108, s[40:43], s84 offen sc1\n" \
    "s_mov_b64 exec, -1\n" \
    "v_cndmask_b32 v41, v68, v69, s[60:61]\n" \
    "v_cndmask_b32 v41, v41, v70, s[62:63]\n" \
    "v_cndmask_b32 v41, v41, v71, s[64:65]\n" \
    "v_mul_f32 v42, 0x3ae38e39, v64\n" \
    "ds_bpermute_b32 v41, v13, v41\n" \
    "v_cvt_pk_bf16_f32 v43, v42, v42\n" \
    "v_lshlrev_b32 v43, 16, v43\n" \
    "v_sub_f32 v60, 0, v43\n" \
    "v_sub_f32 v61, 0, v43\n" \
    "v_sub_f32 v62, 0, v43\n" \
    "v_sub_f32 v63, 0, v43\n" \
    "s_waitcnt vmcnt(15)\n" \
    "ds_read_b128 a[88:91], v11 offset:4096\n" \
    "ds_read_b128 a[92:95], v12 offset:4096\n" \
    "s_waitcnt lgkmcnt(3)\n" \
    "s_add_u32 m0, s55, 2048\n" \
    "s_add_u32 s57, s54, 36992\n" \
    "buffer_load_dwordx4 v9, s[36:39], s57 offen lds\n" \
    "buffer_load_dwordx4 v10, s[36:39], s57 offen offset:1024 lds\n" \
    "v_mfma_f32_16x16x32_bf16 v[80:83], v[48:51], a[0:3], v[60:63]\n" \
    "v_mfma_f32_16x16x32_bf16 v[84:87], v[52:55], a[0:3], v[60:63]\n" \
    "v_mfma_f32_16x16x32_bf16 v[72:75], v[56:59], a[80:83], v[72:75]\n" \
    "v_mfma_f32_16x16x32_bf16 v[76:79], a[80:83], a[80:83], v[76:79]\n" \
    "v_mfma_f32_16x16x32_bf16 v[88:91], v[48:51], a[4:7], v[60:63]\n" \
    "v_mfma_f32_16x16x32_bf16 v[92:95], v[52:55], a[4:7], v[60:63]\n" \
    "v_mfma_f32_16x16x32_bf16 v[72:75], v[56:59], a[84:87], v[72:75]\n" \
    "v_mfma_f32_16x16x32_bf16 v[76:79], a[84:87], a[84:87], v[76:79]\n" \
    "s_waitcnt vmcnt(15)\n" \
    "ds_read_b128 a[96:99], v11 offset:6144\n" \
    "ds_read_b128 a[100:103], v12 offset:6144\n" \
    "s_waitcnt lgkmcnt(2)\n" \
    "s_add_u32 m0, s55, 4096\n" \
    "s_add_u32 s57, s54, 37120\n" \
    "buffer_load_dwordx4 v9, s[36:39], s57 offen lds\n" \
    "buffer_load_dwordx4 v10, s[36:39], s57 offen offset:1024 lds\n" \
    "v_mfma_f32_16x16x32_bf16 v[96:99], v[48:51], a[8:11], v[60:63]\n" \
    "v_cvt_pk_bf16_f32 v104, v80, v81\n" \
    "v_cvt_pk_bf16_f32 v105, v82, v83\n" \
    "v_mfma_f32_16x16x32_bf16 v[100:103], v[52:55], a[8:11], v[60:63]\n" \
    "v_cvt_pk_bf16_f32 v106, v84, v85\n" \
    "v_cvt_pk_bf16_f32 v107, v86, v87\n" \
    "v_mfma_f32_16x16x32_bf16 v[72:75], v[56:59], a[88:91], v[72:75]\n" \
    "v_accvgpr_write_b32 a0, v104\n" \
    "v_accvgpr_write_b32 a1, v105\n" \
    "v_mfma_f32_16x16x32_bf16 v[76:79], a[88:91], a[88:91], v[76:79]\n" \
    "v_accvgpr_write_b32 a2, v106\n" \
    "v_accvgpr_write_b32 a3, v107\n" \
    "v_mfma_f32_16x16x32_bf16 v[80:83], v[48:51], a[12:15], v[60:63]\n" \
    "v_cvt_pk_bf16_f32 v104, v88, v89\n" \
    "v_cvt_pk_bf16_f32 v105, v90, v91\n" \
    "v_mfma_f32_16x16x32_bf16 v[84:87], v[52:55], a[12:15], v[60:63]\n" \
    "v_cvt_pk_bf16_f32 v106, v92, v93\n" \
    "v_cvt_pk_bf16_f32 v107, v94, v95\n" \
    "v_mfma_f32_16x16x32_bf16 v[72:75], v[56:59], a[92:95], v[72:75]\n" \
    "v_accvgpr_write_b32 a4, v104\n" \
    "v_accvgpr_write_b32 a5, v105\n" \
    "v_mfma_f32_16x16x32_bf16 v[76:79], a[92:95], a[92:95], v[76:79]\n" \
    "v_accvgpr_write_b32 a6, v106\n" \
    "v_accvgpr_write_b32 a7, v107\n" \
    "v_mul_f32 v41, 0x3ae38e39, v41\n" \
    "v_fma_f32 v41, -v42, v42, v41\n" \
    "v_max_f32 v41, 0, v41\n" \
    "v_cvt_pk_bf16_f32 v41, v41, v41\n" \
    "v_lshlrev_b32 v41, 16, v41\n" \
    "v_add_f32 v41, 0x3727c5ac, v41\n" \
    "v_cvt_pk_bf16_f32 v41, v41, v41\n" \
    "v_lshlrev_b32 v41, 16, v41\n" \
    "v_sqrt_f32 v41, v41\n" \
    "s_nop 0\n" \
    "v_cvt_pk_bf16_f32 v29, v41, v41\n" \
    "v_lshlrev_b32 v29, 16, v29\n" \
    "v_rcp_f32 v37, v29\n" \
    "s_nop 0\n" \
    "v_mul_f32 v33, 0xbfb8aa3b, v37\n" \
    "s_waitcnt vmcnt(15)\n" \
    "ds_read_b128 a[104:107], v11 offset:8192\n" \
    "ds_read_b128 a[108:111], v12 offset:8192\n" \
    "s_waitcnt lgkmcnt(2)\n" \
    "s_add_u32 m0, s55, 6144\n" \
    "s_add_u32 s57, s54, 37248\n" \
    "buffer_load_dwordx4 v9, s[36:39], s57 offen lds\n" \
    "buffer_load_dwordx4 v10, s[36:39], s57 offen offset:1024 lds\n" \
    "v_mfma_f32_16x16x32_bf16 v[88:91], v[48:51], a[16:19], v[60:63]\n" \
    "v_cvt_pk_bf16_f32 v104, v96, v97\n" \
    "v_cvt_pk_bf16_f32 v105, v98, v99\n" \
    "v_mfma_f32_16x16x32_bf16 v[92:95], v[52:55], a[16:19], v[60:63]\n" \
    "v_cvt_pk_bf16_f32 v106, v100, v101\n" \
    "v_cvt_pk_bf16_f32 v107, v102, v103\n" \
    "v_mfma_f32_16x16x32_bf16 v[72:75], v[56:59], a[96:99], v[72:75]\n" \
    "v_accvgpr_write_b32 a8, v104\n" \
    "v_accvgpr_write_b32 a9, v105\n" \
    "v_mfma_f32_16x16x32_bf16 v[76:79], a[96:99], a[96:99], v[76:79]\n" \
    "v_accvgpr_write_b32 a10, v106\n" \
    "v_accvgpr_write_b32 a11, v107\n" \
    "v_mfma_f32_16x16x32_bf16 v[96:99], v[48:51], a[20:23], v[60:63]\n" \
    "v_cvt_pk_bf16_f32 v104, v80, v81\n" \
    "v_cvt_pk_bf16_f32 v105, v82, v83\n" \
    "v_mfma_f32_16x16x32_bf16 v[100:103], v[52:55], a[20:23], v[60:63]\n" \
    "v_cvt_pk_bf16_f32 v106, v84, v85\n" \
    "v_cvt_pk_bf16_f32 v107, v86, v87\n" \
    "v_mfma_f32_16x16x32_bf16 v[72:75], v[56:59], a[100:103], v[72:75]\n" \
    "v_accvgpr_write_b32 a12, v104\n" \
    "v_accvgpr_write_b32 a13, v105\n" \
    "v_mfma_f32_16x16x32_bf16 v[76:79], a[100:103], a[100:103], v[76:79]\n" \
    "v_accvgpr_write_b32 a14, v106\n" \
    "v_accvgpr_write_b32 a15, v107\n" \
    "s_waitcnt vmcnt(15)\n" \
    "ds_read_b128 a[112:115], v11 offset:10240\n" \
    "ds_read_b128 a[116:119], v12 offset:10240\n" \
    "s_waitcnt lgkmcnt(2)\n" \
    "s_add_u32 m0, s55, 8192\n" \
    "s_add_u32 s57, s54, 37376\n" \
    "buffer_load_dwordx4 v9, s[36:39], s57 offen lds\n" \
    "buffer_load_dwordx4 v10, s[36:39], s57 offen offset:1024 lds\n" \
    "v_mfma_f32_16x16x32_bf16 v[80:83], v[48:51], a[24:27], v[60:63]\n" \
    "v_cvt_pk_bf16_f32 v104, v88, v89\n" \
    "v_cvt_pk_bf16_f32 v105, v90, v91\n" \
    "v_mfma_f32_16x16x32_bf16 v[84:87], v[52:55], a[24:27], v[60:63]\n" \
    "v_cvt_pk_bf16_f32 v106, v92, v93\n" \
    "v_cvt_pk_bf16_f32 v107, v94, v95\n" \
    "v_mfma_f32_16x16x32_bf16 v[72:75], v[56:59], a[104:107], v[72:75]\n" \
    "v_accvgpr_write_b32 a16, v104\n" \
    "v_accvgpr_write_b32 a17, v105\n" \
    "v_mfma_f32_16x16x32_bf16 v[76:79], a[104:107], a[104:107], v[76:79]\n" \
    "v_accvgpr_write_b32 a18, v106\n" \
    "v_accvgpr_write_b32 a19, v107\n" \
    "v_mfma_f32_16x16x32_bf16 v[88:91], v[48:51], a[28:31], v[60:63]\n" \
    "v_cvt_pk_bf16_f32 v104, v96, v97\n" \
    "v_cvt_pk_bf16_f32 v105, v98, v99\n" \
    "v_mfma_f32_16x16x32_bf16 v[92:95], v[52:55], a[28:31], v[60:63]\n" \
    "v_cvt_pk_bf16_f32 v106, v100, v101\n" \
    "v_cvt_pk_bf16_f32 v107, v102, v103\n" \
    "v_mfma_f32_16x16x32_bf16 v[72:75], v[56:59], a[108:111], v[72:75]\n" \
    "v_accvgpr_write_b32 a20, v104\n" \
    "v_accvgpr_write_b32 a21, v105\n" \
    "v_mfma_f32_16x16x32_bf16 v[76:79], a[108:111], a[108:111], v[76:79]\n" \
    "v_accvgpr_write_b32 a22, v106\n" \
    "v_accvgpr_write_b32 a23, v107\n" \
    "s_waitcnt vmcnt(15)\n" \
    "ds_read_b128 a[120:123], v11 offset:12288\n" \
    "ds_read_b128 a[124:127], v12 offset:12288\n" \
    "s_waitcnt lgkmcnt(2)\n" \
    "s_add_u32 m0, s55, 10240\n" \
    "s_add_u32 s57, s54, 37504\n" \
    "buffer_load_dwordx4 v9, s[36:39], s57 offen lds\n" \
    "buffer_load_dwordx4 v10, s[36:39], s57 offen offset:1024 lds\n" \
    "v_mfma_f32_16x16x32_bf16 v[96:99], v[48:51], a[32:35], v[60:63]\n" \
    "v_cvt_pk_bf16_f32 v104, v80, v81\n" \
    "v_cvt_pk_bf16_f32 v105, v82, v83\n" \
    "v_mfma_f32_16x16x32_bf16 v[100:103], v[52:55], a[32:35], v[60:63]\n" \
    "v_cvt_pk_bf16_f32 v106, v84, v85\n" \
    "v_cvt_pk_bf16_f32 v107, v86, v87\n" \
    "v_mfma_f32_16x16x32_bf16 v[72:75], v[56:59], a[112:115], v[72:75]\n" \
    "v_accvgpr_write_b32 a24, v104\n" \
    "v_accvgpr_write_b32 a25, v105\n" \
    "v_mfma_f32_16x16x32_bf16 v[76:79], a[112:115], a[112:115], v[76:79]\n" \
    "v_accvgpr_write_b32 a26, v106\n" \
    "v_accvgpr_write_b32 a27, v107\n" \
    "v_mfma_f32_16x16x32_bf16 v[80:83], v[48:51], a[36:39], v[60:63]\n" \
    "v_cvt_pk_bf16_f32 v104, v88, v89\n" \
    "v_cvt_pk_bf16_f32 v105, v90, v91\n" \
    "v_mfma_f32_16x16x32_bf16 v[84:87], v[52:55], a[36:39], v[60:63]\n" \
    "v_cvt_pk_bf16_f32 v106, v92, v93\n" \
    "v_cvt_pk_bf16_f32 v107, v94, v95\n" \
    "v_mfma_f32_16x16x32_bf16 v[72:75], v[56:59], a[116:119], v[72:75]\n" \
    "v_accvgpr_write_b32 a28, v104\n" \
    "v_accvgpr_write_b32 a29, v105\n" \
    "v_mfma_f32_16x16x32_bf16 v[76:79], a[116:119], a[116:119], v[76:79]\n" \
    "v_accvgpr_write_b32 a30, v106\n" \
    "v_accvgpr_write_b32 a31, v107\n" \
    "s_waitcnt vmcnt(15)\n" \
    "ds_read_b128 a[128:131], v11 offset:14336\n" \
    "ds_read_b128 a[132:135], v12 offset:14336\n" \
    "s_waitcnt lgkmcnt(2)\n" \
    "s_add_u32 m0, s55, 12288\n" \
    "s_add_u32 s57, s54, 37632\n" \
    "buffer_load_dwordx4 v9, s[36:39], s57 offen lds\n" \
    "buffer_load_dwordx4 v10, s[36:39], s57 offen offset:1024 lds\n" \
    "v_mfma_f32_16x16x32_bf16 v[88:91], v[48:51], a[40:43], v[60:63]\n" \
    "v_cvt_pk_bf16_f32 v104, v96, v97\n" \
    "v_cvt_pk_bf16_f32 v105, v98, v99\n" \
    "v_mfma_f32_16x16x32_bf16 v[92:95], v[52:55], a[40:43], v[60:63]\n" \
    "v_cvt_pk_bf16_f32 v106, v100, v101\n" \
    "v_cvt_pk_bf16_f32 v107, v102, v103\n" \
    "v_mfma_f32_16x16x32_bf16 v[72:75], v[56:59], a[120:123], v[72:75]\n" \
    "v_accvgpr_write_b32 a32, v104\n" \
    "v_accvgpr_write_b32 a33, v105\n" \
    "v_mfma_f32_16x16x32_bf16 v[76:79], a[120:123], a[120:123], v[76:79]\n" \
    "v_accvgpr_write_b32 a34, v106\n" \
    "v_accvgpr_write_b32 a35, v107\n" \
    "v_mfma_f32_16x16x32_bf16 v[96:99], v[48:51], a[44:47], v[60:63]\n" \
    "v_cvt_pk_bf16_f32 v104, v80, v81\n" \
    "v_cvt_pk_bf16_f32 v105, v82, v83\n" \
    "v_mfma_f32_16x16x32_bf16 v[100:103], v[52:55], a[44:47], v[60:63]\n" \
    "v_cvt_pk_bf16_f32 v106, v84, v85\n" \
    "v_cvt_pk_bf16_f32 v107, v86, v87\n" \
    "v_mfma_f32_16x16x32_bf16 v[72:75], v[56:59], a[124:127], v[72:75]\n" \
    "v_accvgpr_write_b32 a36, v104\n" \
    "v_accvgpr_write_b32 a37, v105\n" \
    "v_mfma_f32_16x16x32_bf16 v[76:79], a[124:127], a[124:127], v[76:79]\n" \
    "v_accvgpr_write_b32 a38, v106\n" \
    "v_accvgpr_write_b32 a39, v107\n" \
    "s_waitcnt vmcnt(15)\n" \
    "ds_read_b128 a[136:139], v11 offset:16384\n" \
    "ds_read_b128 a[140:143], v12 offset:16384\n" \
    "s_waitcnt lgkmcnt(2)\n" \
    "s_add_u32 m0, s55, 14336\n" \
    "s_add_u32 s57, s54, 37760\n" \
    "buffer_load_dwordx4 v9, s[36:39], s57 offen lds\n" \
    "buffer_load_dwordx4 v10, s[36:39], s57 offen offset:1024 lds\n" \
    "v_mfma_f32_16x16x32_bf16 v[80:83], v[48:51], a[48:51], v[60:63]\n" \
    "v_cvt_pk_bf16_f32 v104, v88, v89\n" \
    "v_cvt_pk_bf16_f32 v105, v90, v91\n" \
    "v_mfma_f32_16x16x32_bf16 v[84:87], v[52:55], a[48:51], v[60:63]\n" \
    "v_cvt_pk_bf16_f32 v106, v92, v93\n" \
    "v_cvt_pk_bf16_f32 v107, v94, v95\n" \
    "v_mfma_f32_16x16x32_bf16 v[72:75], v[56:59], a[128:131], v[72:75]\n" \
    "v_accvgpr_write_b32 a40, v104\n" \
    "v_accvgpr_write_b32 a41, v105\n" \
    "v_mfma_f32_16x16x32_bf16 v[76:79], a[128:131], a[128:131], v[76:79]\n" \
    "v_accvgpr_write_b32 a42, v106\n" \
    "v_accvgpr_write_b32 a43, v107\n" \
    "v_mfma_f32_16x16x32_bf16 v[88:91], v[48:51], a[52:55], v[60:63]\n" \
    "v_cvt_pk_bf16_f32 v104, v96, v97\n" \
    "v_cvt_pk_bf16_f32 v105, v98, v99\n" \
    "v_mfma_f32_16x16x32_bf16 v[92:95], v[52:55], a[52:55], v[60:63]\n" \
    "v_cvt_pk_bf16_f32 v106, v100, v101\n" \
    "v_cvt_pk_bf16_f32 v107, v102, v103\n" \
    "v_mfma_f32_16x16x32_bf16 v[72:75], v[56:59], a[132:135], v[72:75]\n" \
    "v_accvgpr_write_b32 a44, v104\n" \
    "v_accvgpr_write_b32 a45, v105\n" \
    "v_mfma_f32_16x16x32_bf16 v[76:79], a[132:135], a[132:135], v[76:79]\n" \
    "v_accvgpr_write_b32 a46, v106\n" \
    "v_accvgpr_write_b32 a47, v107\n" \
    "s_waitcnt vmcnt(15)\n" \
    "ds_read_b128 v[112:115], v11 offset:0\n" \
    "ds_read_b128 v[116:119], v12 offset:0\n" \
    "s_waitcnt lgkmcnt(2)\n" \
    "s_add_u32 m0, s55, 16384\n" \
    "s_add_u32 s57, s54, 37888\n" \
    "buffer_load_dwordx4 v9, s[36:39], s57 offen lds\n" \
    "buffer_load_dwordx4 v10, s[36:39], s57 offen offset:1024 lds\n" \
    "v_mfma_f32_16x16x32_bf16 v[96:99], v[48:51], a[56:59], v[60:63]\n" \
    "v_cvt_pk_bf16_f32 v104, v80, v81\n" \
    "v_cvt_pk_bf16_f32 v105, v82, v83\n" \
    "v_mfma_f32_16x16x32_bf16 v[100:103], v[52:55], a[56:59], v[60:63]\n" \
    "v_cvt_pk_bf16_f32 v106, v84, v85\n" \
    "v_cvt_pk_bf16_f32 v107, v86, v87\n" \
    "v_mfma_f32_16x16x32_bf16 v[72:75], v[56:59], a[136:139], v[72:75]\n" \
    "v_accvgpr_write_b32 a48, v104\n" \
    "v_accvgpr_write_b32 a49, v105\n" \
    "v_mfma_f32_16x16x32_bf16 v[76:79], a[136:139], a[136:139], v[76:79]\n" \
    "v_accvgpr_write_b32 a50, v106\n" \
    "v_accvgpr_write_b32 a51, v107\n" \
    "v_mfma_f32_16x16x32_bf16 v[80:83], v[48:51], a[60:63], v[60:63]\n" \
    "v_cvt_pk_bf16_f32 v104, v88, v89\n" \
    "v_cvt_pk_bf16_f32 v105, v90, v91\n" \
    "v_mfma_f32_16x16x32_bf16 v[84:87], v[52:55], a[60:63], v[60:63]\n" \
    "v_cvt_pk_bf16_f32 v106, v92, v93\n" \
    "v_cvt_pk_bf16_f32 v107, v94, v95\n" \
    "v_mfma_f32_16x16x32_bf16 v[72:75], v[56:59], a[140:143], v[72:75]\n" \
    "v_accvgpr_write_b32 a52, v104\n" \
    "v_accvgpr_write_b32 a53, v105\n" \
    "v_mfma_f32_16x16x32_bf16 v[76:79], a[140:143], a[140:143], v[76:79]\n" \
    "v_accvgpr_write_b32 a54, v106\n" \
    "v_accvgpr_write_b32 a55, v107\n" \
    "s_waitcnt vmcnt(14)\n" \
    "ds_read_b128 v[120:123], v11 offset:2048\n" \
    "ds_read_b128 v[124:127], v12 offset:2048\n" \
    "s_waitcnt lgkmcnt(2)\n" \
    "s_add_u32 m0, s55, 0\n" \
    "s_add_u32 s57, s54, 55296\n" \
    "buffer_load_dwordx4 v9, s[36:39], s57 offen lds\n" \
    "buffer_load_dwordx4 v10, s[36:39], s57 offen offset:1024 lds\n" \
    "v_mfma_f32_16x16x32_bf16 v[88:91], v[48:51], a[64:67], v[60:63]\n" \
    "v_cvt_pk_bf16_f32 v104, v96, v97\n" \
    "v_cvt_pk_bf16_f32 v105, v98, v99\n" \
    "v_mfma_f32_16x16x32_bf16 v[92:95], v[52:55], a[64:67], v[60:63]\n" \
    "v_cvt_pk_bf16_f32 v106, v100, v101\n" \
    "v_cvt_pk_bf16_f32 v107, v102, v103\n" \
    "v_mfma_f32_16x16x32_bf16 v[64:67], v[56:59], v[112:115], 0\n" \
    "v_accvgpr_write_b32 a56, v104\n" \
    "v_accvgpr_write_b32 a57, v105\n" \
    "v_mfma_f32_16x16x32_bf16 v[68:71], v[112:115], v[112:115], 0\n" \
    "v_accvgpr_write_b32 a58, v106\n" \
    "v_accvgpr_write_b32 a59, v107\n" \
    "v_mfma_f32_16x16x32_bf16 v[96:99], v[48:51], a[68:71], v[60:63]\n" \
    "v_cvt_pk_bf16_f32 v104, v80, v81\n" \
    "v_cvt_pk_bf16_f32 v105, v82, v83\n" \
    "v_mfma_f32_16x16x32_bf16 v[100:103], v[52:55], a[68:71], v[60:63]\n" \
    "v_cvt_pk_bf16_f32 v106, v84, v85\n" \
    "v_cvt_pk_bf16_f32 v107, v86, v87\n" \
    "v_mfma_f32_16x16x32_bf16 v[64:67], v[56:59], v[116:119], v[64:67]\n" \
    "v_accvgpr_write_b32 a60, v104\n" \
    "v_accvgpr_write_b32 a61, v105\n" \
    "v_mfma_f32_16x16x32_bf16 v[68:71], v[116:119], v[116:119], v[68:71]\n" \
    "v_accvgpr_write_b32 a62, v106\n" \
    "v_accvgpr_write_b32 a63, v107\n" \
    "v_cvt_pk_bf16_f32 v104, v88, v89\n" \
    "v_cvt_pk_bf16_f32 v105, v90, v91\n" \
    "v_cvt_pk_bf16_f32 v106, v92, v93\n" \
    "v_cvt_pk_bf16_f32 v107, v94, v95\n" \
    "v_accvgpr_write_b32 a64, v104\n" \
    "v_accvgpr_write_b32 a65, v105\n" \
    "v_accvgpr_write_b32 a66, v106\n" \
    "v_accvgpr_write_b32 a67, v107\n" \
    "v_cvt_pk_bf16_f32 v104, v96, v97\n" \
    "v_cvt_pk_bf16_f32 v105, v98, v99\n" \
    "v_cvt_pk_bf16_f32 v106, v100, v101\n" \
    "v_cvt_pk_bf16_f32 v107, v102, v103\n" \
    "v_accvgpr_write_b32 a68, v104\n" \
    "v_accvgpr_write_b32 a69, v105\n" \
    "v_accvgpr_write_b32 a70, v106\n" \
    "v_accvgpr_write_b32 a71, v107\n" \
    "v_cndmask_b32 v41, v76, v77, s[60:61]\n" \
    "v_cndmask_b32 v41, v41, v78, s[62:63]\n" \
    "v_cndmask_b32 v41, v41, v79, s[64:65]\n" \
    "v_mul_f32 v42, 0x3ae38e39, v72\n" \
    "ds_bpermute_b32 v41, v13, v41\n" \
    "v_cvt_pk_bf16_f32 v43, v42, v42\n" \
    "v_lshlrev_b32 v43, 16, v43\n" \
    "v_sub_f32 v60, 0, v43\n" \
    "v_sub_f32 v61, 0, v43\n" \
    "v_sub_f32 v62, 0, v43\n" \
    "v_sub_f32 v63, 0, v43\n" \
    "s_waitcnt vmcnt(14)\n" \
    "ds_read_b128 v[128:131], v11 offset:4096\n" \
    "ds_read_b128 v[132:135], v12 offset:4096\n" \
    "s_waitcnt lgkmcnt(3)\n" \
    "s_add_u32 m0, s55, 2048\n" \
    "s_add_u32 s57, s54, 55424\n" \
    "buffer_load_dwordx4 v9, s[36:39], s57 offen lds\n" \
    "buffer_load_dwordx4 v10, s[36:39], s57 offen offset:1024 lds\n" \
    "v_mfma_f32_16x16x32_bf16 v[80:83], v[48:51], a[72:75], v[60:63]\n" \
    "v_mfma_f32_16x16x32_bf16 v[84:87], v[52:55], a[72:75], v[60:63]\n" \
    "v_mfma_f32_16x16x32_bf16 v[64:67], v[56:59], v[120:123], v[64:67]\n" \
    "v_mfma_f32_16x16x32_bf16 v[68:71], v[120:123], v[120:123], v[68:71]\n" \
    "v_mfma_f32_16x16x32_bf16 v[88:91], v[48:51], a[76:79], v[60:63]\n" \
    "v_mfma_f32_16x16x32_bf16 v[92:95], v[52:55], a[76:79], v[60:63]\n" \
    "v_mfma_f32_16x16x32_bf16 v[64:67], v[56:59], v[124:127], v[64:67]\n" \
    "v_mfma_f32_16x16x32_bf16 v[68:71], v[124:127], v[124:127], v[68:71]\n" \
    "s_waitcnt vmcnt(14)\n" \
    "ds_read_b128 v[136:139], v11 offset:6144\n" \
    "ds_read_b128 v[140:143], v12 offset:6144\n" \
    "s_waitcnt lgkmcnt(2)\n" \
    "s_add_u32 m0, s55, 4096\n" \
    "s_add_u32 s57, s54, 55552\n" \
    "buffer_load_dwordx4 v9, s[36:39], s57 offen lds\n" \
    "buffer_load_dwordx4 v10, s[36:39], s57 offen offset:1024 lds\n" \
    "v_mfma_f32_16x16x32_bf16 v[96:99], v[48:51], a[80:83], v[60:63]\n" \
    "v_cvt_pk_bf16_f32 v104, v80, v81\n" \
    "v_cvt_pk_bf16_f32 v105, v82, v83\n" \
    "v_mfma_f32_16x16x32_bf16 v[100:103], v[52:55], a[80:83], v[60:63]\n" \
    "v_cvt_pk_bf16_f32 v106, v84, v85\n" \
    "v_cvt_pk_bf16_f32 v107, v86, v87\n" \
    "v_mfma_f32_16x16x32_bf16 v[64:67], v[56:59], v[128:131], v[64:67]\n" \
    "v_accvgpr_write_b32 a72, v104\n" \
    "v_accvgpr_write_b32 a73, v105\n" \
    "v_mfma_f32_16x16x32_bf16 v[68:71], v[128:131], v[128:131], v[68:71]\n" \
    "v_accvgpr_write_b32 a74, v106\n" \
    "v_accvgpr_write_b32 a75, v107\n" \
    "v_mfma_f32_16x16x32_bf16 v[80:83], v[48:51], a[84:87], v[60:63]\n" \
    "v_cvt_pk_bf16_f32 v104, v88, v89\n" \
    "v_cvt_pk_bf16_f32 v105, v90, v91\n" \
    "v_mfma_f32_16x16x32_bf16 v[84:87], v[52:55], a[84:87], v[60:63]\n" \
    "v_cvt_pk_bf16_f32 v106, v92, v93\n" \
    "v_cvt_pk_bf16_f32 v107, v94, v95\n" \
    "v_mfma_f32_16x16x32_bf16 v[64:67], v[56:59], v[132:135], v[64:67]\n" \
    "v_accvgpr_write_b32 a76, v104\n" \
    "v_accvgpr_write_b32 a77, v105\n" \
    "v_mfma_f32_16x16x32_bf16 v[68:71], v[132:135], v[132:135], v[68:71]\n" \
    "v_accvgpr_write_b32 a78, v106\n" \
    "v_accvgpr_write_b32 a79, v107\n" \
    "v_mul_f32 v41, 0x3ae38e39, v41\n" \
    "v_fma_f32 v41, -v42, v42, v41\n" \
    "v_max_f32 v41, 0, v41\n" \
    "v_cvt_pk_bf16_f32 v41, v41, v41\n" \
    "v_lshlrev_b32 v41, 16, v41\n" \
    "v_add_f32 v41, 0x3727c5ac, v41\n" \
    "v_cvt_pk_bf16_f32 v41, v41, v41\n" \
    "v_lshlrev_b32 v41, 16, v41\n" \
    "v_sqrt_f32 v41, v41\n" \
    "s_nop 0\n" \
    "v_cvt_pk_bf16_f32 v30, v41, v41\n" \
    "v_lshlrev_b32 v30, 16, v30\n" \
    "v_rcp_f32 v38, v30\n" \
    "s_nop 0\n" \
    "v_mul_f32 v34, 0xbfb8aa3b, v38\n" \
    "s_waitcnt vmcnt(14)\n" \
    "ds_read_b128 v[144:147], v11 offset:8192\n" \
    "ds_read_b128 v[148:151], v12 offset:8192\n" \
    "s_waitcnt lgkmcnt(2)\n" \
    "s_add_u32 m0, s55, 6144\n" \
    "s_add_u32 s57, s54, 55680\n" \
    "buffer_load_dwordx4 v9, s[36:39], s57 offen lds\n" \
    "buffer_load_dwordx4 v10, s[36:39], s57 offen offset:1024 lds\n" \
    "v_mfma_f32_16x16x32_bf16 v[88:91], v[48:51], a[88:91], v[60:63]\n" \
    "v_cvt_pk_bf16_f32 v104, v96, v97\n" \
    "v_cvt_pk_bf16_f32 v105, v98, v99\n" \
    "v_mfma_f32_16x16x32_bf16 v[92:95], v[52:55], a[88:91], v[60:63]\n" \
    "v_cvt_pk_bf16_f32 v106, v100, v101\n" \
    "v_cvt_pk_bf16_f32 v107, v102, v103\n" \
    "v_mfma_f32_16x16x32_bf16 v[64:67], v[56:59], v[136:139], v[64:67]\n" \
    "v_accvgpr_write_b32 a80, v104\n" \
    "v_accvgpr_write_b32 a81, v105\n" \
    "v_mfma_f32_16x16x32_bf16 v[68:71], v[136:139], v[136:139], v[68:71]\n" \
    "v_accvgpr_write_b32 a82, v106\n" \
    "v_accvgpr_write_b32 a83, v107\n" \
    "v_mfma_f32_16x16x32_bf16 v[96:99], v[48:51], a[92:95], v[60:63]\n" \
    "v_cvt_pk_bf16_f32 v104, v80, v81\n" \
    "v_cvt_pk_bf16_f32 v105, v82, v83\n" \
    "v_mfma_f32_16x16x32_bf16 v[100:103], v[52:55], a[92:95], v[60:63]\n" \
    "v_cvt_pk_bf16_f32 v106, v84, v85\n" \
    "v_cvt_pk_bf16_f32 v107, v86, v87\n" \
    "v_mfma_f32_16x16x32_bf16 v[64:67], v[56:59], v[140:143], v[64:67]\n" \
    "v_accvgpr_write_b32 a84, v104\n" \
    "v_accvgpr_write_b32 a85, v105\n" \
    "v_mfma_f32_16x16x32_bf16 v[68:71], v[140:143], v[140:143], v[68:71]\n" \
    "v_accvgpr_write_b32 a86, v106\n" \
    "v_accvgpr_write_b32 a87, v107\n" \
    "s_waitcnt vmcnt(14)\n" \
    "ds_read_b128 v[152:155], v11 offset:10240\n" \
    "ds_read_b128 v[156:159], v12 offset:10240\n" \
    "s_waitcnt lgkmcnt(2)\n" \
    "s_add_u32 m0, s55, 8192\n" \
    "s_add_u32 s57, s54, 55808\n" \
    "buffer_load_dwordx4 v9, s[36:39], s57 offen lds\n" \
    "buffer_load_dwordx4 v10, s[36:39], s57 offen offset:1024 lds\n" \
    "v_mfma_f32_16x16x32_bf16 v[80:83], v[48:51], a[96:99], v[60:63]\n" \
    "v_cvt_pk_bf16_f32 v104, v88, v89\n" \
    "v_cvt_pk_bf16_f32 v105, v90, v91\n" \
    "v_mfma_f32_16x16x32_bf16 v[84:87], v[52:55], a[96:99], v[60:63]\n" \
    "v_cvt_pk_bf16_f32 v106, v92, v93\n" \
    "v_cvt_pk_bf16_f32 v107, v94, v95\n" \
    "v_mfma_f32_16x16x32_bf16 v[64:67], v[56:59], v[144:147], v[64:67]\n" \
    "v_accvgpr_write_b32 a88, v104\n" \
    "v_accvgpr_write_b32 a89, v105\n" \
    "v_mfma_f32_16x16x32_bf16 v[68:71], v[144:147], v[144:147], v[68:71]\n" \
    "v_accvgpr_write_b32 a90, v106\n" \
    "v_accvgpr_write_b32 a91, v107\n" \
    "v_mfma_f32_16x16x32_bf16 v[88:91], v[48:51], a[100:103], v[60:63]\n" \
    "v_cvt_pk_bf16_f32 v104, v96, v97\n" \
    "v_cvt_pk_bf16_f32 v105, v98, v99\n" \
    "v_mfma_f32_16x16x32_bf16 v[92:95], v[52:55], a[100:103], v[60:63]\n" \
    "v_cvt_pk_bf16_f32 v106, v100, v101\n" \
    "v_cvt_pk_bf16_f32 v107, v102, v103\n" \
    "v_mfma_f32_16x16x32_bf16 v[64:67], v[56:59], v[148:151], v[64:67]\n" \
    "v_accvgpr_write_b32 a92, v104\n" \
    "v_accvgpr_write_b32 a93, v105\n" \
    "v_mfma_f32_16x16x32_bf16 v[68:71], v[148:151], v[148:151], v[68:71]\n" \
    "v_accvgpr_write_b32 a94, v106\n" \
    "v_accvgpr_write_b32 a95, v107\n" \
    "s_waitcnt vmcnt(14)\n" \
    "ds_read_b128 v[160:163], v11 offset:12288\n" \
    "ds_read_b128 v[164:167], v12 offset:12288\n" \
    "s_waitcnt lgkmcnt(2)\n" \
    "s_add_u32 m0, s55, 10240\n" \
    "s_add_u32 s57, s54, 55936\n" \
    "buffer_load_dwordx4 v9, s[36:39], s57 offen lds\n" \
    "buffer_load_dwordx4 v10, s[36:39], s57 offen offset:1024 lds\n" \
    "v_mfma_f32_16x16x32_bf16 v[96:99], v[48:51], a[104:107], v[60:63]\n" \
    "v_cvt_pk_bf16_f32 v104, v80, v81\n" \
    "v_cvt_pk_bf16_f32 v105, v82, v83\n" \
    "v_mfma_f32_16x16x32_bf16 v[100:103], v[52:55], a[104:107], v[60:63]\n" \
    "v_cvt_pk_bf16_f32 v106, v84, v85\n" \
    "v_cvt_pk_bf16_f32 v107, v86, v87\n" \
    "v_mfma_f32_16x16x32_bf16 v[64:67], v[56:59], v[152:155], v[64:67]\n" \
    "v_accvgpr_write_b32 a96, v104\n" \
    "v_accvgpr_write_b32 a97, v105\n" \
    "v_mfma_f32_16x16x32_bf16 v[68:71], v[152:155], v[152:155], v[68:71]\n" \
    "v_accvgpr_write_b32 a98, v106\n" \
    "v_accvgpr_write_b32 a99, v107\n" \
    "v_mfma_f32_16x16x32_bf16 v[80:83], v[48:51], a[108:111], v[60:63]\n" \
    "v_cvt_pk_bf16_f32 v104, v88, v89\n" \
    "v_cvt_pk_bf16_f32 v105, v90, v91\n" \
    "v_mfma_f32_16x16x32_bf16 v[84:87], v[52:55], a[108:111], v[60:63]\n" \
    "v_cvt_pk_bf16_f32 v106, v92, v93\n" \
    "v_cvt_pk_bf16_f32 v107, v94, v95\n" \
    "v_mfma_f32_16x16x32_bf16 v[64:67], v[56:59], v[156:159], v[64:67]\n" \
    "v_accvgpr_write_b32 a100, v104\n" \
    "v_accvgpr_write_b32 a101, v105\n" \
    "v_mfma_f32_16x16x32_bf16 v[68:71], v[156:159], v[156:159], v[68:71]\n" \
    "v_accvgpr_write_b32 a102, v106\n" \
    "v_accvgpr_write_b32 a103, v107\n" \
    "s_waitcnt vmcnt(14)\n" \
    "ds_read_b128 v[168:171], v11 offset:14336\n" \
    "ds_read_b128 v[172:175], v12 offset:14336\n" \
    "s_waitcnt lgkmcnt(2)\n" \
    "s_add_u32 m0, s55, 12288\n" \
    "s_add_u32 s57, s54, 56064\n" \
    "buffer_load_dwordx4 v9, s[36:39], s57 offen lds\n" \
    "buffer_load_dwordx4 v10, s[36:39], s57 offen offset:1024 lds\n" \
    "v_mfma_f32_16x16x32_bf16 v[88:91], v[48:51], a[112:115], v[60:63]\n" \
    "v_cvt_pk_bf16_f32 v104, v96, v97\n" \
    "v_cvt_pk_bf16_f32 v105, v98, v99\n" \
    "v_mfma_f32_16x16x32_bf16 v[92:95], v[52:55], a[112:115], v[60:63]\n" \
    "v_cvt_pk_bf16_f32 v106, v100, v101\n" \
    "v_cvt_pk_bf16_f32 v107, v102, v103\n" \
    "v_mfma_f32_16x16x32_bf16 v[64:67], v[56:59], v[160:163], v[64:67]\n" \
    "v_accvgpr_write_b32 a104, v104\n" \
    "v_accvgpr_write_b32 a105, v105\n" \
    "v_mfma_f32_16x16x32_bf16 v[68:71], v[160:163], v[160:163], v[68:71]\n" \
    "v_accvgpr_write_b32 a106, v106\n" \
    "v_accvgpr_write_b32 a107, v107\n" \
    "v_mfma_f32_16x16x32_bf16 v[96:99], v[48:51], a[116:119], v[60:63]\n" \
    "v_cvt_pk_bf16_f32 v104, v80, v81\n" \
    "v_cvt_pk_bf16_f32 v105, v82, v83\n" \
    "v_mfma_f32_16x16x32_bf16 v[100:103], v[52:55], a[116:119], v[60:63]\n" \
    "v_cvt_pk_bf16_f32 v106, v84, v85\n" \
    "v_cvt_pk_bf16_f32 v107, v86, v87\n" \
    "v_mfma_f32_16x16x32_bf16 v[64:67], v[56:59], v[164:167], v[64:67]\n" \
    "v_accvgpr_write_b32 a108, v104\n" \
    "v_accvgpr_write_b32 a109, v105\n" \
    "v_mfma_f32_16x16x32_bf16 v[68:71], v[164:167], v[164:167], v[68:71]\n" \
    "v_accvgpr_write_b32 a110, v106\n" \
    "v_accvgpr_write_b32 a111, v107\n" \
    "s_waitcnt vmcnt(14)\n" \
    "ds_read_b128 v[176:179], v11 offset:16384\n" \
    "ds_read_b128 v[180:183], v12 offset:16384\n" \
    "s_waitcnt lgkmcnt(2)\n" \
    "s_add_u32 m0, s55, 14336\n" \
    "s_add_u32 s57, s54, 56192\n" \
    "buffer_load_dwordx4 v9, s[36:39], s57 offen lds\n" \
    "buffer_load_dwordx4 v10, s[36:39], s57 offen offset:1024 lds\n" \
    "v_mfma_f32_16x16x32_bf16 v[80:83], v[48:51], a[120:123], v[60:63]\n" \
    "v_cvt_pk_bf16_f32 v104, v88, v89\n" \
    "v_cvt_pk_bf16_f32 v105, v90, v91\n" \
    "v_mfma_f32_16x16x32_bf16 v[84:87], v[52:55], a[120:123], v[60:63]\n" \
    "v_cvt_pk_bf16_f32 v106, v92, v93\n" \
    "v_cvt_pk_bf16_f32 v107, v94, v95\n" \
    "v_mfma_f32_16x16x32_bf16 v[64:67], v[56:59], v[168:171], v[64:67]\n" \
    "v_accvgpr_write_b32 a112, v104\n" \
    "v_accvgpr_write_b32 a113, v105\n" \
    "v_mfma_f32_16x16x32_bf16 v[68:71], v[168:171], v[168:171], v[68:71]\n" \
    "v_accvgpr_write_b32 a114, v106\n" \
    "v_accvgpr_write_b32 a115, v107\n" \
    "v_mfma_f32_16x16x32_bf16 v[88:91], v[48:51], a[124:127], v[60:63]\n" \
    "v_cvt_pk_bf16_f32 v104, v96, v97\n" \
    "v_cvt_pk_bf16_f32 v105, v98, v99\n" \
    "v_mfma_f32_16x16x32_bf16 v[92:95], v[52:55], a[124:127], v[60:63]\n" \
    "v_cvt_pk_bf16_f32 v106, v100, v101\n" \
    "v_cvt_pk_bf16_f32 v107, v102, v103\n" \
    "v_mfma_f32_16x16x32_bf16 v[64:67], v[56:59], v[172:175], v[64:67]\n" \
    "v_accvgpr_write_b32 a116, v104\n" \
    "v_accvgpr_write_b32 a117, v105\n" \
    "v_mfma_f32_16x16x32_bf16 v[68:71], v[172:175], v[172:175], v[68:71]\n" \
    "v_accvgpr_write_b32 a118, v106\n" \
    "v_accvgpr_write_b32 a119, v107\n" \
    "s_waitcnt vmcnt(14)\n" \
    "ds_read_b128 v[184:187], v11 offset:0\n" \
    "ds_read_b128 v[188:191], v12 offset:0\n" \
    "s_waitcnt lgkmcnt(2)\n" \
    "s_add_u32 m0, s55, 16384\n" \
    "s_add_u32 s57, s54, 56320\n" \
    "buffer_load_dwordx4 v9, s[36:39], s57 offen lds\n" \
    "buffer_load_dwordx4 v10, s[36:39], s57 offen offset:1024 lds\n" \
    "v_mfma_f32_16x16x32_bf16 v[96:99], v[48:51], a[128:131], v[60:63]\n" \
    "v_cvt_pk_bf16_f32 v104, v80, v81\n" \
    "v_cvt_pk_bf16_f32 v105, v82, v83\n" \
    "v_mfma_f32_16x16x32_bf16 v[100:103], v[52:55], a[128:131], v[60:63]\n" \
    "v_cvt_pk_bf16_f32 v106, v84, v85\n" \
    "v_cvt_pk_bf16_f32 v107, v86, v87\n" \
    "v_mfma_f32_16x16x32_bf16 v[64:67], v[56:59], v[176:179], v[64:67]\n" \
    "v_accvgpr_write_b32 a120, v104\n" \
    "v_accvgpr_write_b32 a121, v105\n" \
    "v_mfma_f32_16x16x32_bf16 v[68:71], v[176:179], v[176:179], v[68:71]\n" \
    "v_accvgpr_write_b32 a122, v106\n" \
    "v_accvgpr_write_b32 a123, v107\n" \
    "v_mfma_f32_16x16x32_bf16 v[80:83], v[48:51], a[132:135], v[60:63]\n" \
    "v_cvt_pk_bf16_f32 v104, v88, v89\n" \
    "v_cvt_pk_bf16_f32 v105, v90, v91\n" \
    "v_mfma_f32_16x16x32_bf16 v[84:87], v[52:55], a[132:135], v[60:63]\n" \
    "v_cvt_pk_bf16_f32 v106, v92, v93\n" \
    "v_cvt_pk_bf16_f32 v107, v94, v95\n" \
    "v_mfma_f32_16x16x32_bf16 v[64:67], v[56:59], v[180:183], v[64:67]\n" \
    "v_accvgpr_write_b32 a124, v104\n" \
    "v_accvgpr_write_b32 a125, v105\n" \
    "v_mfma_f32_16x16x32_bf16 v[68:71], v[180:183], v[180:183], v[68:71]\n" \
    "v_accvgpr_write_b32 a126, v106\n" \
    "v_accvgpr_write_b32 a127, v107\n" \
    "s_waitcnt vmcnt(14)\n" \
    "ds_read_b128 v[192:195], v11 offset:2048\n" \
    "ds_read_b128 v[196:199], v12 offset:2048\n" \
    "s_waitcnt lgkmcnt(2)\n" \
    "v_readlane_b32 s44, v21, s50\n" \
    "v_readlane_b32 s45, v22, s50\n" \
    "v_readlane_b32 s46, v23, s50\n" \
    "v_readlane_b32 s57, v24, s50\n" \
    "s_add_u32 m0, s69, s53\n" \
    "s_add_u32 s50, s50, 1\n" \
    "v_mad_u32_u24 v20, v14, s57, v15\n" \
    "s_mul_i32 s48, s57, s56\n" \
    "s_lshl_b32 s49, s57, 4\n" \
    "s_sub_u32 s49, s49, 0x400\n" \
    "s_add_u32 s53, s53, 36864\n" \
    "s_cmp_eq_u32 s53, 147456\n" \
    "s_cselect_b32 s53, 0, s53\n" \
    "buffer_load_dwordx4 v20, s[44:47], s48 offen lds\n" \
    "s_add_u32 s48, s48, s49\n" \
    "v_mfma_f32_16x16x32_bf16 v[88:91], v[48:51], a[136:139], v[60:63]\n" \
    "v_cvt_pk_bf16_f32 v104, v96, v97\n" \
    "v_cvt_pk_bf16_f32 v105, v98, v99\n" \
    "v_mfma_f32_16x16x32_bf16 v[92:95], v[52:55], a[136:139], v[60:63]\n" \
    "v_cvt_pk_bf16_f32 v106, v100, v101\n" \
    "v_cvt_pk_bf16_f32 v107, v102, v103\n" \
    "v_mfma_f32_16x16x32_bf16 v[72:75], v[56:59], v[184:187], 0\n" \
    "v_accvgpr_write_b32 a128, v104\n" \
    "v_accvgpr_write_b32 a129, v105\n" \
    "v_mfma_f32_16x16x32_bf16 v[76:79], v[184:187], v[184:187], 0\n" \
    "v_accvgpr_write_b32 a130, v106\n" \
    "v_accvgpr_write_b32 a131, v107\n" \
    "v_mfma_f32_16x16x32_bf16 v[96:99], v[48:51], a[140:143], v[60:63]\n" \
    "v_cvt_pk_bf16_f32 v104, v80, v81\n" \
    "v_cvt_pk_bf16_f32 v105, v82, v83\n" \
    "v_mfma_f32_16x16x32_bf16 v[100:103], v[52:55], a[140:143], v[60:63]\n" \
    "v_cvt_pk_bf16_f32 v106, v84, v85\n" \
    "v_cvt_pk_bf16_f32 v107, v86, v87\n" \
    "v_mfma_f32_16x16x32_bf16 v[72:75], v[56:59], v[188:191], v[72:75]\n" \
    "v_accvgpr_write_b32 a132, v104\n" \
    "v_accvgpr_write_b32 a133, v105\n" \
    "v_mfma_f32_16x16x32_bf16 v[76:79], v[188:191], v[188:191], v[76:79]\n" \
    "v_accvgpr_write_b32 a134, v106\n" \
    "v_accvgpr_write_b32 a135, v107\n" \
    "v_cvt_pk_bf16_f32 v104, v88, v89\n" \
    "v_cvt_pk_bf16_f32 v105, v90, v91\n" \
    "v_cvt_pk_bf16_f32 v106, v92, v93\n" \
    "v_cvt_pk_bf16_f32 v107, v94, v95\n" \
    "v_accvgpr_write_b32 a136, v104\n" \
    "v_accvgpr_write_b32 a137, v105\n" \
    "v_accvgpr_write_b32 a138, v106\n" \
    "v_accvgpr_write_b32 a139, v107\n" \
    "v_cvt_pk_bf16_f32 v104, v96, v97\n" \
    "v_cvt_pk_bf16_f32 v105, v98, v99\n" \
    "v_cvt_pk_bf16_f32 v106, v100, v101\n" \
    "v_cvt_pk_bf16_f32 v107, v102, v103\n" \
    "v_accvgpr_write_b32 a140, v104\n" \
    "v_accvgpr_write_b32 a141, v105\n" \
    "v_accvgpr_write_b32 a142, v106\n" \
    "v_accvgpr_write_b32 a143, v107\n" \
    "v_cndmask_b32 v41, v68, v69, s[60:61]\n" \
    "v_cndmask_b32 v41, v41, v70, s[62:63]\n" \
    "v_cndmask_b32 v41, v41, v71, s[64:65]\n" \
    "v_mul_f32 v42, 0x3ae38e39, v64\n" \
    "ds_bpermute_b32 v41, v13, v41\n" \
    "v_cvt_pk_bf16_f32 v43, v42, v42\n" \
    "v_lshlrev_b32 v43, 16, v43\n" \
    "v_sub_f32 v60, 0, v43\n" \
    "v_sub_f32 v61, 0, v43\n" \
    "v_sub_f32 v62, 0, v43\n" \
    "v_sub_f32 v63, 0, v43\n" \
    "s_waitcnt vmcnt(13)\n" \
    "ds_read_b128 v[200:203], v11 offset:4096\n" \
    "ds_read_b128 v[204:207], v12 offset:4096\n" \
    "s_waitcnt lgkmcnt(3)\n" \
    "buffer_load_dwordx4 v20, s[44:47], s48 offen offset:1024 lds\n" \
    "s_add_u32 s48, s48, s49\n" \
    "buffer_load_dwordx4 v20, s[44:47], s48 offen offset:2048 lds\n" \
    "s_add_u32 s48, s48, s49\n" \
    "v_mfma_f32_16x16x32_bf16 v[80:83], v[48:51], v[112:115], v[60:63]\n" \
    "v_mfma_f32_16x16x32_bf16 v[84:87], v[52:55], v[112:115], v[60:63]\n" \
    "v_mfma_f32_16x16x32_bf16 v[72:75], v[56:59], v[192:195], v[72:75]\n" \
    "v_mfma_f32_16x16x32_bf16 v[76:79], v[192:195], v[192:195], v[76:79]\n" \
    "v_mfma_f32_16x16x32_bf16 v[88:91], v[48:51], v[116:119], v[60:63]\n" \
    "v_mfma_f32_16x16x32_bf16 v[92:95], v[52:55], v[116:119], v[60:63]\n" \
    "v_mfma_f32_16x16x32_bf16 v[72:75], v[56:59], v[196:199], v[72:75]\n" \
    "v_mfma_f32_16x16x32_bf16 v[76:79], v[196:199], v[196:199], v[76:79]\n" \
    "s_waitcnt vmcnt(13)\n" \
    "ds_read_b128 v[208:211], v11 offset:6144\n" \
    "ds_read_b128 v[212:215], v12 offset:6144\n" \
    "s_waitcnt lgkmcnt(2)\n" \
    "buffer_load_dwordx4 v20, s[44:47], s48 offen offset:3072 lds\n" \
    "s_add_u32 s48, s48, s49\n" \
    "s_add_u32 m0, m0, 0x1000\n" \
    "s_add_u32 s48, s48, 0x1000\n" \
    "buffer_load_dwordx4 v20, s[44:47], s48 offen lds\n" \
    "s_add_u32 s48, s48, s49\n" \
    "v_mfma_f32_16x16x32_bf16 v[96:99], v[48:51], v[120:123], v[60:63]\n" \
    "v_cvt_pk_bf16_f32 v112, v80, v81\n" \
    "v_mfma_f32_16x16x32_bf16 v[100:103], v[52:55], v[120:123], v[60:63]\n" \
    "v_cvt_pk_bf16_f32 v113, v82, v83\n" \
    "v_mfma_f32_16x16x32_bf16 v[72:75], v[56:59], v[200:203], v[72:75]\n" \
    "v_cvt_pk_bf16_f32 v114, v84, v85\n" \
    "v_mfma_f32_16x16x32_bf16 v[76:79], v[200:203], v[200:203], v[76:79]\n" \
    "v_cvt_pk_bf16_f32 v115, v86, v87\n" \
    "v_mfma_f32_16x16x32_bf16 v[80:83], v[48:51], v[124:127], v[60:63]\n" \
    "v_cvt_pk_bf16_f32 v116, v88, v89\n" \
    "v_mfma_f32_16x16x32_bf16 v[84:87], v[52:55], v[124:127], v[60:63]\n" \
    "v_cvt_pk_bf16_f32 v117, v90, v91\n" \
    "v_mfma_f32_16x16x32_bf16 v[72:75], v[56:59], v[204:207], v[72:75]\n" \
    "v_cvt_pk_bf16_f32 v118, v92, v93\n" \
    "v_mfma_f32_16x16x32_bf16 v[76:79], v[204:207], v[204:207], v[76:79]\n" \
    "v_cvt_pk_bf16_f32 v119, v94, v95\n" \
    "v_mul_f32 v41, 0x3ae38e39, v41\n" \
    "v_fma_f32 v41, -v42, v42, v41\n" \
    "v_max_f32 v41, 0, v41\n" \
    "v_cvt_pk_bf16_f32 v41, v41, v41\n" \
    "v_lshlrev_b32 v41, 16, v41\n" \
    "v_add_f32 v41, 0x3727c5ac, v41\n" \
    "v_cvt_pk_bf16_f32 v41, v41, v41\n" \
    "v_lshlrev_b32 v41, 16, v41\n" \
    "v_sqrt_f32 v41, v41\n" \
    "s_nop 0\n" \
    "v_cvt_pk_bf16_f32 v31, v41, v41\n" \
    "v_lshlrev_b32 v31, 16, v31\n" \
    "v_rcp_f32 v39, v31\n" \
    "s_nop 0\n" \
    "v_mul_f32 v35, 0xbfb8aa3b, v39\n" \
    "s_waitcnt vmcnt(13)\n" \
    "ds_read_b128 v[216:219], v11 offset:8192\n" \
    "ds_read_b128 v[220:223], v12 offset:8192\n" \
    "s_waitcnt lgkmcnt(2)\n" \
    "buffer_load_dwordx4 v20, s[44:47], s48 offen offset:1024 lds\n" \
    "s_add_u32 s48, s48, s49\n" \
    "buffer_load_dwordx4 v20, s[44:47], s48 offen offset:2048 lds\n" \
    "s_add_u32 s48, s48, s49\n" \
    "v_mfma_f32_16x16x32_bf16 v[88:91], v[48:51], v[128:131], v[60:63]\n" \
    "v_cvt_pk_bf16_f32 v120, v96, v97\n" \
    "v_mfma_f32_16x16x32_bf16 v[92:95], v[52:55], v[128:131], v[60:63]\n" \
    "v_cvt_pk_bf16_f32 v121, v98, v99\n" \
    "v_mfma_f32_16x16x32_bf16 v[72:75], v[56:59], v[208:211], v[72:75]\n" \
    "v_cvt_pk_bf16_f32 v122, v100, v101\n" \
    "v_mfma_f32_16x16x32_bf16 v[76:79], v[208:211], v[208:211], v[76:79]\n" \
    "v_cvt_pk_bf16_f32 v123, v102, v103\n" \
    "v_mfma_f32_16x16x32_bf16 v[96:99], v[48:51], v[132:135], v[60:63]\n" \
    "v_cvt_pk_bf16_f32 v124, v80, v81\n" \
    "v_mfma_f32_16x16x32_bf16 v[100:103], v[52:55], v[132:135], v[60:63]\n" \
    "v_cvt_pk_bf16_f32 v125, v82, v83\n" \
    "v_mfma_f32_16x16x32_bf16 v[72:75], v[56:59], v[212:215], v[72:75]\n" \
    "v_cvt_pk_bf16_f32 v126, v84, v85\n" \
    "v_mfma_f32_16x16x32_bf16 v[76:79], v[212:215], v[212:215], v[76:79]\n" \
    "v_cvt_pk_bf16_f32 v127, v86, v87\n" \
    "s_waitcnt vmcnt(13)\n" \
    "ds_read_b128 v[224:227], v11 offset:10240\n" \
    "ds_read_b128 v[228:231], v12 offset:10240\n" \
    "s_waitcnt lgkmcnt(2)\n" \
    "buffer_load_dwordx4 v20, s[44:47], s48 offen offset:3072 lds\n" \
    "s_add_u32 s48, s48, s49\n" \
    "s_add_u32 m0, m0, 0x1000\n" \
    "s_add_u32 s48, s48, 0x1000\n" \
    "buffer_load_dwordx4 v20, s[44:47], s48 offen lds\n" \
    "v_mfma_f32_16x16x32_bf16 v[80:83], v[48:51], v[136:139], v[60:63]\n" \
    "v_cvt_pk_bf16_f32 v128, v88, v89\n" \
    "v_mfma_f32_16x16x32_bf16 v[84:87], v[52:55], v[136:139], v[60:63]\n" \
    "v_cvt_pk_bf16_f32 v129, v90, v91\n" \
    "v_mfma_f32_16x16x32_bf16 v[72:75], v[56:59], v[216:219], v[72:75]\n" \
    "v_cvt_pk_bf16_f32 v130, v92, v93\n" \
    "v_mfma_f32_16x16x32_bf16 v[76:79], v[216:219], v[216:219], v[76:79]\n" \
    "v_cvt_pk_bf16_f32 v131, v94, v95\n" \
    "v_mfma_f32_16x16x32_bf16 v[88:91], v[48:51], v[140:143], v[60:63]\n" \
    "v_cvt_pk_bf16_f32 v132, v96, v97\n" \
    "v_mfma_f32_16x16x32_bf16 v[92:95], v[52:55], v[140:143], v[60:63]\n" \
    "v_cvt_pk_bf16_f32 v133, v98, v99\n" \
    "v_mfma_f32_16x16x32_bf16 v[72:75], v[56:59], v[220:223], v[72:75]\n" \
    "v_cvt_pk_bf16_f32 v134, v100, v101\n" \
    "v_mfma_f32_16x16x32_bf16 v[76:79], v[220:223], v[220:223], v[76:79]\n" \
    "v_cvt_pk_bf16_f32 v135, v102, v103\n" \
    "s_waitcnt vmcnt(13)\n" \
    "ds_read_b128 v[232:235], v11 offset:12288\n" \
    "ds_read_b128 v[236:239], v12 offset:12288\n" \
    "s_waitcnt lgkmcnt(2)\n" \
    "v_readlane_b32 s44, v21, s50\n" \
    "v_readlane_b32 s45, v22, s50\n" \
    "v_readlane_b32 s46, v23, s50\n" \
    "v_readlane_b32 s57, v24, s50\n" \
    "s_add_u32 m0, s69, s53\n" \
    "s_add_u32 s50, s50, 1\n" \
    "v_mad_u32_u24 v20, v14, s57, v15\n" \
    "s_mul_i32 s48, s57, s56\n" \
    "s_lshl_b32 s49, s57, 4\n" \
    "s_sub_u32 s49, s49, 0x400\n" \
    "s_add_u32 s53, s53, 36864\n" \
    "s_cmp_eq_u32 s53, 147456\n" \
    "s_cselect_b32 s53, 0, s53\n" \
    "buffer_load_dwordx4 v20, s[44:47], s48 offen lds\n" \
    "s_add_u32 s48, s48, s49\n" \
    "v_mfma_f32_16x16x32_bf16 v[96:99], v[48:51], v[144:147], v[60:63]\n" \
    "v_cvt_pk_bf16_f32 v136, v80, v81\n" \
    "v_mfma_f32_16x16x32_bf16 v[100:103], v[52:55], v[144:147], v[60:63]\n" \
    "v_cvt_pk_bf16_f32 v137, v82, v83\n" \
    "v_mfma_f32_16x16x32_bf16 v[72:75], v[56:59], v[224:227], v[72:75]\n" \
    "v_cvt_pk_bf16_f32 v138, v84, v85\n" \
    "v_mfma_f32_16x16x32_bf16 v[76:79], v[224:227], v[224:227], v[76:79]\n" \
    "v_cvt_pk_bf16_f32 v139, v86, v87\n" \
    "v_mfma_f32_16x16x32_bf16 v[80:83], v[48:51], v[148:151], v[60:63]\n" \
    "v_cvt_pk_bf16_f32 v140, v88, v89\n" \
    "v_mfma_f32_16x16x32_bf16 v[84:87], v[52:55], v[148:151], v[60:63]\n" \
    "v_cvt_pk_bf16_f32 v141, v90, v91\n" \
    "v_mfma_f32_16x16x32_bf16 v[72:75], v[56:59], v[228:231], v[72:75]\n" \
    "v_cvt_pk_bf16_f32 v142, v92, v93\n" \
    "v_mfma_f32_16x16x32_bf16 v[76:79], v[228:231], v[228:231], v[76:79]\n" \
    "v_cvt_pk_bf16_f32 v143, v94, v95\n" \
    "s_waitcnt vmcnt(12)\n" \
    "ds_read_b128 v[240:243], v11 offset:14336\n" \
    "ds_read_b128 v[244:247], v12 offset:14336\n" \
    "s_waitcnt lgkmcnt(2)\n" \
    "buffer_load_dwordx4 v20, s[44:47], s48 offen offset:1024 lds\n" \
    "s_add_u32 s48, s48, s49\n" \
    "buffer_load_dwordx4 v20, s[44:47], s48 offen offset:2048 lds\n" \
    "s_add_u32 s48, s48, s49\n" \
    "v_mfma_f32_16x16x32_bf16 v[88:91], v[48:51], v[152:155], v[60:63]\n" \
    "v_cvt_pk_bf16_f32 v144, v96, v97\n" \
    "v_mfma_f32_16x16x32_bf16 v[92:95], v[52:55], v[152:155], v[60:63]\n" \
    "v_cvt_pk_bf16_f32 v145, v98, v99\n" \
    "v_mfma_f32_16x16x32_bf16 v[72:75], v[56:59], v[232:235], v[72:75]\n" \
    "v_cvt_pk_bf16_f32 v146, v100, v101\n" \
    "v_mfma_f32_16x16x32_bf16 v[76:79], v[232:235], v[232:235], v[76:79]\n" \
    "v_cvt_pk_bf16_f32 v147, v102, v103\n" \
    "v_mfma_f32_16x16x32_bf16 v[96:99], v[48:51], v[156:159], v[60:63]\n" \
    "v_cvt_pk_bf16_f32 v148, v80, v81\n" \
    "v_mfma_f32_16x16x32_bf16 v[100:103], v[52:55], v[156:159], v[60:63]\n" \
    "v_cvt_pk_bf16_f32 v149, v82, v83\n" \
    "v_mfma_f32_16x16x32_bf16 v[72:75], v[56:59], v[236:239], v[72:75]\n" \
    "v_cvt_pk_bf16_f32 v150, v84, v85\n" \
    "v_mfma_f32_16x16x32_bf16 v[76:79], v[236:239], v[236:239], v[76:79]\n" \
    "v_cvt_pk_bf16_f32 v151, v86, v87\n" \
    "s_waitcnt vmcnt(12)\n" \
    "ds_read_b128 v[248:251], v11 offset:16384\n" \
    "ds_read_b128 v[252:255], v12 offset:16384\n" \
    "s_waitcnt lgkmcnt(2)\n" \
    "buffer_load_dwordx4 v20, s[44:47], s48 offen offset:3072 lds\n" \
    "s_add_u32 s48, s48, s49\n" \
    "s_add_u32 m0, m0, 0x1000\n" \
    "s_add_u32 s48, s48, 0x1000\n" \
    "buffer_load_dwordx4 v20, s[44:47], s48 offen lds\n" \
    "s_add_u32 s48, s48, s49\n" \
    "v_mfma_f32_16x16x32_bf16 v[80:83], v[48:51], v[160:163], v[60:63]\n" \
    "v_cvt_pk_bf16_f32 v152, v88, v89\n" \
    "v_mfma_f32_16x16x32_bf16 v[84:87], v[52:55], v[160:163], v[60:63]\n" \
    "v_cvt_pk_bf16_f32 v153, v90, v91\n" \
    "v_mfma_f32_16x16x32_bf16 v[72:75], v[56:59], v[240:243], v[72:75]\n" \
    "v_cvt_pk_bf16_f32 v154, v92, v93\n" \
    "v_mfma_f32_16x16x32_bf16 v[76:79], v[240:243], v[240:243], v[76:79]\n" \
    "v_cvt_pk_bf16_f32 v155, v94, v95\n" \
    "v_mfma_f32_16x16x32_bf16 v[88:91], v[48:51], v[164:167], v[60:63]\n" \
    "v_cvt_pk_bf16_f32 v156, v96, v97\n" \
    "v_mfma_f32_16x16x32_bf16 v[92:95], v[52:55], v[164:167], v[60:63]\n" \
    "v_cvt_pk_bf16_f32 v157, v98, v99\n" \
    "v_mfma_f32_16x16x32_bf16 v[72:75], v[56:59], v[244:247], v[72:75]\n" \
    "v_cvt_pk_bf16_f32 v158, v100, v101\n" \
    "v_mfma_f32_16x16x32_bf16 v[76:79], v[244:247], v[244:247], v[76:79]\n" \
    "v_cvt_pk_bf16_f32 v159, v102, v103\n" \
    "s_waitcnt lgkmcnt(0)\n" \
    "buffer_load_dwordx4 v20, s[44:47], s48 offen offset:1024 lds\n" \
    "s_add_u32 s48, s48, s49\n" \
    "buffer_load_dwordx4 v20, s[44:47], s48 offen offset:2048 lds\n" \
    "s_add_u32 s48, s48, s49\n" \
    "v_mfma_f32_16x16x32_bf16 v[96:99], v[48:51], v[168:171], v[60:63]\n" \
    "v_cvt_pk_bf16_f32 v160, v80, v81\n" \
    "v_mfma_f32_16x16x32_bf16 v[100:103], v[52:55], v[168:171], v[60:63]\n" \
    "v_cvt_pk_bf16_f32 v161, v82, v83\n" \
    "v_mfma_f32_16x16x32_bf16 v[72:75], v[56:59], v[248:251], v[72:75]\n" \
    "v_cvt_pk_bf16_f32 v162, v84, v85\n" \
    "v_mfma_f32_16x16x32_bf16 v[76:79], v[248:251], v[248:251], v[76:79]\n" \
    "v_cvt_pk_bf16_f32 v163, v86, v87\n" \
    "v_mfma_f32_16x16x32_bf16 v[80:83], v[48:51], v[172:175], v[60:63]\n" \
    "v_cvt_pk_bf16_f32 v164, v88, v89\n" \
    "v_mfma_f32_16x16x32_bf16 v[84:87], v[52:55], v[172:175], v[60:63]\n" \
    "v_cvt_pk_bf16_f32 v165, v90, v91\n" \
    "v_mfma_f32_16x16x32_bf16 v[72:75], v[56:59], v[252:255], v[72:75]\n" \
    "v_cvt_pk_bf16_f32 v166, v92, v93\n" \
    "v_mfma_f32_16x16x32_bf16 v[76:79], v[252:255], v[252:255], v[76:79]\n" \
    "v_cvt_pk_bf16_f32 v167, v94, v95\n" \
    "v_mfma_f32_16x16x32_bf16 v[88:91], v[48:51], v[176:179], v[60:63]\n" \
    "v_cvt_pk_bf16_f32 v168, v96, v97\n" \
    "v_cvt_pk_bf16_f32 v169, v98, v99\n" \
    "v_mfma_f32_16x16x32_bf16 v[92:95], v[52:55], v[176:179], v[60:63]\n" \
    "v_cvt_pk_bf16_f32 v170, v100, v101\n" \
    "v_cvt_pk_bf16_f32 v171, v102, v103\n" \
    "v_mfma_f32_16x16x32_bf16 v[96:99], v[48:51], v[180:183], v[60:63]\n" \
    "v_cvt_pk_bf16_f32 v172, v80, v81\n" \
    "v_cvt_pk_bf16_f32 v173, v82, v83\n" \
    "v_mfma_f32_16x16x32_bf16 v[100:103], v[52:55], v[180:183], v[60:63]\n" \
    "v_cvt_pk_bf16_f32 v174, v84, v85\n" \
    "v_cvt_pk_bf16_f32 v175, v86, v87\n" \
    "v_cvt_pk_bf16_f32 v176, v88, v89\n" \
    "v_cvt_pk_bf16_f32 v177, v90, v91\n" \
    "v_cvt_pk_bf16_f32 v178, v92, v93\n" \
    "v_cvt_pk_bf16_f32 v179, v94, v95\n" \
    "v_cvt_pk_bf16_f32 v180, v96, v97\n" \
    "v_cvt_pk_bf16_f32 v181, v98, v99\n" \
    "v_cvt_pk_bf16_f32 v182, v100, v101\n" \
    "v_cvt_pk_bf16_f32 v183, v102, v103\n" \
    "buffer_load_dwordx4 v20, s[44:47], s48 offen offset:3072 lds\n" \
    "s_add_u32 s48, s48, s49\n" \
    "s_add_u32 m0, m0, 0x1000\n" \
    "s_add_u32 s48, s48, 0x1000\n" \
    "buffer_load_dwordx4 v20, s[44:47], s48 offen lds\n" \
    "s_nop 3\n" \
    "v_cndmask_b32 v41, v76, v77, s[60:61]\n" \
    "v_cndmask_b32 v41, v41, v78, s[62:63]\n" \
    "v_cndmask_b32 v41, v41, v79, s[64:65]\n" \
    "v_mul_f32 v42, 0x3ae38e39, v72\n" \
    "ds_bpermute_b32 v41, v13, v41\n" \
    "v_cvt_pk_bf16_f32 v43, v42, v42\n" \
    "v_lshlrev_b32 v43, 16, v43\n" \
    "v_sub_f32 v60, 0, v43\n" \
    "v_sub_f32 v61, 0, v43\n" \
    "v_sub_f32 v62, 0, v43\n" \
    "v_sub_f32 v63, 0, v43\n" \
    "s_barrier\n" \
    "v_readlane_b32 s44, v21, s50\n" \
    "v_readlane_b32 s45, v22, s50\n" \
    "v_readlane_b32 s46, v23, s50\n" \
    "v_readlane_b32 s57, v24, s50\n" \
    "s_add_u32 m0, s69, s53\n" \
    "s_add_u32 s50, s50, 1\n" \
    "v_mad_u32_u24 v20, v14, s57, v15\n" \
    "v_mfma_f32_16x16x32_bf16 v[80:83], v[48:51], v[184:187], v[60:63]\n" \
    "v_mfma_f32_16x16x32_bf16 v[84:87], v[52:55], v[184:187], v[60:63]\n" \
    "s_mul_i32 s48, s57, s56\n" \
    "s_lshl_b32 s49, s57, 4\n" \
    "s_sub_u32 s49, s49, 0x400\n" \
    "s_add_u32 s53, s53, 36864\n" \
    "s_cmp_eq_u32 s53, 147456\n" \
    "s_cselect_b32 s53, 0, s53\n" \
    "buffer_load_dwordx4 v20, s[44:47], s48 offen lds\n" \
    "s_add_u32 s48, s48, s49\n" \
    "v_mfma_f32_16x16x32_bf16 v[88:91], v[48:51], v[188:191], v[60:63]\n" \
    "v_mfma_f32_16x16x32_bf16 v[92:95], v[52:55], v[188:191], v[60:63]\n" \
    "v_mfma_f32_16x16x32_bf16 v[96:99], v[48:51], v[192:195], v[60:63]\n" \
    "v_cvt_pk_bf16_f32 v184, v80, v81\n" \
    "v_cvt_pk_bf16_f32 v185, v82, v83\n" \
    "v_mfma_f32_16x16x32_bf16 v[100:103], v[52:55], v[192:195], v[60:63]\n" \
    "v_cvt_pk_bf16_f32 v186, v84, v85\n" \
    "v_cvt_pk_bf16_f32 v187, v86, v87\n" \
    "buffer_load_dwordx4 v20, s[44:47], s48 offen offset:1024 lds\n" \
    "s_add_u32 s48, s48, s49\n" \
    "v_mfma_f32_16x16x32_bf16 v[80:83], v[48:51], v[196:199], v[60:63]\n" \
    "v_cvt_pk_bf16_f32 v188, v88, v89\n" \
    "v_cvt_pk_bf16_f32 v189, v90, v91\n" \
    "v_mfma_f32_16x16x32_bf16 v[84:87], v[52:55], v[196:199], v[60:63]\n" \
    "v_cvt_pk_bf16_f32 v190, v92, v93\n" \
    "v_cvt_pk_bf16_f32 v191, v94, v95\n" \
    "s_waitcnt lgkmcnt(0)\n" \
    "v_mul_f32 v41, 0x3ae38e39, v41\n" \
    "v_fma_f32 v41, -v42, v42, v41\n" \
    "v_max_f32 v41, 0, v41\n" \
    "v_cvt_pk_bf16_f32 v41, v41, v41\n" \
    "v_lshlrev_b32 v41, 16, v41\n" \
    "v_add_f32 v41, 0x3727c5ac, v41\n" \
    "v_cvt_pk_bf16_f32 v41, v41, v41\n" \
    "v_lshlrev_b32 v41, 16, v41\n" \
    "v_sqrt_f32 v41, v41\n" \
    "s_nop 0\n" \
    "v_cvt_pk_bf16_f32 v32, v41, v41\n" \
    "v_lshlrev_b32 v32, 16, v32\n" \
    "v_rcp_f32 v40, v32\n" \
    "s_nop 0\n" \
    "v_mul_f32 v36, 0xbfb8aa3b, v40\n" \
    "v_mfma_f32_16x16x32_bf16 v[88:91], v[48:51], v[200:203], v[60:63]\n" \
    "v_cvt_pk_bf16_f32 v192, v96, v97\n" \
    "v_cvt_pk_bf16_f32 v193, v98, v99\n" \
    "v_mfma_f32_16x16x32_bf16 v[92:95], v[52:55], v[200:203], v[60:63]\n" \
    "v_cvt_pk_bf16_f32 v194, v100, v101\n" \
    "v_cvt_pk_bf16_f32 v195, v102, v103\n" \
    "buffer_load_dwordx4 v20, s[44:47], s48 offen offset:2048 lds\n" \
    "s_add_u32 s48, s48, s49\n" \
    "v_mfma_f32_16x16x32_bf16 v[96:99], v[48:51], v[204:207], v[60:63]\n" \
    "v_cvt_pk_bf16_f32 v196, v80, v81\n" \
    "v_cvt_pk_bf16_f32 v197, v82, v83\n" \
    "v_mfma_f32_16x16x32_bf16 v[100:103], v[52:55], v[204:207], v[60:63]\n" \
    "v_cvt_pk_bf16_f32 v198, v84, v85\n" \
    "v_cvt_pk_bf16_f32 v199, v86, v87\n" \
    "v_mfma_f32_16x16x32_bf16 v[80:83], v[48:51], v[208:211], v[60:63]\n" \
    "v_cvt_pk_bf16_f32 v200, v88, v89\n" \
    "v_cvt_pk_bf16_f32 v201, v90, v91\n" \
    "v_mfma_f32_16x16x32_bf16 v[84:87], v[52:55], v[208:211], v[60:63]\n" \
    "v_cvt_pk_bf16_f32 v202, v92, v93\n" \
    "v_cvt_pk_bf16_f32 v203, v94, v95\n" \
    "buffer_load_dwordx4 v20, s[44:47], s48 offen offset:3072 lds\n" \
    "s_add_u32 s48, s48, s49\n" \
    "v_mfma_f32_16x16x32_bf16 v[88:91], v[48:51], v[212:215], v[60:63]\n" \
    "v_cvt_pk_bf16_f32 v204, v96, v97\n" \
    "v_cvt_pk_bf16_f32 v205, v98, v99\n" \
    "v_mfma_f32_16x16x32_bf16 v[92:95], v[52:55], v[212:215], v[60:63]\n" \
    "v_cvt_pk_bf16_f32 v206, v100, v101\n" \
    "v_cvt_pk_bf16_f32 v207, v102, v103\n" \
    "v_mfma_f32_16x16x32_bf16 v[96:99], v[48:51], v[216:219], v[60:63]\n" \
    "v_cvt_pk_bf16_f32 v208, v80, v81\n" \
    "v_cvt_pk_bf16_f32 v209, v82, v83\n" \
    "v_mfma_f32_16x16x32_bf16 v[100:103], v[52:55], v[216:219], v[60:63]\n" \
    "v_cvt_pk_bf16_f32 v210, v84, v85\n" \
    "v_cvt_pk_bf16_f32 v211, v86, v87\n" \
    "s_add_u32 m0, m0, 0x1000\n" \
    "s_add_u32 s48, s48, 0x1000\n" \
    "buffer_load_dwordx4 v20, s[44:47], s48 offen lds\n" \
    "s_add_u32 s48, s48, s49\n" \
    "v_mfma_f32_16x16x32_bf16 v[80:83], v[48:51], v[220:223], v[60:63]\n" \
    "v_cvt_pk_bf16_f32 v212, v88, v89\n" \
    "v_cvt_pk_bf16_f32 v213, v90, v91\n" \
    "v_mfma_f32_16x16x32_bf16 v[84:87], v[52:55], v[220:223], v[60:63]\n" \
    "v_cvt_pk_bf16_f32 v214, v92, v93\n" \
    "v_cvt_pk_bf16_f32 v215, v94, v95\n" \
    "v_mfma_f32_16x16x32_bf16 v[88:91], v[48:51], v[224:227], v[60:63]\n" \
    "v_cvt_pk_bf16_f32 v216, v96, v97\n" \
    "v_cvt_pk_bf16_f32 v217, v98, v99\n" \
    "v_mfma_f32_16x16x32_bf16 v[92:95], v[52:55], v[224:227], v[60:63]\n" \
    "v_cvt_pk_bf16_f32 v218, v100, v101\n" \
    "v_cvt_pk_bf16_f32 v219, v102, v103\n" \
    "buffer_load_dwordx4 v20, s[44:47], s48 offen offset:1024 lds\n" \
    "s_add_u32 s48, s48, s49\n" \
    "v_mfma_f32_16x16x32_bf16 v[96:99], v[48:51], v[228:231], v[60:63]\n" \
    "v_cvt_pk_bf16_f32 v220, v80, v81\n" \
    "v_cvt_pk_bf16_f32 v221, v82, v83\n" \
    "v_mfma_f32_16x16x32_bf16 v[100:103], v[52:55], v[228:231], v[60:63]\n" \
    "v_cvt_pk_bf16_f32 v222, v84, v85\n" \
    "v_cvt_pk_bf16_f32 v223, v86, v87\n" \
    "v_mfma_f32_16x16x32_bf16 v[80:83], v[48:51], v[232:235], v[60:63]\n" \
    "v_cvt_pk_bf16_f32 v224, v88, v89\n" \
    "v_cvt_pk_bf16_f32 v225, v90, v91\n" \
    "v_mfma_f32_16x16x32_bf16 v[84:87], v[52:55], v[232:235], v[60:63]\n" \
    "v_cvt_pk_bf16_f32 v226, v92, v93\n" \
    "v_cvt_pk_bf16_f32 v227, v94, v95\n" \
    "buffer_load_dwordx4 v20, s[44:47], s48 offen offset:2048 lds\n" \
    "s_add_u32 s48, s48, s49\n" \
    "v_mfma_f32_16x16x32_bf16 v[88:91], v[48:51], v[236:239], v[60:63]\n" \
    "v_cvt_pk_bf16_f32 v228, v96, v97\n" \
    "v_cvt_pk_bf16_f32 v229, v98, v99\n" \
    "v_mfma_f32_16x16x32_bf16 v[92:95], v[52:55], v[236:239], v[60:63]\n" \
    "v_cvt_pk_bf16_f32 v230, v100, v101\n" \
    "v_cvt_pk_bf16_f32 v231, v102, v103\n" \
    "v_mfma_f32_16x16x32_bf16 v[96:99], v[48:51], v[240:243], v[60:63]\n" \
    "v_cvt_pk_bf16_f32 v232, v80, v81\n" \
    "v_cvt_pk_bf16_f32 v233, v82, v83\n" \
    "v_mfma_f32_16x16x32_bf16 v[100:103], v[52:55], v[240:243], v[60:63]\n" \
    "v_cvt_pk_bf16_f32 v234, v84, v85\n" \
    "v_cvt_pk_bf16_f32 v235, v86, v87\n" \
    "buffer_load_dwordx4 v20, s[44:47], s48 offen offset:3072 lds\n" \
    "s_add_u32 s48, s48, s49\n" \
    "v_mfma_f32_16x16x32_bf16 v[80:83], v[48:51], v[244:247], v[60:63]\n" \
    "v_cvt_pk_bf16_f32 v236, v88, v89\n" \
    "v_cvt_pk_bf16_f32 v237, v90, v91\n" \
    "v_mfma_f32_16x16x32_bf16 v[84:87], v[52:55], v[244:247], v[60:63]\n" \
    "v_cvt_pk_bf16_f32 v238, v92, v93\n" \
    "v_cvt_pk_bf16_f32 v239, v94, v95\n" \
    "v_mfma_f32_16x16x32_bf16 v[88:91], v[48:51], v[248:251], v[60:63]\n" \
    "v_cvt_pk_bf16_f32 v240, v96, v97\n" \
    "v_cvt_pk_bf16_f32 v241, v98, v99\n" \
    "v_mfma_f32_16x16x32_bf16 v[92:95], v[52:55], v[248:251], v[60:63]\n" \
    "v_cvt_pk_bf16_f32 v242, v100, v101\n" \
    "v_cvt_pk_bf16_f32 v243, v102, v103\n" \
    "s_add_u32 m0, m0, 0x1000\n" \
    "s_add_u32 s48, s48, 0x1000\n" \
    "buffer_load_dwordx4 v20, s[44:47], s48 offen lds\n" \
    "v_mfma_f32_16x16x32_bf16 v[96:99], v[48:51], v[252:255], v[60:63]\n" \
    "v_cvt_pk_bf16_f32 v244, v80, v81\n" \
    "v_cvt_pk_bf16_f32 v245, v82, v83\n" \
    "v_mfma_f32_16x16x32_bf16 v[100:103], v[52:55], v[252:255], v[60:63]\n" \
    "v_cvt_pk_bf16_f32 v246, v84, v85\n" \
    "v_cvt_pk_bf16_f32 v247, v86, v87\n" \
    "v_cvt_pk_bf16_f32 v248, v88, v89\n" \
    "v_cvt_pk_bf16_f32 v249, v90, v91\n" \
    "v_cvt_pk_bf16_f32 v250, v92, v93\n" \
    "v_cvt_pk_bf16_f32 v251, v94, v95\n" \
    "v_cvt_pk_bf16_f32 v252, v96, v97\n" \
    "v_cvt_pk_bf16_f32 v253, v98, v99\n" \
    "v_cvt_pk_bf16_f32 v254, v100, v101\n" \
    "v_cvt_pk_bf16_f32 v255, v102, v103\n" \
    "v_mov_b32 v99, 0xf0000000\n" \
    "s_mov_b64 s[62:63], 0\n" \
    "s_mov_b32 s83, 0xf0000000\n" \
    "s_mov_b32 s74, 0\n" \
    "s_mov_b32 s75, 0\n" \
    "s_mov_b32 s76, 0\n" \
    "s_mov_b32 s77, 0\n" \
    "s_waitcnt vmcnt(18)\n" \
    "s_barrier\n" \
    "ds_read_b64_tr_b16 a[144:145], v16 offset:0\n" \
    "ds_read_b64_tr_b16 a[146:147], v16 offset:1024\n" \
    "ds_read_b64_tr_b16 a[148:149], v16 offset:2048\n" \
    "ds_read_b64_tr_b16 a[150:151], v16 offset:3072\n" \
    "ds_read_b64_tr_b16 a[152:153], v16 offset:4096\n" \
    "ds_read_b64_tr_b16 a[154:155], v16 offset:5120\n" \
    "ds_read_b64_tr_b16 a[156:157], v16 offset:6144\n" \
    "ds_read_b64_tr_b16 a[158:159], v16 offset:7168\n" \
    "ds_read_b64_tr_b16 a[160:161], v16 offset:8192\n" \
    "ds_read_b64_tr_b16 a[162:163], v16 offset:9216\n" \
    "ds_read_b64_tr_b16 a[164:165], v16 offset:10240\n" \
    "ds_read_b64_tr_b16 a[166:167], v16 offset:11264\n" \
    "s_cmp_lt_u32 s73, 18\n" \
    "s_cbranch_scc0 K1_QK%=\n" \
    "K1_V%=:\n" \
    "s_waitcnt lgkmcnt(0)\n" \
    "v_mfma_f32_16x16x32_bf16 v[48:51], a[0:3], a[144:147], 0\n" \
    "ds_read_b64_tr_b16 a[168:169], v16 offset:12288\n" \
    "v_mfma_f32_16x16x32_bf16 v[48:51], a[4:7], a[148:151], v[48:51]\n" \
    "ds_read_b64_tr_b16 a[170:171], v16 offset:13312\n" \
    "v_readlane_b32 s44, v21, s50\n" \
    "v_mfma_f32_16x16x32_bf16 v[48:51], a[8:11], a[152:155], v[48:51]\n" \
    "v_readlane_b32 s70, v26, s51\n" \
    "v_readlane_b32 s45, v22, s50\n" \
    "v_mfma_f32_16x16x32_bf16 v[48:51], a[12:15], a[156:159], v[48:51]\n" \
    "ds_read_b64_tr_b16 a[172:173], v16 offset:14336\n" \
    "v_lshlrev_b32 v41, 4, v28\n" \
    "v_mfma_f32_16x16x32_bf16 v[48:51], a[16:19], a[160:163], v[48:51]\n" \
    "ds_read_b64_tr_b16 a[174:175], v16 offset:15360\n" \
    "v_readlane_b32 s46, v23, s50\n" \
    "v_mfma_f32_16x16x32_bf16 v[48:51], a[20:23], a[164:167], v[48:51]\n" \
    "v_add_u32 v104, 0, v41\n" \
    "v_readlane_b32 s57, v24, s50\n" \
    "v_mfma_f32_16x16x32_bf16 v[52:55], a[72:75], a[144:147], 0\n" \
    "ds_read_b64_tr_b16 a[176:177], v16 offset:16384\n" \
    "v_add_u32 v105, 4, v41\n" \
    "v_mfma_f32_16x16x32_bf16 v[52:55], a[76:79], a[148:151], v[52:55]\n" \
    "ds_read_b64_tr_b16 a[178:179], v16 offset:17408\n" \
    "s_add_u32 m0, s69, s53\n" \
    "v_mfma_f32_16x16x32_bf16 v[52:55], a[80:83], a[152:155], v[52:55]\n" \
    "v_add_u32 v106, 8, v41\n" \
    "s_add_u32 s50, s50, 1\n" \
    "v_mfma_f32_16x16x32_bf16 v[52:55], a[84:87], a[156:159], v[52:55]\n" \
    "ds_read_b64_tr_b16 a[180:181], v16 offset:18432\n" \
    "v_add_u32 v107, 12, v41\n" \
    "v_mfma_f32_16x16x32_bf16 v[52:55], a[88:91], a[160:163], v[52:55]\n" \
    "ds_read_b64_tr_b16 a[182:183], v16 offset:19456\n" \
    "v_mad_u32_u24 v20, v14, s57, v15\n" \
    "v_mfma_f32_16x16x32_bf16 v[52:55], a[92:95], a[164:167], v[52:55]\n" \
    "s_mul_i32 s48, s57, s56\n" \
    "s_lshl_b32 s49, s57, 4\n" \
    "v_mfma_f32_16x16x32_bf16 v[56:59], v[112:115], a[144:147], 0\n" \
    "ds_read_b64_tr_b16 a[184:185], v16 offset:20480\n" \
    "s_sub_u32 s49, s49, 0x400\n" \
    "v_mfma_f32_16x16x32_bf16 v[56:59], v[116:119], a[148:151], v[56:59]\n" \
    "ds_read_b64_tr_b16 a[186:187], v16 offset:21504\n" \
    "v_mfma_f32_16x16x32_bf16 v[56:59], v[120:123], a[152:155], v[56:59]\n" \
    "s_add_u32 s53, s53, 36864\n" \
    "v_mfma_f32_16x16x32_bf16 v[56:59], v[124:127], a[156:159], v[56:59]\n" \
    "ds_read_b64_tr_b16 a[188:189], v16 offset:22528\n" \
    "s_mul_i32 s74, s66, 2560\n" \
    "v_mfma_f32_16x16x32_bf16 v[56:59], v[128:131], a[160:163], v[56:59]\n" \
    "ds_read_b64_tr_b16 a[190:191], v16 offset:23552\n" \
    "s_add_u32 s74, s74, 2622464\n" \
    "v_mfma_f32_16x16x32_bf16 v[56:59], v[132:135], a[164:167], v[56:59]\n" \
    "s_add_u32 s75, s74, 2560\n" \
    "v_lshrrev_b32 v43, 1, v28\n" \
    "v_mfma_f32_16x16x32_bf16 v[60:63], v[184:187], a[144:147], 0\n" \
    "ds_bpermute_b32 v80, v104, v37\n" \
    "v_mfma_f32_16x16x32_bf16 v[60:63], v[188:191], a[148:151], v[60:63]\n" \
    "s_cmp_eq_u32 s53, 147456\n" \
    "s_cselect_b32 s53, 0, s53\n" \
    "v_mfma_f32_16x16x32_bf16 v[60:63], v[192:195], a[152:155], v[60:63]\n" \
    "ds_bpermute_b32 v81, v105, v37\n" \
    "v_mfma_f32_16x16x32_bf16 v[60:63], v[196:199], a[156:159], v[60:63]\n" \
    "v_mul_u32_u24 v43, 640, v43\n" \
    "buffer_load_dwordx4 v20, s[44:47], s48 offen lds\n" \
    "v_mfma_f32_16x16x32_bf16 v[60:63], v[200:203], a[160:163], v[60:63]\n" \
    "ds_bpermute_b32 v82, v106, v37\n" \
    "v_mfma_f32_16x16x32_bf16 v[60:63], v[204:207], a[164:167], v[60:63]\n" \
    "v_and_b32 v42, 1, v28\n" \
    "s_waitcnt lgkmcnt(3)\n" \
    "v_mfma_f32_16x16x32_bf16 v[48:51], a[24:27], a[168:171], v[48:51]\n" \
    "ds_read_b64_tr_b16 a[144:145], v16 offset:24576\n" \
    "s_add_u32 s48, s48, s49\n" \
    "v_mfma_f32_16x16x32_bf16 v[48:51], a[28:31], a[172:175], v[48:51]\n" \
    "ds_read_b64_tr_b16 a[146:147], v16 offset:25600\n" \
    "v_mul_u32_u24 v42, 320, v42\n" \
    "v_mfma_f32_16x16x32_bf16 v[48:51], a[32:35], a[176:179], v[48:51]\n" \
    "ds_bpermute_b32 v83, v107, v37\n" \
    "v_mfma_f32_16x16x32_bf16 v[48:51], a[36:39], a[180:183], v[48:51]\n" \
    "ds_read_b64_tr_b16 a[148:149], v16 offset:26624\n" \
    "buffer_load_dwordx4 v20, s[44:47], s48 offen offset:1024 lds\n" \
    "v_mfma_f32_16x16x32_bf16 v[48:51], a[40:43], a[184:187], v[48:51]\n" \
    "ds_read_b64_tr_b16 a[150:151], v16 offset:27648\n" \
    "s_add_u32 s48, s48, s49\n" \
    "v_mfma_f32_16x16x32_bf16 v[48:51], a[44:47], a[188:191], v[48:51]\n" \
    "ds_bpermute_b32 v84, v104, v38\n" \
    "v_mfma_f32_16x16x32_bf16 v[52:55], a[96:99], a[168:171], v[52:55]\n" \
    "ds_read_b64_tr_b16 a[152:153], v16 offset:28672\n" \
    "v_add_u32 v43, v43, v42\n" \
    "v_mfma_f32_16x16x32_bf16 v[52:55], a[100:103], a[172:175], v[52:55]\n" \
    "ds_read_b64_tr_b16 a[154:155], v16 offset:29696\n" \
    "buffer_load_dwordx4 v20, s[44:47], s48 offen offset:2048 lds\n" \
    "v_mfma_f32_16x16x32_bf16 v[52:55], a[104:107], a[176:179], v[52:55]\n" \
    "ds_bpermute_b32 v85, v105, v38\n" \
    "v_mfma_f32_16x16x32_bf16 v[52:55], a[108:111], a[180:183], v[52:55]\n" \
    "ds_read_b64_tr_b16 a[156:157], v16 offset:30720\n" \
    "s_movk_i32 s72, 3641\n" \
    "v_mfma_f32_16x16x32_bf16 v[52:55], a[112:115], a[184:187], v[52:55]\n" \
    "ds_read_b64_tr_b16 a[158:159], v16 offset:31744\n" \
    "s_add_u32 s48, s48, s49\n" \
    "v_mfma_f32_16x16x32_bf16 v[52:55], a[116:119], a[188:191], v[52:55]\n" \
    "ds_bpermute_b32 v86, v106, v38\n" \
    "v_mfma_f32_16x16x32_bf16 v[56:59], v[136:139], a[168:171], v[56:59]\n" \
    "ds_read_b64_tr_b16 a[160:161], v16 offset:32768\n" \
    "s_mov_b32 s73, 163840\n" \
    "v_mfma_f32_16x16x32_bf16 v[56:59], v[140:143], a[172:175], v[56:59]\n" \
    "ds_read_b64_tr_b16 a[162:163], v16 offset:33792\n" \
    "v_add_u32 v41, s70, v27\n" \
    "v_mfma_f32_16x16x32_bf16 v[56:59], v[144:147], a[176:179], v[56:59]\n" \
    "ds_bpermute_b32 v87, v107, v38\n" \
    "v_mfma_f32_16x16x32_bf16 v[56:59], v[148:151], a[180:183], v[56:59]\n" \
    "ds_read_b64_tr_b16 a[164:165], v16 offset:34816\n" \
    "v_mul_u32_u24 v42, s72, v41\n" \
    "v_mfma_f32_16x16x32_bf16 v[56:59], v[152:155], a[184:187], v[56:59]\n" \
    "ds_read_b64_tr_b16 a[166:167], v16 offset:35840\n" \
    "v_lshrrev_b32 v42, 16, v42\n" \
    "v_mfma_f32_16x16x32_bf16 v[56:59], v[156:159], a[188:191], v[56:59]\n" \
    "buffer_load_dwordx4 v20, s[44:47], s48 offen offset:3072 lds\n" \
    "v_mfma_f32_16x16x32_bf16 v[60:63], v[208:211], a[168:171], v[60:63]\n" \
    "ds_bpermute_b32 v88, v104, v39\n" \
    "v_mfma_f32_16x16x32_bf16 v[60:63], v[212:215], a[172:175], v[60:63]\n" \
    "s_add_u32 s48, s48, s49\n" \
    "s_add_u32 m0, m0, 0x1000\n" \
    "v_mfma_f32_16x16x32_bf16 v[60:63], v[216:219], a[176:179], v[60:63]\n" \
    "ds_bpermute_b32 v89, v105, v39\n" \
    "v_mfma_f32_16x16x32_bf16 v[60:63], v[220:223], a[180:183], v[60:63]\n" \
    "s_add_u32 s48, s48, 0x1000\n" \
    "v_mul_u32_u24 v44, 18, v42\n" \
    "v_mfma_f32_16x16x32_bf16 v[60:63], v[224:227], a[184:187], v[60:63]\n" \
    "ds_bpermute_b32 v90, v106, v39\n" \
    "v_mfma_f32_16x16x32_bf16 v[60:63], v[228:231], a[188:191], v[60:63]\n" \
    "buffer_load_dwordx4 v20, s[44:47], s48 offen lds\n" \
    "s_waitcnt lgkmcnt(3)\n" \
    "v_mfma_f32_16x16x32_bf16 v[48:51], a[48:51], a[144:147], v[48:51]\n" \
    "ds_read_b64_tr_b16 a[168:169], v17 offset:0\n" \
    "s_add_u32 s48, s48, s49\n" \
    "v_mfma_f32_16x16x32_bf16 v[48:51], a[52:55], a[148:151], v[48:51]\n" \
    "ds_read_b64_tr_b16 a[170:171], v17 offset:1024\n" \
    "v_sub_u32 v44, v41, v44\n" \
    "v_mfma_f32_16x16x32_bf16 v[48:51], a[56:59], a[152:155], v[48:51]\n" \
    "ds_bpermute_b32 v91, v107, v39\n" \
    "v_mfma_f32_16x16x32_bf16 v[48:51], a[60:63], a[156:159], v[48:51]\n" \
    "ds_read_b64_tr_b16 a[172:173], v17 offset:2048\n" \
    "buffer_load_dwordx4 v20, s[44:47], s48 offen offset:1024 lds\n" \
    "v_mfma_f32_16x16x32_bf16 v[48:51], a[64:67], a[160:163], v[48:51]\n" \
    "ds_read_b64_tr_b16 a[174:175], v17 offset:3072\n" \
    "v_mad_u32_u24 v42, v42, s73, v43\n" \
    "v_mfma_f32_16x16x32_bf16 v[48:51], a[68:71], a[164:167], v[48:51]\n" \
    "ds_bpermute_b32 v92, v104, v40\n" \
    "v_mfma_f32_16x16x32_bf16 v[52:55], a[120:123], a[144:147], v[52:55]\n" \
    "ds_read_b64_tr_b16 a[176:177], v17 offset:4096\n" \
    "s_add_u32 s48, s48, s49\n" \
    "v_mfma_f32_16x16x32_bf16 v[52:55], a[124:127], a[148:151], v[52:55]\n" \
    "ds_read_b64_tr_b16 a[178:179], v17 offset:5120\n" \
    "v_lshl_add_u32 v44, v44, 4, v42\n" \
    "v_mfma_f32_16x16x32_bf16 v[52:55], a[128:131], a[152:155], v[52:55]\n" \
    "ds_bpermute_b32 v93, v105, v40\n" \
    "v_mfma_f32_16x16x32_bf16 v[52:55], a[132:135], a[156:159], v[52:55]\n" \
    "ds_read_b64_tr_b16 a[180:181], v17 offset:6144\n" \
    "buffer_load_dwordx4 v20, s[44:47], s48 offen offset:2048 lds\n" \
    "v_mfma_f32_16x16x32_bf16 v[52:55], a[136:139], a[160:163], v[52:55]\n" \
    "ds_read_b64_tr_b16 a[182:183], v17 offset:7168\n" \
    "v_add_u32 v41, s70, v27\n" \
    "v_mfma_f32_16x16x32_bf16 v[52:55], a[140:143], a[164:167], v[52:55]\n" \
    "ds_bpermute_b32 v94, v106, v40\n" \
    "v_mfma_f32_16x16x32_bf16 v[56:59], v[160:163], a[144:147], v[56:59]\n" \
    "ds_read_b64_tr_b16 a[184:185], v17 offset:8192\n" \
    "s_add_u32 s48, s48, s49\n" \
    "buffer_load_dwordx4 v20, s[44:47], s48 offen offset:3072 lds\n" \
    "v_mfma_f32_16x16x32_bf16 v[56:59], v[164:167], a[148:151], v[56:59]\n" \
    "ds_read_b64_tr_b16 a[186:187], v17 offset:9216\n" \
    "v_add_u32 v41, 16, v41\n" \
    "v_mfma_f32_16x16x32_bf16 v[56:59], v[168:171], a[152:155], v[56:59]\n" \
    "ds_bpermute_b32 v95, v107, v40\n" \
    "v_mfma_f32_16x16x32_bf16 v[56:59], v[172:175], a[156:159], v[56:59]\n" \
    "ds_read_b64_tr_b16 a[188:189], v17 offset:10240\n" \
    "v_mul_u32_u24 v42, s72, v41\n" \
    "v_mfma_f32_16x16x32_bf16 v[56:59], v[176:179], a[160:163], v[56:59]\n" \
    "ds_read_b64_tr_b16 a[190:191], v17 offset:11264\n" \
    "s_add_u32 s48, s48, s49\n" \
    "v_mfma_f32_16x16x32_bf16 v[56:59], v[180:183], a[164:167], v[56:59]\n" \
    "v_lshrrev_b32 v42, 16, v42\n" \
    "s_add_u32 m0, m0, 0x1000\n" \
    "v_mfma_f32_16x16x32_bf16 v[60:63], v[232:235], a[144:147], v[60:63]\n" \
    "v_mul_u32_u24 v45, 18, v42\n" \
    "s_add_u32 s48, s48, 0x1000\n" \
    "v_mfma_f32_16x16x32_bf16 v[60:63], v[236:239], a[148:151], v[60:63]\n" \
    "buffer_load_dwordx4 v20, s[44:47], s48 offen lds\n" \
    "v_mfma_f32_16x16x32_bf16 v[60:63], v[240:243], a[152:155], v[60:63]\n" \
    "v_sub_u32 v45, v41, v45\n" \
    "v_mad_u32_u24 v42, v42, s73, v43\n" \
    "v_mfma_f32_16x16x32_bf16 v[60:63], v[244:247], a[156:159], v[60:63]\n" \
    "v_lshl_add_u32 v45, v45, 4, v42\n" \
    "v_mfma_f32_16x16x32_bf16 v[60:63], v[248:251], a[160:163], v[60:63]\n" \
    "v_mfma_f32_16x16x32_bf16 v[60:63], v[252:255], a[164:167], v[60:63]\n" \
    "s_waitcnt lgkmcnt(0)\n" \
    "v_mfma_f32_16x16x32_bf16 v[64:67], a[0:3], a[168:171], 0\n" \
    "ds_read_b64_tr_b16 a[144:145], v17 offset:12288\n" \
    "v_mfma_f32_16x16x32_bf16 v[64:67], a[4:7], a[172:175], v[64:67]\n" \
    "ds_read_b64_tr_b16 a[146:147], v17 offset:13312\n" \
    "v_mfma_f32_16x16x32_bf16 v[64:67], a[8:11], a[176:179], v[64:67]\n" \
    "v_mfma_f32_16x16x32_bf16 v[64:67], a[12:15], a[180:183], v[64:67]\n" \
    "ds_read_b64_tr_b16 a[148:149], v17 offset:14336\n" \
    "v_mfma_f32_16x16x32_bf16 v[64:67], a[16:19], a[184:187], v[64:67]\n" \
    "ds_read_b64_tr_b16 a[150:151], v17 offset:15360\n" \
    "v_mfma_f32_16x16x32_bf16 v[64:67], a[20:23], a[188:191], v[64:67]\n" \
    "v_mfma_f32_16x16x32_bf16 v[68:71], a[72:75], a[168:171], 0\n" \
    "ds_read_b64_tr_b16 a[152:153], v17 offset:16384\n" \
    "v_mfma_f32_16x16x32_bf16 v[68:71], a[76:79], a[172:175], v[68:71]\n" \
    "ds_read_b64_tr_b16 a[154:155], v17 offset:17408\n" \
    "v_mfma_f32_16x16x32_bf16 v[68:71], a[80:83], a[176:179], v[68:71]\n" \
    "v_readlane_b32 s88, v21, s87\n" \
    "v_readlane_b32 s89, v22, s87\n" \
    "v_mfma_f32_16x16x32_bf16 v[68:71], a[84:87], a[180:183], v[68:71]\n" \
    "ds_read_b64_tr_b16 a[156:157], v17 offset:18432\n" \
    "s_add_u32 s87, s87, 1\n" \
    "v_mfma_f32_16x16x32_bf16 v[68:71], a[88:91], a[184:187], v[68:71]\n" \
    "ds_read_b64_tr_b16 a[158:159], v17 offset:19456\n" \
    "v_mfma_f32_16x16x32_bf16 v[68:71], a[92:95], a[188:191], v[68:71]\n" \
    "v_lshrrev_b32 v47, 2, v8\n" \
    "v_mfma_f32_16x16x32_bf16 v[72:75], v[112:115], a[168:171], 0\n" \
    "ds_read_b64_tr_b16 a[160:161], v17 offset:20480\n" \
    "v_mfma_f32_16x16x32_bf16 v[72:75], v[116:119], a[172:175], v[72:75]\n" \
    "ds_read_b64_tr_b16 a[162:163], v17 offset:21504\n" \
    "v_mfma_f32_16x16x32_bf16 v[72:75], v[120:123], a[176:179], v[72:75]\n" \
    "v_mul_u32_u24 v47, 9, v47\n" \
    "v_mfma_f32_16x16x32_bf16 v[72:75], v[124:127], a[180:183], v[72:75]\n" \
    "ds_read_b64_tr_b16 a[164:165], v17 offset:22528\n" \
    "v_mfma_f32_16x16x32_bf16 v[72:75], v[128:131], a[184:187], v[72:75]\n" \
    "ds_read_b64_tr_b16 a[166:167], v17 offset:23552\n" \
    "v_mfma_f32_16x16x32_bf16 v[72:75], v[132:135], a[188:191], v[72:75]\n" \
    "global_load_dword a251, v47, s[88:89]\n" \
    "v_mfma_f32_16x16x32_bf16 v[76:79], v[184:187], a[168:171], 0\n" \
    "v_mfma_f32_16x16x32_bf16 v[76:79], v[188:191], a[172:175], v[76:79]\n" \
    "v_mfma_f32_16x16x32_bf16 v[76:79], v[192:195], a[176:179], v[76:79]\n" \
    "v_readlane_b32 s88, v21, s87\n" \
    "v_mfma_f32_16x16x32_bf16 v[76:79], v[196:199], a[180:183], v[76:79]\n" \
    "v_mfma_f32_16x16x32_bf16 v[76:79], v[200:203], a[184:187], v[76:79]\n" \
    "v_mfma_f32_16x16x32_bf16 v[76:79], v[204:207], a[188:191], v[76:79]\n" \
    "v_readlane_b32 s89, v22, s87\n" \
    "s_waitcnt lgkmcnt(0)\n" \
    "v_mfma_f32_16x16x32_bf16 v[64:67], a[24:27], a[144:147], v[64:67]\n" \
    "ds_read_b64_tr_b16 a[168:169], v17 offset:24576\n" \
    "v_mfma_f32_16x16x32_bf16 v[64:67], a[28:31], a[148:151], v[64:67]\n" \
    "ds_read_b64_tr_b16 a[170:171], v17 offset:25600\n" \
    "v_mfma_f32_16x16x32_bf16 v[64:67], a[32:35], a[152:155], v[64:67]\n" \
    "s_add_u32 s87, s87, 1\n" \
    "v_mfma_f32_16x16x32_bf16 v[64:67], a[36:39], a[156:159], v[64:67]\n" \
    "ds_read_b64_tr_b16 a[172:173], v17 offset:26624\n" \
    "v_mfma_f32_16x16x32_bf16 v[64:67], a[40:43], a[160:163], v[64:67]\n" \
    "ds_read_b64_tr_b16 a[174:175], v17 offset:27648\n" \
    "v_mfma_f32_16x16x32_bf16 v[64:67], a[44:47], a[164:167], v[64:67]\n" \
    "v_lshrrev_b32 v47, 2, v8\n" \
    "v_mfma_f32_16x16x32_bf16 v[68:71], a[96:99], a[144:147], v[68:71]\n" \
    "ds_read_b64_tr_b16 a[176:177], v17 offset:28672\n" \
    "v_mfma_f32_16x16x32_bf16 v[68:71], a[100:103], a[148:151], v[68:71]\n" \
    "ds_read_b64_tr_b16 a[178:179], v17 offset:29696\n" \
    "v_mfma_f32_16x16x32_bf16 v[68:71], a[104:107], a[152:155], v[68:71]\n" \
    "v_mul_u32_u24 v47, 9, v47\n" \
    "v_mfma_f32_16x16x32_bf16 v[68:71], a[108:111], a[156:159], v[68:71]\n" \
    "ds_read_b64_tr_b16 a[180:181], v17 offset:30720\n" \
    "v_mfma_f32_16x16x32_bf16 v[68:71], a[112:115], a[160:163], v[68:71]\n" \
    "ds_read_b64_tr_b16 a[182:183], v17 offset:31744\n" \
    "v_mfma_f32_16x16x32_bf16 v[68:71], a[116:119], a[164:167], v[68:71]\n" \
    "global_load_dword a251, v47, s[88:89]\n" \
    "v_mfma_f32_16x16x32_bf16 v[72:75], v[136:139], a[144:147], v[72:75]\n" \
    "ds_read_b64_tr_b16 a[184:185], v17 offset:32768\n" \
    "v_mfma_f32_16x16x32_bf16 v[72:75], v[140:143], a[148:151], v[72:75]\n" \
    "ds_read_b64_tr_b16 a[186:187], v17 offset:33792\n" \
    "v_mfma_f32_16x16x32_bf16 v[72:75], v[144:147], a[152:155], v[72:75]\n" \
    "v_mfma_f32_16x16x32_bf16 v[72:75], v[148:151], a[156:159], v[72:75]\n" \
    "ds_read_b64_tr_b16 a[188:189], v17 offset:34816\n" \
    "v_mul_f32 v48, v48, v80\n" \
    "v_mfma_f32_16x16x32_bf16 v[72:75], v[152:155], a[160:163], v[72:75]\n" \
    "ds_read_b64_tr_b16 a[190:191], v17 offset:35840\n" \
    "v_mul_f32 v49, v49, v81\n" \
    "v_mfma_f32_16x16x32_bf16 v[72:75], v[156:159], a[164:167], v[72:75]\n" \
    "v_mul_f32 v50, v50, v82\n" \
    "v_mul_f32 v51, v51, v83\n" \
    "v_mfma_f32_16x16x32_bf16 v[76:79], v[208:211], a[144:147], v[76:79]\n" \
    "v_mul_f32 v52, v52, v84\n" \
    "v_mul_f32 v53, v53, v85\n" \
    "v_mfma_f32_16x16x32_bf16 v[76:79], v[212:215], a[148:151], v[76:79]\n" \
    "v_mul_f32 v54, v54, v86\n" \
    "v_mul_f32 v55, v55, v87\n" \
    "v_mfma_f32_16x16x32_bf16 v[76:79], v[216:219], a[152:155], v[76:79]\n" \
    "s_add_u32 s52, s52, 36864\n" \
    "v_cvt_pk_bf16_f32 v104, v48, v49\n" \
    "v_mfma_f32_16x16x32_bf16 v[76:79], v[220:223], a[156:159], v[76:79]\n" \
    "s_cmp_eq_u32 s52, 147456\n" \
    "s_cselect_b32 s52, 0, s52\n" \
    "v_mfma_f32_16x16x32_bf16 v[76:79], v[224:227], a[160:163], v[76:79]\n" \
    "s_cmp_lg_u32 s51, 0\n" \
    "s_cbranch_scc1 K1_V%=a\n" \
    "s_waitcnt vmcnt(20)\n" \
    "s_barrier\n" \
    "K1_V%=a:\n" \
    "v_mfma_f32_16x16x32_bf16 v[76:79], v[228:231], a[164:167], v[76:79]\n" \
    "v_cvt_pk_bf16_f32 v105, v50, v51\n" \
    "s_waitcnt lgkmcnt(0)\n" \
    "v_mfma_f32_16x16x32_bf16 v[64:67], a[48:51], a[168:171], v[64:67]\n" \
    "ds_read_b64_tr_b16 a[144:145], v18 offset:0\n" \
    "v_cvt_pk_bf16_f32 v106, v52, v53\n" \
    "v_mfma_f32_16x16x32_bf16 v[64:67], a[52:55], a[172:175], v[64:67]\n" \
    "ds_read_b64_tr_b16 a[146:147], v18 offset:1024\n" \
    "v_cvt_pk_bf16_f32 v107, v54, v55\n" \
    "v_mfma_f32_16x16x32_bf16 v[64:67], a[56:59], a[176:179], v[64:67]\n" \
    "v_mov_b32 v16, v18\n" \
    "v_xor_b32 v17, 32, v18\n" \
    "v_mfma_f32_16x16x32_bf16 v[64:67], a[60:63], a[180:183], v[64:67]\n" \
    "ds_read_b64_tr_b16 a[148:149], v18 offset:2048\n" \
    "v_mfma_f32_16x16x32_bf16 v[64:67], a[64:67], a[184:187], v[64:67]\n" \
    "ds_read_b64_tr_b16 a[150:151], v18 offset:3072\n" \
    "v_mfma_f32_16x16x32_bf16 v[64:67], a[68:71], a[188:191], v[64:67]\n" \
    "buffer_store_dwordx4 v[104:107], v44, s[40:43], s74 offen sc1\n" \
    "v_mfma_f32_16x16x32_bf16 v[68:71], a[120:123], a[168:171], v[68:71]\n" \
    "ds_read_b64_tr_b16 a[152:153], v18 offset:4096\n" \
    "v_mul_f32 v56, v56, v88\n" \
    "v_mfma_f32_16x16x32_bf16 v[68:71], a[124:127], a[172:175], v[68:71]\n" \
    "ds_read_b64_tr_b16 a[154:155], v18 offset:5120\n" \
    "v_mul_f32 v57, v57, v89\n" \
    "v_mfma_f32_16x16x32_bf16 v[68:71], a[128:131], a[176:179], v[68:71]\n" \
    "v_mul_f32 v58, v58, v90\n" \
    "v_mul_f32 v59, v59, v91\n" \
    "v_mfma_f32_16x16x32_bf16 v[68:71], a[132:135], a[180:183], v[68:71]\n" \
    "ds_read_b64_tr_b16 a[156:157], v18 offset:6144\n" \
    "v_mul_f32 v60, v60, v92\n" \
    "v_mfma_f32_16x16x32_bf16 v[68:71], a[136:139], a[184:187], v[68:71]\n" \
    "ds_read_b64_tr_b16 a[158:159], v18 offset:7168\n" \
    "v_mul_f32 v61, v61, v93\n" \
    "v_mfma_f32_16x16x32_bf16 v[68:71], a[140:143], a[188:191], v[68:71]\n" \
    "v_mul_f32 v62, v62, v94\n" \
    "v_mul_f32 v63, v63, v95\n" \
    "v_mfma_f32_16x16x32_bf16 v[72:75], v[160:163], a[168:171], v[72:75]\n" \
    "ds_read_b64_tr_b16 a[160:161], v18 offset:8192\n" \
    "v_cvt_pk_bf16_f32 v108, v56, v57\n" \
    "v_mfma_f32_16x16x32_bf16 v[72:75], v[164:167], a[172:175], v[72:75]\n" \
    "ds_read_b64_tr_b16 a[162:163], v18 offset:9216\n" \
    "v_cvt_pk_bf16_f32 v109, v58, v59\n" \
    "v_mfma_f32_16x16x32_bf16 v[72:75], v[168:171], a[176:179], v[72:75]\n" \
    "v_cvt_pk_bf16_f32 v110, v60, v61\n" \
    "v_cvt_pk_bf16_f32 v111, v62, v63\n" \
    "v_mfma_f32_16x16x32_bf16 v[72:75], v[172:175], a[180:183], v[72:75]\n" \
    "ds_read_b64_tr_b16 a[164:165], v18 offset:10240\n" \
    "v_mfma_f32_16x16x32_bf16 v[72:75], v[176:179], a[184:187], v[72:75]\n" \
    "ds_read_b64_tr_b16 a[166:167], v18 offset:11264\n" \
    "v_mfma_f32_16x16x32_bf16 v[72:75], v[180:183], a[188:191], v[72:75]\n" \
    "v_add_u32 v18, s52, v19\n" \
    "v_mfma_f32_16x16x32_bf16 v[76:79], v[232:235], a[168:171], v[76:79]\n" \
    "buffer_store_dwordx4 v[108:111], v44, s[40:43], s75 offen sc1\n" \
    "v_mfma_f32_16x16x32_bf16 v[76:79], v[236:239], a[172:175], v[76:79]\n" \
    "s_add_u32 s51, s51, 1\n" \
    "v_mfma_f32_16x16x32_bf16 v[76:79], v[240:243], a[176:179], v[76:79]\n" \
    "v_mfma_f32_16x16x32_bf16 v[76:79], v[244:247], a[180:183], v[76:79]\n" \
    "v_mfma_f32_16x16x32_bf16 v[76:79], v[248:251], a[184:187], v[76:79]\n" \
    "v_mfma_f32_16x16x32_bf16 v[76:79], v[252:255], a[188:191], v[76:79]\n" \
    "s_nop 7\n" \
    "v_mul_f32 v64, v64, v80\n" \
    "v_mul_f32 v65, v65, v81\n" \
    "v_mul_f32 v66, v66, v82\n" \
    "v_mul_f32 v67, v67, v83\n" \
    "v_mul_f32 v68, v68, v84\n" \
    "v_mul_f32 v69, v69, v85\n" \
    "v_mul_f32 v70, v70, v86\n" \
    "v_mul_f32 v71, v71, v87\n" \
    "v_cvt_pk_bf16_f32 v104, v64, v65\n" \
    "v_cvt_pk_bf16_f32 v105, v66, v67\n" \
    "v_cvt_pk_bf16_f32 v106, v68, v69\n" \
    "v_cvt_pk_bf16_f32 v107, v70, v71\n" \
    "buffer_store_dwordx4 v[104:107], v45, s[40:43], s74 offen sc1\n" \
    "v_mul_f32 v72, v72, v88\n" \
    "v_mul_f32 v73, v73, v89\n" \
    "v_mul_f32 v74, v74, v90\n" \
    "v_mul_f32 v75, v75, v91\n" \
    "v_mul_f32 v76, v76, v92\n" \
    "v_mul_f32 v77, v77, v93\n" \
    "v_mul_f32 v78, v78, v94\n" \
    "v_mul_f32 v79, v79, v95\n" \
    "v_cvt_pk_bf16_f32 v108, v72, v73\n" \
    "v_cvt_pk_bf16_f32 v109, v74, v75\n" \
    "v_cvt_pk_bf16_f32 v110, v76, v77\n" \
    "v_cvt_pk_bf16_f32 v111, v78, v79\n" \
    "buffer_store_dwordx4 v[108:111], v45, s[40:43], s75 offen sc1\n" \
    "s_waitcnt vmcnt(15)\n" \
    "s_barrier\n" \
    "s_branch K1_GU%=\n" \
    "K1_QK%=:\n" \
    "s_waitcnt lgkmcnt(0)\n" \
    "v_mfma_f32_16x16x32_bf16 v[48:51], a[144:147], a[0:3], 0\n" \
    "ds_read_b64_tr_b16 a[168:169], v16 offset:12288\n" \
    "v_mfma_f32_16x16x32_bf16 v[48:51], a[148:151], a[4:7], v[48:51]\n" \
    "ds_read_b64_tr_b16 a[170:171], v16 offset:13312\n" \
    "v_readlane_b32 s44, v21, s50\n" \
    "v_mfma_f32_16x16x32_bf16 v[48:51], a[152:155], a[8:11], v[48:51]\n" \
    "v_readlane_b32 s45, v22, s50\n" \
    "v_readlane_b32 s46, v23, s50\n" \
    "v_mfma_f32_16x16x32_bf16 v[48:51], a[156:159], a[12:15], v[48:51]\n" \
    "ds_read_b64_tr_b16 a[172:173], v16 offset:14336\n" \
    "v_readlane_b32 s57, v24, s50\n" \
    "v_mfma_f32_16x16x32_bf16 v[48:51], a[160:163], a[16:19], v[48:51]\n" \
    "ds_read_b64_tr_b16 a[174:175], v16 offset:15360\n" \
    "s_add_u32 m0, s69, s53\n" \
    "v_mfma_f32_16x16x32_bf16 v[48:51], a[164:167], a[20:23], v[48:51]\n" \
    "s_add_u32 s50, s50, 1\n" \
    "v_mfma_f32_16x16x32_bf16 v[52:55], a[144:147], a[72:75], 0\n" \
    "ds_read_b64_tr_b16 a[176:177], v16 offset:16384\n" \
    "v_mad_u32_u24 v20, v14, s57, v15\n" \
    "v_mfma_f32_16x16x32_bf16 v[52:55], a[148:151], a[76:79], v[52:55]\n" \
    "ds_read_b64_tr_b16 a[178:179], v16 offset:17408\n" \
    "v_mul_f32 v64, v64, v80\n" \
    "v_mfma_f32_16x16x32_bf16 v[52:55], a[152:155], a[80:83], v[52:55]\n" \
    "v_mul_f32 v65, v65, v80\n" \
    "v_mul_f32 v66, v66, v80\n" \
    "v_mfma_f32_16x16x32_bf16 v[52:55], a[156:159], a[84:87], v[52:55]\n" \
    "ds_read_b64_tr_b16 a[180:181], v16 offset:18432\n" \
    "v_mul_f32 v67, v67, v80\n" \
    "v_mfma_f32_16x16x32_bf16 v[52:55], a[160:163], a[88:91], v[52:55]\n" \
    "ds_read_b64_tr_b16 a[182:183], v16 offset:19456\n" \
    "v_cvt_pk_bf16_f32 v88, v64, v65\n" \
    "v_mfma_f32_16x16x32_bf16 v[52:55], a[164:167], a[92:95], v[52:55]\n" \
    "v_cvt_pk_bf16_f32 v89, v66, v67\n" \
    "s_mul_i32 s48, s57, s56\n" \
    "v_mfma_f32_16x16x32_bf16 v[56:59], a[144:147], v[112:115], 0\n" \
    "ds_read_b64_tr_b16 a[184:185], v16 offset:20480\n" \
    "s_lshl_b32 s49, s57, 4\n" \
    "v_mfma_f32_16x16x32_bf16 v[56:59], a[148:151], v[116:119], v[56:59]\n" \
    "ds_read_b64_tr_b16 a[186:187], v16 offset:21504\n" \
    "s_sub_u32 s49, s49, 0x400\n" \
    "v_mfma_f32_16x16x32_bf16 v[56:59], a[152:155], v[120:123], v[56:59]\n" \
    "buffer_store_dwordx2 v[88:89], v99, s[40:43], s74 offen sc1\n" \
    "v_mfma_f32_16x16x32_bf16 v[56:59], a[156:159], v[124:127], v[56:59]\n" \
    "ds_read_b64_tr_b16 a[188:189], v16 offset:22528\n" \
    "v_mul_f32 v68, v68, v81\n" \
    "v_mfma_f32_16x16x32_bf16 v[56:59], a[160:163], v[128:131], v[56:59]\n" \
    "ds_read_b64_tr_b16 a[190:191], v16 offset:23552\n" \
    "v_mul_f32 v69, v69, v81\n" \
    "v_mfma_f32_16x16x32_bf16 v[56:59], a[164:167], v[132:135], v[56:59]\n" \
    "v_mul_f32 v70, v70, v81\n" \
    "v_mul_f32 v71, v71, v81\n" \
    "v_mfma_f32_16x16x32_bf16 v[60:63], a[144:147], v[184:187], 0\n" \
    "s_add_u32 s53, s53, 36864\n" \
    "v_cvt_pk_bf16_f32 v90, v68, v69\n" \
    "v_mfma_f32_16x16x32_bf16 v[60:63], a[148:151], v[188:191], v[60:63]\n" \
    "v_cvt_pk_bf16_f32 v91, v70, v71\n" \
    "v_mfma_f32_16x16x32_bf16 v[60:63], a[152:155], v[192:195], v[60:63]\n" \
    "s_cmp_eq_u32 s53, 147456\n" \
    "s_cselect_b32 s53, 0, s53\n" \
    "v_mfma_f32_16x16x32_bf16 v[60:63], a[156:159], v[196:199], v[60:63]\n" \
    "buffer_store_dwordx2 v[90:91], v99, s[40:43], s75 offen sc1\n" \
    "v_mfma_f32_16x16x32_bf16 v[60:63], a[160:163], v[200:203], v[60:63]\n" \
    "buffer_load_dwordx4 v20, s[44:47], s48 offen lds\n" \
    "v_mfma_f32_16x16x32_bf16 v[60:63], a[164:167], v[204:207], v[60:63]\n" \
    "v_mul_f32 v72, v72, v82\n" \
    "s_waitcnt lgkmcnt(0)\n" \
    "v_mfma_f32_16x16x32_bf16 v[48:51], a[168:171], a[24:27], v[48:51]\n" \
    "ds_read_b64_tr_b16 a[144:145], v16 offset:24576\n" \
    "v_mul_f32 v73, v73, v82\n" \
    "v_mfma_f32_16x16x32_bf16 v[48:51], a[172:175], a[28:31], v[48:51]\n" \
    "ds_read_b64_tr_b16 a[146:147], v16 offset:25600\n" \
    "s_add_u32 s48, s48, s49\n" \
    "v_mfma_f32_16x16x32_bf16 v[48:51], a[176:179], a[32:35], v[48:51]\n" \
    "v_mul_f32 v74, v74, v82\n" \
    "v_mul_f32 v75, v75, v82\n" \
    "v_mfma_f32_16x16x32_bf16 v[48:51], a[180:183], a[36:39], v[48:51]\n" \
    "ds_read_b64_tr_b16 a[148:149], v16 offset:26624\n" \
    "v_cvt_pk_bf16_f32 v92, v72, v73\n" \
    "v_mfma_f32_16x16x32_bf16 v[48:51], a[184:187], a[40:43], v[48:51]\n" \
    "ds_read_b64_tr_b16 a[150:151], v16 offset:27648\n" \
    "v_cvt_pk_bf16_f32 v93, v74, v75\n" \
    "v_mfma_f32_16x16x32_bf16 v[48:51], a[188:191], a[44:47], v[48:51]\n" \
    "buffer_load_dwordx4 v20, s[44:47], s48 offen offset:1024 lds\n" \
    "v_mfma_f32_16x16x32_bf16 v[52:55], a[168:171], a[96:99], v[52:55]\n" \
    "ds_read_b64_tr_b16 a[152:153], v16 offset:28672\n" \
    "s_add_u32 s48, s48, s49\n" \
    "v_mfma_f32_16x16x32_bf16 v[52:55], a[172:175], a[100:103], v[52:55]\n" \
    "ds_read_b64_tr_b16 a[154:155], v16 offset:29696\n" \
    "v_mfma_f32_16x16x32_bf16 v[52:55], a[176:179], a[104:107], v[52:55]\n" \
    "buffer_load_dwordx4 v20, s[44:47], s48 offen offset:2048 lds\n" \
    "v_mfma_f32_16x16x32_bf16 v[52:55], a[180:183], a[108:111], v[52:55]\n" \
    "ds_read_b64_tr_b16 a[156:157], v16 offset:30720\n" \
    "s_add_u32 s48, s48, s49\n" \
    "v_mfma_f32_16x16x32_bf16 v[52:55], a[184:187], a[112:115], v[52:55]\n" \
    "ds_read_b64_tr_b16 a[158:159], v16 offset:31744\n" \
    "v_mfma_f32_16x16x32_bf16 v[52:55], a[188:191], a[116:119], v[52:55]\n" \
    "buffer_store_dwordx2 v[92:93], v99, s[40:43], s76 offen sc1\n" \
    "v_mfma_f32_16x16x32_bf16 v[56:59], a[168:171], v[136:139], v[56:59]\n" \
    "ds_read_b64_tr_b16 a[160:161], v16 offset:32768\n" \
    "v_mul_f32 v76, v76, v83\n" \
    "v_mfma_f32_16x16x32_bf16 v[56:59], a[172:175], v[140:143], v[56:59]\n" \
    "ds_read_b64_tr_b16 a[162:163], v16 offset:33792\n" \
    "v_mul_f32 v77, v77, v83\n" \
    "v_mfma_f32_16x16x32_bf16 v[56:59], a[176:179], v[144:147], v[56:59]\n" \
    "v_mul_f32 v78, v78, v83\n" \
    "v_mul_f32 v79, v79, v83\n" \
    "v_mfma_f32_16x16x32_bf16 v[56:59], a[180:183], v[148:151], v[56:59]\n" \
    "ds_read_b64_tr_b16 a[164:165], v16 offset:34816\n" \
    "v_cvt_pk_bf16_f32 v94, v76, v77\n" \
    "v_mfma_f32_16x16x32_bf16 v[56:59], a[184:187], v[152:155], v[56:59]\n" \
    "ds_read_b64_tr_b16 a[166:167], v16 offset:35840\n" \
    "v_cvt_pk_bf16_f32 v95, v78, v79\n" \
    "v_mfma_f32_16x16x32_bf16 v[56:59], a[188:191], v[156:159], v[56:59]\n" \
    "buffer_load_dwordx4 v20, s[44:47], s48 offen offset:3072 lds\n" \
    "v_mfma_f32_16x16x32_bf16 v[60:63], a[168:171], v[208:211], v[60:63]\n" \
    "s_add_u32 s48, s48, s49\n" \
    "s_add_u32 m0, m0, 0x1000\n" \
    "v_mfma_f32_16x16x32_bf16 v[60:63], a[172:175], v[212:215], v[60:63]\n" \
    "buffer_store_dwordx2 v[94:95], v99, s[40:43], s77 offen sc1\n" \
    "v_mfma_f32_16x16x32_bf16 v[60:63], a[176:179], v[216:219], v[60:63]\n" \
    "s_add_u32 s48, s48, 0x1000\n" \
    "v_mfma_f32_16x16x32_bf16 v[60:63], a[180:183], v[220:223], v[60:63]\n" \
    "s_cmp_eq_u64 s[62:63], 0\n" \
    "s_cbranch_scc1 K1_QKt1%=\n" \
    "buffer_store_dword v88, v100, s[40:43], s78 offen sc1\n" \
    "buffer_store_dword v89, v101, s[40:43], s78 offen sc1\n" \
    "buffer_store_dword v90, v100, s[40:43], s79 offen sc1\n" \
    "buffer_store_dword v91, v101, s[40:43], s79 offen sc1\n" \
    "buffer_store_dword v92, v100, s[40:43], s80 offen sc1\n" \
    "buffer_store_dword v93, v101, s[40:43], s80 offen sc1\n" \
    "buffer_store_dword v94, v100, s[40:43], s81 offen sc1\n" \
    "buffer_store_dword v95, v101, s[40:43], s81 offen sc1\n" \
    "K1_QKt1%=:\n" \
    "v_mfma_f32_16x16x32_bf16 v[60:63], a[184:187], v[224:227], v[60:63]\n" \
    "buffer_load_dwordx4 v20, s[44:47], s48 offen lds\n" \
    "v_mfma_f32_16x16x32_bf16 v[60:63], a[188:191], v[228:231], v[60:63]\n" \
    "v_readlane_b32 s71, v26, s51\n" \
    "s_waitcnt lgkmcnt(0)\n" \
    "v_mfma_f32_16x16x32_bf16 v[48:51], a[144:147], a[48:51], v[48:51]\n" \
    "ds_read_b64_tr_b16 a[168:169], v17 offset:0\n" \
    "s_add_u32 s48, s48, s49\n" \
    "v_mfma_f32_16x16x32_bf16 v[48:51], a[148:151], a[52:55], v[48:51]\n" \
    "ds_read_b64_tr_b16 a[170:171], v17 offset:1024\n" \
    "s_movk_i32 s70, 1280\n" \
    "v_mfma_f32_16x16x32_bf16 v[48:51], a[152:155], a[56:59], v[48:51]\n" \
    "s_movk_i32 s72, 1024\n" \
    "buffer_load_dwordx4 v20, s[44:47], s48 offen offset:1024 lds\n" \
    "v_mfma_f32_16x16x32_bf16 v[48:51], a[156:159], a[60:63], v[48:51]\n" \
    "ds_read_b64_tr_b16 a[172:173], v17 offset:2048\n" \
    "s_add_u32 s48, s48, s49\n" \
    "v_mfma_f32_16x16x32_bf16 v[48:51], a[160:163], a[64:67], v[48:51]\n" \
    "ds_read_b64_tr_b16 a[174:175], v17 offset:3072\n" \
    "buffer_load_dwordx4 v20, s[44:47], s48 offen offset:2048 lds\n" \
    "v_mfma_f32_16x16x32_bf16 v[48:51], a[164:167], a[68:71], v[48:51]\n" \
    "s_bitcmp1_b32 s71, 15\n" \
    "s_cselect_b32 s70, 2560, s70\n" \
    "s_cselect_b32 s74, 2621440, 0\n" \
    "s_cselect_b32 s72, 2304, s72\n" \
    "s_cselect_b32 s73, 1.0, 0x3eae1a93\n" \
    "s_cselect_b32 s59, 8, 16\n" \
    "s_cselect_b64 vcc, -1, 0\n" \
    "v_mfma_f32_16x16x32_bf16 v[52:55], a[144:147], a[120:123], v[52:55]\n" \
    "ds_read_b64_tr_b16 a[176:177], v17 offset:4096\n" \
    "s_and_b32 s71, s71, 0x7fff\n" \
    "s_mul_i32 s58, s66, s70\n" \
    "v_mfma_f32_16x16x32_bf16 v[52:55], a[148:151], a[124:127], v[52:55]\n" \
    "ds_read_b64_tr_b16 a[178:179], v17 offset:5120\n" \
    "s_add_u32 s74, s74, s58\n" \
    "v_and_b32 v41, 8, v27\n" \
    "v_mfma_f32_16x16x32_bf16 v[52:55], a[152:155], a[128:131], v[52:55]\n" \
    "v_add_u32 v41, v41, v27\n" \
    "v_cndmask_b32 v41, v27, v41, vcc\n" \
    "v_mfma_f32_16x16x32_bf16 v[52:55], a[156:159], a[132:135], v[52:55]\n" \
    "ds_read_b64_tr_b16 a[180:181], v17 offset:6144\n" \
    "v_mul_f32 v80, s73, v37\n" \
    "v_mul_f32 v81, s73, v38\n" \
    "v_mfma_f32_16x16x32_bf16 v[52:55], a[160:163], a[136:139], v[52:55]\n" \
    "ds_read_b64_tr_b16 a[182:183], v17 offset:7168\n" \
    "v_mul_f32 v82, s73, v39\n" \
    "v_mul_f32 v83, s73, v40\n" \
    "v_mfma_f32_16x16x32_bf16 v[52:55], a[164:167], a[140:143], v[52:55]\n" \
    "s_lshl_b32 s73, s70, 6\n" \
    "s_lshl_b32 s58, s59, 4\n" \
    "s_add_u32 s75, s74, s58\n" \
    "v_mfma_f32_16x16x32_bf16 v[56:59], a[144:147], v[160:163], v[56:59]\n" \
    "ds_read_b64_tr_b16 a[184:185], v17 offset:8192\n" \
    "s_add_u32 s48, s48, s49\n" \
    "v_mfma_f32_16x16x32_bf16 v[56:59], a[148:151], v[164:167], v[56:59]\n" \
    "ds_read_b64_tr_b16 a[186:187], v17 offset:9216\n" \
    "s_add_u32 s76, s74, s70\n" \
    "s_add_u32 s77, s75, s70\n" \
    "v_mfma_f32_16x16x32_bf16 v[56:59], a[152:155], v[168:171], v[56:59]\n" \
    "s_add_u32 s78, s74, s72\n" \
    "buffer_load_dwordx4 v20, s[44:47], s48 offen offset:3072 lds\n" \
    "s_lshl_b32 s58, s59, 2\n" \
    "v_mfma_f32_16x16x32_bf16 v[56:59], a[156:159], v[172:175], v[56:59]\n" \
    "ds_read_b64_tr_b16 a[188:189], v17 offset:10240\n" \
    "s_add_u32 s79, s78, s58\n" \
    "s_add_u32 s80, s78, s70\n" \
    "v_mfma_f32_16x16x32_bf16 v[56:59], a[160:163], v[176:179], v[56:59]\n" \
    "ds_read_b64_tr_b16 a[190:191], v17 offset:11264\n" \
    "s_add_u32 s48, s48, s49\n" \
    "s_add_u32 s81, s79, s70\n" \
    "s_movk_i32 s58, 1821\n" \
    "v_mfma_f32_16x16x32_bf16 v[56:59], a[164:167], v[180:183], v[56:59]\n" \
    "v_lshl_add_u32 v42, v28, 2, s71\n" \
    "v_mul_u32_u24 v43, s58, v42\n" \
    "v_mfma_f32_16x16x32_bf16 v[60:63], a[144:147], v[232:235], v[60:63]\n" \
    "s_add_u32 m0, m0, 0x1000\n" \
    "v_lshrrev_b32 v43, 16, v43\n" \
    "v_mul_u32_u24 v44, 36, v43\n" \
    "v_mfma_f32_16x16x32_bf16 v[60:63], a[148:151], v[236:239], v[60:63]\n" \
    "v_sub_u32 v42, v42, v44\n" \
    "v_lshrrev_b32 v42, 2, v42\n" \
    "v_mfma_f32_16x16x32_bf16 v[60:63], a[152:155], v[240:243], v[60:63]\n" \
    "s_add_u32 s48, s48, 0x1000\n" \
    "v_cmp_lt_u32 vcc, 4, v42\n" \
    "v_lshlrev_b32 v43, 1, v43\n" \
    "v_mfma_f32_16x16x32_bf16 v[60:63], a[156:159], v[244:247], v[60:63]\n" \
    "v_subrev_u32 v44, 5, v42\n" \
    "v_cndmask_b32 v44, v42, v44, vcc\n" \
    "v_addc_co_u32 v43, vcc, 0, v43, vcc\n" \
    "v_mfma_f32_16x16x32_bf16 v[60:63], a[160:163], v[248:251], v[60:63]\n" \
    "buffer_load_dwordx4 v20, s[44:47], s48 offen lds\n" \
    "v_mfma_f32_16x16x32_bf16 v[60:63], a[164:167], v[252:255], v[60:63]\n" \
    "v_mul_lo_u32 v43, v43, s73\n" \
    "v_lshrrev_b32 v96, 1, v44\n" \
    "v_lshl_add_u32 v96, v96, 9, v43\n" \
    "v_and_b32 v44, 1, v44\n" \
    "v_lshl_add_u32 v96, v44, 3, v96\n" \
    "v_lshl_add_u32 v96, v41, 4, v96\n" \
    "v_lshl_add_u32 v97, v41, 2, v43\n" \
    "v_cmp_eq_u32 s[60:61], 4, v42\n" \
    "v_mov_b32 v44, 0xf0000000\n" \
    "v_add_u32 v98, s73, v97\n" \
    "v_cndmask_b32 v96, v96, v44, s[60:61]\n" \
    "v_cndmask_b32 v97, v44, v97, s[60:61]\n" \
    "v_cndmask_b32 v98, v44, v98, s[60:61]\n" \
    "s_waitcnt lgkmcnt(0)\n" \
    "v_mfma_f32_16x16x32_bf16 v[64:67], a[168:171], a[0:3], 0\n" \
    "ds_read_b64_tr_b16 a[144:145], v17 offset:12288\n" \
    "v_mfma_f32_16x16x32_bf16 v[64:67], a[172:175], a[4:7], v[64:67]\n" \
    "ds_read_b64_tr_b16 a[146:147], v17 offset:13312\n" \
    "v_mfma_f32_16x16x32_bf16 v[64:67], a[176:179], a[8:11], v[64:67]\n" \
    "v_lshl_add_u32 v42, v28, 2, s71\n" \
    "v_add_u32 v42, 16, v42\n" \
    "v_mfma_f32_16x16x32_bf16 v[64:67], a[180:183], a[12:15], v[64:67]\n" \
    "ds_read_b64_tr_b16 a[148:149], v17 offset:14336\n" \
    "v_mul_u32_u24 v43, s58, v42\n" \
    "v_mfma_f32_16x16x32_bf16 v[64:67], a[184:187], a[16:19], v[64:67]\n" \
    "ds_read_b64_tr_b16 a[150:151], v17 offset:15360\n" \
    "v_lshrrev_b32 v43, 16, v43\n" \
    "v_mfma_f32_16x16x32_bf16 v[64:67], a[188:191], a[20:23], v[64:67]\n" \
    "v_mul_u32_u24 v44, 36, v43\n" \
    "v_sub_u32 v42, v42, v44\n" \
    "v_mfma_f32_16x16x32_bf16 v[68:71], a[168:171], a[72:75], 0\n" \
    "ds_read_b64_tr_b16 a[152:153], v17 offset:16384\n" \
    "v_lshrrev_b32 v42, 2, v42\n" \
    "v_mfma_f32_16x16x32_bf16 v[68:71], a[172:175], a[76:79], v[68:71]\n" \
    "ds_read_b64_tr_b16 a[154:155], v17 offset:17408\n" \
    "v_cmp_lt_u32 vcc, 4, v42\n" \
    "v_mfma_f32_16x16x32_bf16 v[68:71], a[176:179], a[80:83], v[68:71]\n" \
    "v_readlane_b32 s88, v21, s87\n" \
    "v_lshlrev_b32 v43, 1, v43\n" \
    "v_mfma_f32_16x16x32_bf16 v[68:71], a[180:183], a[84:87], v[68:71]\n" \
    "ds_read_b64_tr_b16 a[156:157], v17 offset:18432\n" \
    "v_readlane_b32 s89, v22, s87\n" \
    "v_mfma_f32_16x16x32_bf16 v[68:71], a[184:187], a[88:91], v[68:71]\n" \
    "ds_read_b64_tr_b16 a[158:159], v17 offset:19456\n" \
    "v_subrev_u32 v44, 5, v42\n" \
    "v_mfma_f32_16x16x32_bf16 v[68:71], a[188:191], a[92:95], v[68:71]\n" \
    "v_cndmask_b32 v44, v42, v44, vcc\n" \
    "s_add_u32 s87, s87, 1\n" \
    "v_mfma_f32_16x16x32_bf16 v[72:75], a[168:171], v[112:115], 0\n" \
    "ds_read_b64_tr_b16 a[160:161], v17 offset:20480\n" \
    "v_addc_co_u32 v43, vcc, 0, v43, vcc\n" \
    "v_mfma_f32_16x16x32_bf16 v[72:75], a[172:175], v[116:119], v[72:75]\n" \
    "ds_read_b64_tr_b16 a[162:163], v17 offset:21504\n" \
    "v_mul_lo_u32 v43, v43, s73\n" \
    "v_mfma_f32_16x16x32_bf16 v[72:75], a[176:179], v[120:123], v[72:75]\n" \
    "v_lshrrev_b32 v99, 1, v44\n" \
    "v_lshrrev_b32 v47, 2, v8\n" \
    "v_mfma_f32_16x16x32_bf16 v[72:75], a[180:183], v[124:127], v[72:75]\n" \
    "ds_read_b64_tr_b16 a[164:165], v17 offset:22528\n" \
    "v_lshl_add_u32 v99, v99, 9, v43\n" \
    "v_mfma_f32_16x16x32_bf16 v[72:75], a[184:187], v[128:131], v[72:75]\n" \
    "ds_read_b64_tr_b16 a[166:167], v17 offset:23552\n" \
    "v_and_b32 v44, 1, v44\n" \
    "v_mfma_f32_16x16x32_bf16 v[72:75], a[188:191], v[132:135], v[72:75]\n" \
    "v_lshl_add_u32 v99, v44, 3, v99\n" \
    "v_mul_u32_u24 v47, 9, v47\n" \
    "v_mfma_f32_16x16x32_bf16 v[76:79], a[168:171], v[184:187], 0\n" \
    "v_lshl_add_u32 v99, v41, 4, v99\n" \
    "v_lshl_add_u32 v100, v41, 2, v43\n" \
    "v_mfma_f32_16x16x32_bf16 v[76:79], a[172:175], v[188:191], v[76:79]\n" \
    "global_load_dword a251, v47, s[88:89]\n" \
    "v_mfma_f32_16x16x32_bf16 v[76:79], a[176:179], v[192:195], v[76:79]\n" \
    "v_cmp_eq_u32 s[62:63], 4, v42\n" \
    "v_mov_b32 v44, 0xf0000000\n" \
    "v_mfma_f32_16x16x32_bf16 v[76:79], a[180:183], v[196:199], v[76:79]\n" \
    "v_add_u32 v101, s73, v100\n" \
    "v_readlane_b32 s88, v21, s87\n" \
    "v_mfma_f32_16x16x32_bf16 v[76:79], a[184:187], v[200:203], v[76:79]\n" \
    "v_cndmask_b32 v99, v99, v44, s[62:63]\n" \
    "v_cndmask_b32 v100, v44, v100, s[62:63]\n" \
    "v_mfma_f32_16x16x32_bf16 v[76:79], a[188:191], v[204:207], v[76:79]\n" \
    "v_cndmask_b32 v101, v44, v101, s[62:63]\n" \
    "s_waitcnt lgkmcnt(0)\n" \
    "v_mfma_f32_16x16x32_bf16 v[64:67], a[144:147], a[24:27], v[64:67]\n" \
    "ds_read_b64_tr_b16 a[168:169], v17 offset:24576\n" \
    "v_readlane_b32 s89, v22, s87\n" \
    "v_mfma_f32_16x16x32_bf16 v[64:67], a[148:151], a[28:31], v[64:67]\n" \
    "ds_read_b64_tr_b16 a[170:171], v17 offset:25600\n" \
    "v_mfma_f32_16x16x32_bf16 v[64:67], a[152:155], a[32:35], v[64:67]\n" \
    "s_add_u32 s87, s87, 1\n" \
    "v_mfma_f32_16x16x32_bf16 v[64:67], a[156:159], a[36:39], v[64:67]\n" \
    "ds_read_b64_tr_b16 a[172:173], v17 offset:26624\n" \
    "v_mfma_f32_16x16x32_bf16 v[64:67], a[160:163], a[40:43], v[64:67]\n" \
    "ds_read_b64_tr_b16 a[174:175], v17 offset:27648\n" \
    "v_mfma_f32_16x16x32_bf16 v[64:67], a[164:167], a[44:47], v[64:67]\n" \
    "v_lshrrev_b32 v47, 2, v8\n" \
    "v_mfma_f32_16x16x32_bf16 v[68:71], a[144:147], a[96:99], v[68:71]\n" \
    "ds_read_b64_tr_b16 a[176:177], v17 offset:28672\n" \
    "v_mfma_f32_16x16x32_bf16 v[68:71], a[148:151], a[100:103], v[68:71]\n" \
    "ds_read_b64_tr_b16 a[178:179], v17 offset:29696\n" \
    "v_mfma_f32_16x16x32_bf16 v[68:71], a[152:155], a[104:107], v[68:71]\n" \
    "v_mul_u32_u24 v47, 9, v47\n" \
    "v_mfma_f32_16x16x32_bf16 v[68:71], a[156:159], a[108:111], v[68:71]\n" \
    "ds_read_b64_tr_b16 a[180:181], v17 offset:30720\n" \
    "v_mfma_f32_16x16x32_bf16 v[68:71], a[160:163], a[112:115], v[68:71]\n" \
    "ds_read_b64_tr_b16 a[182:183], v17 offset:31744\n" \
    "v_mul_f32 v48, v48, v80\n" \
    "v_mfma_f32_16x16x32_bf16 v[68:71], a[164:167], a[116:119], v[68:71]\n" \
    "v_mul_f32 v49, v49, v80\n" \
    "v_mul_f32 v50, v50, v80\n" \
    "v_mfma_f32_16x16x32_bf16 v[72:75], a[144:147], v[136:139], v[72:75]\n" \
    "ds_read_b64_tr_b16 a[184:185], v17 offset:32768\n" \
    "v_mul_f32 v51, v51, v80\n" \
    "v_mfma_f32_16x16x32_bf16 v[72:75], a[148:151], v[140:143], v[72:75]\n" \
    "ds_read_b64_tr_b16 a[186:187], v17 offset:33792\n" \
    "v_cvt_pk_bf16_f32 v88, v48, v49\n" \
    "v_mfma_f32_16x16x32_bf16 v[72:75], a[152:155], v[144:147], v[72:75]\n" \
    "v_cvt_pk_bf16_f32 v89, v50, v51\n" \
    "v_mfma_f32_16x16x32_bf16 v[72:75], a[156:159], v[148:151], v[72:75]\n" \
    "ds_read_b64_tr_b16 a[188:189], v17 offset:34816\n" \
    "v_mfma_f32_16x16x32_bf16 v[72:75], a[160:163], v[152:155], v[72:75]\n" \
    "ds_read_b64_tr_b16 a[190:191], v17 offset:35840\n" \
    "v_mfma_f32_16x16x32_bf16 v[72:75], a[164:167], v[156:159], v[72:75]\n" \
    "global_load_dword a251, v47, s[88:89]\n" \
    "v_mfma_f32_16x16x32_bf16 v[76:79], a[144:147], v[208:211], v[76:79]\n" \
    "buffer_store_dwordx2 v[88:89], v96, s[40:43], s74 offen sc1\n" \
    "v_mfma_f32_16x16x32_bf16 v[76:79], a[148:151], v[212:215], v[76:79]\n" \
    "v_mul_f32 v52, v52, v81\n" \
    "v_mul_f32 v53, v53, v81\n" \
    "v_mfma_f32_16x16x32_bf16 v[76:79], a[152:155], v[216:219], v[76:79]\n" \
    "s_add_u32 s52, s52, 36864\n" \
    "v_mul_f32 v54, v54, v81\n" \
    "v_mfma_f32_16x16x32_bf16 v[76:79], a[156:159], v[220:223], v[76:79]\n" \
    "v_mul_f32 v55, v55, v81\n" \
    "v_cvt_pk_bf16_f32 v90, v52, v53\n" \
    "v_mfma_f32_16x16x32_bf16 v[76:79], a[160:163], v[224:227], v[76:79]\n" \
    "s_cmp_lg_u32 s51, 0\n" \
    "s_cbranch_scc1 K1_QK%=a\n" \
    "s_waitcnt vmcnt(25)\n" \
    "s_barrier\n" \
    "K1_QK%=a:\n" \
    "v_mfma_f32_16x16x32_bf16 v[76:79], a[164:167], v[228:231], v[76:79]\n" \
    "v_cvt_pk_bf16_f32 v91, v54, v55\n" \
    "s_waitcnt lgkmcnt(0)\n" \
    "v_mfma_f32_16x16x32_bf16 v[64:67], a[168:171], a[48:51], v[64:67]\n" \
    "ds_read_b64_tr_b16 a[144:145], v18 offset:0\n" \
    "v_mfma_f32_16x16x32_bf16 v[64:67], a[172:175], a[52:55], v[64:67]\n" \
    "ds_read_b64_tr_b16 a[146:147], v18 offset:1024\n" \
    "v_mfma_f32_16x16x32_bf16 v[64:67], a[176:179], a[56:59], v[64:67]\n" \
    "s_cmp_eq_u32 s52, 147456\n" \
    "s_cselect_b32 s52, 0, s52\n" \
    "v_mfma_f32_16x16x32_bf16 v[64:67], a[180:183], a[60:63], v[64:67]\n" \
    "ds_read_b64_tr_b16 a[148:149], v18 offset:2048\n" \
    "v_mov_b32 v16, v18\n" \
    "v_mfma_f32_16x16x32_bf16 v[64:67], a[184:187], a[64:67], v[64:67]\n" \
    "ds_read_b64_tr_b16 a[150:151], v18 offset:3072\n" \
    "v_xor_b32 v17, 32, v18\n" \
    "v_mfma_f32_16x16x32_bf16 v[64:67], a[188:191], a[68:71], v[64:67]\n" \
    "buffer_store_dwordx2 v[90:91], v96, s[40:43], s75 offen sc1\n" \
    "v_mfma_f32_16x16x32_bf16 v[68:71], a[168:171], a[120:123], v[68:71]\n" \
    "ds_read_b64_tr_b16 a[152:153], v18 offset:4096\n" \
    "v_mul_f32 v56, v56, v82\n" \
    "v_mfma_f32_16x16x32_bf16 v[68:71], a[172:175], a[124:127], v[68:71]\n" \
    "ds_read_b64_tr_b16 a[154:155], v18 offset:5120\n" \
    "v_mul_f32 v57, v57, v82\n" \
    "v_mfma_f32_16x16x32_bf16 v[68:71], a[176:179], a[128:131], v[68:71]\n" \
    "v_mul_f32 v58, v58, v82\n" \
    "v_mul_f32 v59, v59, v82\n" \
    "v_mfma_f32_16x16x32_bf16 v[68:71], a[180:183], a[132:135], v[68:71]\n" \
    "ds_read_b64_tr_b16 a[156:157], v18 offset:6144\n" \
    "v_cvt_pk_bf16_f32 v92, v56, v57\n" \
    "v_mfma_f32_16x16x32_bf16 v[68:71], a[184:187], a[136:139], v[68:71]\n" \
    "ds_read_b64_tr_b16 a[158:159], v18 offset:7168\n" \
    "v_cvt_pk_bf16_f32 v93, v58, v59\n" \
    "v_mfma_f32_16x16x32_bf16 v[68:71], a[188:191], a[140:143], v[68:71]\n" \
    "buffer_store_dwordx2 v[92:93], v96, s[40:43], s76 offen sc1\n" \
    "v_mfma_f32_16x16x32_bf16 v[72:75], a[168:171], v[160:163], v[72:75]\n" \
    "ds_read_b64_tr_b16 a[160:161], v18 offset:8192\n" \
    "v_mul_f32 v60, v60, v83\n" \
    "v_mfma_f32_16x16x32_bf16 v[72:75], a[172:175], v[164:167], v[72:75]\n" \
    "ds_read_b64_tr_b16 a[162:163], v18 offset:9216\n" \
    "v_mul_f32 v61, v61, v83\n" \
    "v_mfma_f32_16x16x32_bf16 v[72:75], a[176:179], v[168:171], v[72:75]\n" \
    "v_mul_f32 v62, v62, v83\n" \
    "v_mul_f32 v63, v63, v83\n" \
    "v_mfma_f32_16x16x32_bf16 v[72:75], a[180:183], v[172:175], v[72:75]\n" \
    "ds_read_b64_tr_b16 a[164:165], v18 offset:10240\n" \
    "v_cvt_pk_bf16_f32 v94, v60, v61\n" \
    "v_mfma_f32_16x16x32_bf16 v[72:75], a[184:187], v[176:179], v[72:75]\n" \
    "ds_read_b64_tr_b16 a[166:167], v18 offset:11264\n" \
    "v_cvt_pk_bf16_f32 v95, v62, v63\n" \
    "v_mfma_f32_16x16x32_bf16 v[72:75], a[188:191], v[180:183], v[72:75]\n" \
    "v_add_u32 v18, s52, v19\n" \
    "v_mfma_f32_16x16x32_bf16 v[76:79], a[168:171], v[232:235], v[76:79]\n" \
    "buffer_store_dwordx2 v[94:95], v96, s[40:43], s77 offen sc1\n" \
    "v_mfma_f32_16x16x32_bf16 v[76:79], a[172:175], v[236:239], v[76:79]\n" \
    "s_add_u32 s51, s51, 1\n" \
    "v_mfma_f32_16x16x32_bf16 v[76:79], a[176:179], v[240:243], v[76:79]\n" \
    "s_cmp_eq_u64 s[60:61], 0\n" \
    "s_cbranch_scc1 K1_QKt0%=\n" \
    "buffer_store_dword v88, v97, s[40:43], s78 offen sc1\n" \
    "buffer_store_dword v89, v98, s[40:43], s78 offen sc1\n" \
    "buffer_store_dword v90, v97, s[40:43], s79 offen sc1\n" \
    "buffer_store_dword v91, v98, s[40:43], s79 offen sc1\n" \
    "buffer_store_dword v92, v97, s[40:43], s80 offen sc1\n" \
    "buffer_store_dword v93, v98, s[40:43], s80 offen sc1\n" \
    "buffer_store_dword v94, v97, s[40:43], s81 offen sc1\n" \
    "buffer_store_dword v95, v98, s[40:43], s81 offen sc1\n" \
    "K1_QKt0%=:\n" \
    "v_mfma_f32_16x16x32_bf16 v[76:79], a[180:183], v[244:247], v[76:79]\n" \
    "v_mfma_f32_16x16x32_bf16 v[76:79], a[184:187], v[248:251], v[76:79]\n" \
    "v_mfma_f32_16x16x32_bf16 v[76:79], a[188:191], v[252:255], v[76:79]\n" \
    "s_waitcnt vmcnt(19)\n" \
    "s_barrier\n" \
    "s_cmp_lt_u32 s51, s67\n" \
    "s_cbranch_scc1 K1_QK%=\n" \
    "v_mul_f32 v64, v64, v80\n" \
    "v_mul_f32 v65, v65, v80\n" \
    "v_mul_f32 v66, v66, v80\n" \
    "v_mul_f32 v67, v67, v80\n" \
    "v_cvt_pk_bf16_f32 v88, v64, v65\n" \
    "v_cvt_pk_bf16_f32 v89, v66, v67\n" \
    "buffer_store_dwordx2 v[88:89], v99, s[40:43], s74 offen sc1\n" \
    "v_mul_f32 v68, v68, v81\n" \
    "v_mul_f32 v69, v69, v81\n" \
    "v_mul_f32 v70, v70, v81\n" \
    "v_mul_f32 v71, v71, v81\n" \
    "v_cvt_pk_bf16_f32 v90, v68, v69\n" \
    "v_cvt_pk_bf16_f32 v91, v70, v71\n" \
    "buffer_store_dwordx2 v[90:91], v99, s[40:43], s75 offen sc1\n" \
    "v_mul_f32 v72, v72, v82\n" \
    "v_mul_f32 v73, v73, v82\n" \
    "v_mul_f32 v74, v74, v82\n" \
    "v_mul_f32 v75, v75, v82\n" \
    "v_cvt_pk_bf16_f32 v92, v72, v73\n" \
    "v_cvt_pk_bf16_f32 v93, v74, v75\n" \
    "buffer_store_dwordx2 v[92:93], v99, s[40:43], s76 offen sc1\n" \
    "v_mul_f32 v76, v76, v83\n" \
    "v_mul_f32 v77, v77, v83\n" \
    "v_mul_f32 v78, v78, v83\n" \
    "v_mul_f32 v79, v79, v83\n" \
    "v_cvt_pk_bf16_f32 v94, v76, v77\n" \
    "v_cvt_pk_bf16_f32 v95, v78, v79\n" \
    "buffer_store_dwordx2 v[94:95], v99, s[40:43], s77 offen sc1\n" \
    "s_cmp_eq_u64 s[62:63], 0\n" \
    "s_cbranch_scc1 K1_QKt2%=\n" \
    "buffer_store_dword v88, v100, s[40:43], s78 offen sc1\n" \
    "buffer_store_dword v89, v101, s[40:43], s78 offen sc1\n" \
    "buffer_store_dword v90, v100, s[40:43], s79 offen sc1\n" \
    "buffer_store_dword v91, v101, s[40:43], s79 offen sc1\n" \
    "buffer_store_dword v92, v100, s[40:43], s80 offen sc1\n" \
    "buffer_store_dword v93, v101, s[40:43], s80 offen sc1\n" \
    "buffer_store_dword v94, v100, s[40:43], s81 offen sc1\n" \
    "buffer_store_dword v95, v101, s[40:43], s81 offen sc1\n" \
    "K1_QKt2%=:\n" \
    "K1_GU%=:\n" \
    "s_waitcnt lgkmcnt(0)\n" \
    "v_mfma_f32_16x16x32_bf16 v[48:51], a[144:147], a[0:3], 0\n" \
    "ds_read_b64_tr_b16 a[168:169], v16 offset:12288\n" \
    "v_mfma_f32_16x16x32_bf16 v[48:51], a[148:151], a[4:7], v[48:51]\n" \
    "ds_read_b64_tr_b16 a[170:171], v16 offset:13312\n" \
    "v_readlane_b32 s44, v21, s50\n" \
    "v_mfma_f32_16x16x32_bf16 v[48:51], a[152:155], a[8:11], v[48:51]\n" \
    "v_readlane_b32 s45, v22, s50\n" \
    "v_readlane_b32 s46, v23, s50\n" \
    "v_mfma_f32_16x16x32_bf16 v[48:51], a[156:159], a[12:15], v[48:51]\n" \
    "ds_read_b64_tr_b16 a[172:173], v16 offset:14336\n" \
    "v_readlane_b32 s57, v24, s50\n" \
    "v_mfma_f32_16x16x32_bf16 v[48:51], a[160:163], a[16:19], v[48:51]\n" \
    "ds_read_b64_tr_b16 a[174:175], v16 offset:15360\n" \
    "s_add_u32 m0, s69, s53\n" \
    "v_mfma_f32_16x16x32_bf16 v[48:51], a[164:167], a[20:23], v[48:51]\n" \
    "s_add_u32 s50, s50, 1\n" \
    "v_mfma_f32_16x16x32_bf16 v[52:55], a[144:147], a[72:75], 0\n" \
    "ds_read_b64_tr_b16 a[176:177], v16 offset:16384\n" \
    "v_mul_f32 v78, v78, v36\n" \
    "v_mfma_f32_16x16x32_bf16 v[52:55], a[148:151], a[76:79], v[52:55]\n" \
    "ds_read_b64_tr_b16 a[178:179], v16 offset:17408\n" \
    "v_mul_f32 v79, v79, v36\n" \
    "v_mfma_f32_16x16x32_bf16 v[52:55], a[152:155], a[80:83], v[52:55]\n" \
    "v_exp_f32 v78, v78\n" \
    "v_mfma_f32_16x16x32_bf16 v[52:55], a[156:159], a[84:87], v[52:55]\n" \
    "ds_read_b64_tr_b16 a[180:181], v16 offset:18432\n" \
    "v_mad_u32_u24 v20, v14, s57, v15\n" \
    "v_mfma_f32_16x16x32_bf16 v[52:55], a[160:163], a[88:91], v[52:55]\n" \
    "ds_read_b64_tr_b16 a[182:183], v16 offset:19456\n" \
    "s_mul_i32 s48, s57, s56\n" \
    "v_mfma_f32_16x16x32_bf16 v[52:55], a[164:167], a[92:95], v[52:55]\n" \
    "v_exp_f32 v79, v79\n" \
    "v_mfma_f32_16x16x32_bf16 v[56:59], a[144:147], v[112:115], 0\n" \
    "ds_read_b64_tr_b16 a[184:185], v16 offset:20480\n" \
    "v_fma_f32 v78, v78, v32, v32\n" \
    "v_mfma_f32_16x16x32_bf16 v[56:59], a[148:151], v[116:119], v[56:59]\n" \
    "ds_read_b64_tr_b16 a[186:187], v16 offset:21504\n" \
    "v_fma_f32 v79, v79, v32, v32\n" \
    "v_mfma_f32_16x16x32_bf16 v[56:59], a[152:155], v[120:123], v[56:59]\n" \
    "s_lshl_b32 s49, s57, 4\n" \
    "s_sub_u32 s49, s49, 0x400\n" \
    "v_mfma_f32_16x16x32_bf16 v[56:59], a[156:159], v[124:127], v[56:59]\n" \
    "ds_read_b64_tr_b16 a[188:189], v16 offset:22528\n" \
    "s_add_u32 s53, s53, 36864\n" \
    "v_mfma_f32_16x16x32_bf16 v[56:59], a[160:163], v[128:131], v[56:59]\n" \
    "ds_read_b64_tr_b16 a[190:191], v16 offset:23552\n" \
    "v_mfma_f32_16x16x32_bf16 v[56:59], a[164:167], v[132:135], v[56:59]\n" \
    "v_rcp_f32 v78, v78\n" \
    "v_mfma_f32_16x16x32_bf16 v[60:63], a[144:147], v[184:187], 0\n" \
    "v_rcp_f32 v79, v79\n" \
    "v_mfma_f32_16x16x32_bf16 v[60:63], a[148:151], v[188:191], v[60:63]\n" \
    "v_mul_f32 v64, v64, v96\n" \
    "v_mul_f32 v65, v65, v97\n" \
    "v_mfma_f32_16x16x32_bf16 v[60:63], a[152:155], v[192:195], v[60:63]\n" \
    "s_cmp_eq_u32 s53, 147456\n" \
    "s_cselect_b32 s53, 0, s53\n" \
    "v_mfma_f32_16x16x32_bf16 v[60:63], a[156:159], v[196:199], v[60:63]\n" \
    "buffer_load_dwordx4 v20, s[44:47], s48 offen lds\n" \
    "v_mfma_f32_16x16x32_bf16 v[60:63], a[160:163], v[200:203], v[60:63]\n" \
    "v_mul_f32 v66, v66, v98\n" \
    "s_add_u32 s48, s48, s49\n" \
    "v_mfma_f32_16x16x32_bf16 v[60:63], a[164:167], v[204:207], v[60:63]\n" \
    "v_mul_f32 v67, v67, v99\n" \
    "s_waitcnt lgkmcnt(0)\n" \
    "v_mfma_f32_16x16x32_bf16 v[48:51], a[168:171], a[24:27], v[48:51]\n" \
    "ds_read_b64_tr_b16 a[144:145], v16 offset:24576\n" \
    "v_cvt_pk_bf16_f32 v82, v64, v65\n" \
    "v_mfma_f32_16x16x32_bf16 v[48:51], a[172:175], a[28:31], v[48:51]\n" \
    "ds_read_b64_tr_b16 a[146:147], v16 offset:25600\n" \
    "v_mfma_f32_16x16x32_bf16 v[48:51], a[176:179], a[32:35], v[48:51]\n" \
    "buffer_load_dwordx4 v20, s[44:47], s48 offen offset:1024 lds\n" \
    "v_mfma_f32_16x16x32_bf16 v[48:51], a[180:183], a[36:39], v[48:51]\n" \
    "ds_read_b64_tr_b16 a[148:149], v16 offset:26624\n" \
    "s_add_u32 s48, s48, s49\n" \
    "v_mfma_f32_16x16x32_bf16 v[48:51], a[184:187], a[40:43], v[48:51]\n" \
    "ds_read_b64_tr_b16 a[150:151], v16 offset:27648\n" \
    "v_cvt_pk_bf16_f32 v83, v66, v67\n" \
    "v_mfma_f32_16x16x32_bf16 v[48:51], a[188:191], a[44:47], v[48:51]\n" \
    "buffer_load_dwordx4 v20, s[44:47], s48 offen offset:2048 lds\n" \
    "v_mfma_f32_16x16x32_bf16 v[52:55], a[168:171], a[96:99], v[52:55]\n" \
    "ds_read_b64_tr_b16 a[152:153], v16 offset:28672\n" \
    "v_mul_f32 v68, v68, v100\n" \
    "v_mfma_f32_16x16x32_bf16 v[52:55], a[172:175], a[100:103], v[52:55]\n" \
    "ds_read_b64_tr_b16 a[154:155], v16 offset:29696\n" \
    "s_add_u32 s48, s48, s49\n" \
    "v_mfma_f32_16x16x32_bf16 v[52:55], a[176:179], a[104:107], v[52:55]\n" \
    "v_mul_f32 v69, v69, v101\n" \
    "v_mul_f32 v70, v70, v102\n" \
    "v_mfma_f32_16x16x32_bf16 v[52:55], a[180:183], a[108:111], v[52:55]\n" \
    "ds_read_b64_tr_b16 a[156:157], v16 offset:30720\n" \
    "v_mfma_f32_16x16x32_bf16 v[52:55], a[184:187], a[112:115], v[52:55]\n" \
    "ds_read_b64_tr_b16 a[158:159], v16 offset:31744\n" \
    "v_mul_f32 v71, v71, v103\n" \
    "v_mfma_f32_16x16x32_bf16 v[52:55], a[188:191], a[116:119], v[52:55]\n" \
    "buffer_load_dwordx4 v20, s[44:47], s48 offen offset:3072 lds\n" \
    "v_mfma_f32_16x16x32_bf16 v[56:59], a[168:171], v[136:139], v[56:59]\n" \
    "ds_read_b64_tr_b16 a[160:161], v16 offset:32768\n" \
    "s_add_u32 s48, s48, s49\n" \
    "v_mfma_f32_16x16x32_bf16 v[56:59], a[172:175], v[140:143], v[56:59]\n" \
    "ds_read_b64_tr_b16 a[162:163], v16 offset:33792\n" \
    "s_add_u32 m0, m0, 0x1000\n" \
    "v_mfma_f32_16x16x32_bf16 v[56:59], a[176:179], v[144:147], v[56:59]\n" \
    "v_cvt_pk_bf16_f32 v86, v68, v69\n" \
    "s_add_u32 s48, s48, 0x1000\n" \
    "v_mfma_f32_16x16x32_bf16 v[56:59], a[180:183], v[148:151], v[56:59]\n" \
    "ds_read_b64_tr_b16 a[164:165], v16 offset:34816\n" \
    "v_cvt_pk_bf16_f32 v87, v70, v71\n" \
    "v_mfma_f32_16x16x32_bf16 v[56:59], a[184:187], v[152:155], v[56:59]\n" \
    "ds_read_b64_tr_b16 a[166:167], v16 offset:35840\n" \
    "v_mul_f32 v72, v72, v104\n" \
    "v_mfma_f32_16x16x32_bf16 v[56:59], a[188:191], v[156:159], v[56:59]\n" \
    "buffer_load_dwordx4 v20, s[44:47], s48 offen lds\n" \
    "v_mfma_f32_16x16x32_bf16 v[60:63], a[168:171], v[208:211], v[60:63]\n" \
    "s_add_u32 s48, s48, s49\n" \
    "v_mul_f32 v73, v73, v105\n" \
    "v_mfma_f32_16x16x32_bf16 v[60:63], a[172:175], v[212:215], v[60:63]\n" \
    "buffer_load_dwordx4 v20, s[44:47], s48 offen offset:1024 lds\n" \
    "v_mfma_f32_16x16x32_bf16 v[60:63], a[176:179], v[216:219], v[60:63]\n" \
    "v_mul_f32 v74, v74, v106\n" \
    "s_add_u32 s48, s48, s49\n" \
    "v_mfma_f32_16x16x32_bf16 v[60:63], a[180:183], v[220:223], v[60:63]\n" \
    "v_mfma_f32_16x16x32_bf16 v[60:63], a[184:187], v[224:227], v[60:63]\n" \
    "v_mul_f32 v75, v75, v107\n" \
    "v_mfma_f32_16x16x32_bf16 v[60:63], a[188:191], v[228:231], v[60:63]\n" \
    "s_waitcnt lgkmcnt(0)\n" \
    "v_mfma_f32_16x16x32_bf16 v[48:51], a[144:147], a[48:51], v[48:51]\n" \
    "ds_read_b64_tr_b16 a[168:169], v17 offset:0\n" \
    "v_cvt_pk_bf16_f32 v90, v72, v73\n" \
    "v_mfma_f32_16x16x32_bf16 v[48:51], a[148:151], a[52:55], v[48:51]\n" \
    "ds_read_b64_tr_b16 a[170:171], v17 offset:1024\n" \
    "v_mfma_f32_16x16x32_bf16 v[48:51], a[152:155], a[56:59], v[48:51]\n" \
    "buffer_load_dwordx4 v20, s[44:47], s48 offen offset:2048 lds\n" \
    "v_mfma_f32_16x16x32_bf16 v[48:51], a[156:159], a[60:63], v[48:51]\n" \
    "ds_read_b64_tr_b16 a[172:173], v17 offset:2048\n" \
    "s_add_u32 s48, s48, s49\n" \
    "v_mfma_f32_16x16x32_bf16 v[48:51], a[160:163], a[64:67], v[48:51]\n" \
    "ds_read_b64_tr_b16 a[174:175], v17 offset:3072\n" \
    "v_cvt_pk_bf16_f32 v91, v74, v75\n" \
    "v_mfma_f32_16x16x32_bf16 v[48:51], a[164:167], a[68:71], v[48:51]\n" \
    "buffer_load_dwordx4 v20, s[44:47], s48 offen offset:3072 lds\n" \
    "v_mfma_f32_16x16x32_bf16 v[52:55], a[144:147], a[120:123], v[52:55]\n" \
    "ds_read_b64_tr_b16 a[176:177], v17 offset:4096\n" \
    "v_mul_f32 v76, v76, v108\n" \
    "v_mfma_f32_16x16x32_bf16 v[52:55], a[148:151], a[124:127], v[52:55]\n" \
    "ds_read_b64_tr_b16 a[178:179], v17 offset:5120\n" \
    "s_add_u32 s48, s48, s49\n" \
    "v_mfma_f32_16x16x32_bf16 v[52:55], a[152:155], a[128:131], v[52:55]\n" \
    "v_mul_f32 v77, v77, v109\n" \
    "s_add_u32 m0, m0, 0x1000\n" \
    "v_mfma_f32_16x16x32_bf16 v[52:55], a[156:159], a[132:135], v[52:55]\n" \
    "ds_read_b64_tr_b16 a[180:181], v17 offset:6144\n" \
    "v_mul_f32 v78, v78, v110\n" \
    "v_mfma_f32_16x16x32_bf16 v[52:55], a[160:163], a[136:139], v[52:55]\n" \
    "ds_read_b64_tr_b16 a[182:183], v17 offset:7168\n" \
    "s_add_u32 s48, s48, 0x1000\n" \
    "v_mfma_f32_16x16x32_bf16 v[52:55], a[164:167], a[140:143], v[52:55]\n" \
    "v_mul_f32 v79, v79, v111\n" \
    "v_mfma_f32_16x16x32_bf16 v[56:59], a[144:147], v[160:163], v[56:59]\n" \
    "ds_read_b64_tr_b16 a[184:185], v17 offset:8192\n" \
    "v_cvt_pk_bf16_f32 v94, v76, v77\n" \
    "v_mfma_f32_16x16x32_bf16 v[56:59], a[148:151], v[164:167], v[56:59]\n" \
    "ds_read_b64_tr_b16 a[186:187], v17 offset:9216\n" \
    "v_mfma_f32_16x16x32_bf16 v[56:59], a[152:155], v[168:171], v[56:59]\n" \
    "buffer_load_dwordx4 v20, s[44:47], s48 offen lds\n" \
    "v_mfma_f32_16x16x32_bf16 v[56:59], a[156:159], v[172:175], v[56:59]\n" \
    "ds_read_b64_tr_b16 a[188:189], v17 offset:10240\n" \
    "v_cvt_pk_bf16_f32 v95, v78, v79\n" \
    "v_mfma_f32_16x16x32_bf16 v[56:59], a[160:163], v[176:179], v[56:59]\n" \
    "ds_read_b64_tr_b16 a[190:191], v17 offset:11264\n" \
    "v_mfma_f32_16x16x32_bf16 v[56:59], a[164:167], v[180:183], v[56:59]\n" \
    "buffer_store_dwordx4 v[80:83], v8, s[40:43], s83 offen sc1\n" \
    "s_add_u32 s83, s83, 147456\n" \
    "v_mfma_f32_16x16x32_bf16 v[60:63], a[144:147], v[232:235], v[60:63]\n" \
    "v_mfma_f32_16x16x32_bf16 v[60:63], a[148:151], v[236:239], v[60:63]\n" \
    "v_mfma_f32_16x16x32_bf16 v[60:63], a[152:155], v[240:243], v[60:63]\n" \
    "v_mfma_f32_16x16x32_bf16 v[60:63], a[156:159], v[244:247], v[60:63]\n" \
    "v_mfma_f32_16x16x32_bf16 v[60:63], a[160:163], v[248:251], v[60:63]\n" \
    "v_mfma_f32_16x16x32_bf16 v[60:63], a[164:167], v[252:255], v[60:63]\n" \
    "s_waitcnt lgkmcnt(0)\n" \
    "v_mfma_f32_16x16x32_bf16 v[64:67], a[168:171], a[0:3], 0\n" \
    "ds_read_b64_tr_b16 a[144:145], v17 offset:12288\n" \
    "v_mfma_f32_16x16x32_bf16 v[64:67], a[172:175], a[4:7], v[64:67]\n" \
    "ds_read_b64_tr_b16 a[146:147], v17 offset:13312\n" \
    "v_mfma_f32_16x16x32_bf16 v[64:67], a[176:179], a[8:11], v[64:67]\n" \
    "v_mfma_f32_16x16x32_bf16 v[64:67], a[180:183], a[12:15], v[64:67]\n" \
    "ds_read_b64_tr_b16 a[148:149], v17 offset:14336\n" \
    "v_mfma_f32_16x16x32_bf16 v[64:67], a[184:187], a[16:19], v[64:67]\n" \
    "ds_read_b64_tr_b16 a[150:151], v17 offset:15360\n" \
    "v_mfma_f32_16x16x32_bf16 v[64:67], a[188:191], a[20:23], v[64:67]\n" \
    "v_mfma_f32_16x16x32_bf16 v[68:71], a[168:171], a[72:75], 0\n" \
    "ds_read_b64_tr_b16 a[152:153], v17 offset:16384\n" \
    "v_mul_f32 v48, v48, v33\n" \
    "v_mfma_f32_16x16x32_bf16 v[68:71], a[172:175], a[76:79], v[68:71]\n" \
    "ds_read_b64_tr_b16 a[154:155], v17 offset:17408\n" \
    "v_mul_f32 v49, v49, v33\n" \
    "v_mfma_f32_16x16x32_bf16 v[68:71], a[176:179], a[80:83], v[68:71]\n" \
    "v_readlane_b32 s88, v21, s87\n" \
    "v_readlane_b32 s89, v22, s87\n" \
    "v_mfma_f32_16x16x32_bf16 v[68:71], a[180:183], a[84:87], v[68:71]\n" \
    "ds_read_b64_tr_b16 a[156:157], v17 offset:18432\n" \
    "s_add_u32 s87, s87, 1\n" \
    "v_mfma_f32_16x16x32_bf16 v[68:71], a[184:187], a[88:91], v[68:71]\n" \
    "ds_read_b64_tr_b16 a[158:159], v17 offset:19456\n" \
    "v_mfma_f32_16x16x32_bf16 v[68:71], a[188:191], a[92:95], v[68:71]\n" \
    "v_exp_f32 v48, v48\n" \
    "v_mfma_f32_16x16x32_bf16 v[72:75], a[168:171], v[112:115], 0\n" \
    "ds_read_b64_tr_b16 a[160:161], v17 offset:20480\n" \
    "v_lshrrev_b32 v47, 2, v8\n" \
    "v_mfma_f32_16x16x32_bf16 v[72:75], a[172:175], v[116:119], v[72:75]\n" \
    "ds_read_b64_tr_b16 a[162:163], v17 offset:21504\n" \
    "v_mfma_f32_16x16x32_bf16 v[72:75], a[176:179], v[120:123], v[72:75]\n" \
    "v_exp_f32 v49, v49\n" \
    "v_mfma_f32_16x16x32_bf16 v[72:75], a[180:183], v[124:127], v[72:75]\n" \
    "ds_read_b64_tr_b16 a[164:165], v17 offset:22528\n" \
    "v_fma_f32 v48, v48, v29, v29\n" \
    "v_mfma_f32_16x16x32_bf16 v[72:75], a[184:187], v[128:131], v[72:75]\n" \
    "ds_read_b64_tr_b16 a[166:167], v17 offset:23552\n" \
    "v_fma_f32 v49, v49, v29, v29\n" \
    "v_mfma_f32_16x16x32_bf16 v[72:75], a[188:191], v[132:135], v[72:75]\n" \
    "buffer_store_dwordx4 v[84:87], v8, s[40:43], s83 offen sc1\n" \
    "s_add_u32 s83, s83, 147456\n" \
    "v_mfma_f32_16x16x32_bf16 v[76:79], a[168:171], v[184:187], 0\n" \
    "v_rcp_f32 v48, v48\n" \
    "v_mfma_f32_16x16x32_bf16 v[76:79], a[172:175], v[188:191], v[76:79]\n" \
    "v_rcp_f32 v49, v49\n" \
    "v_mfma_f32_16x16x32_bf16 v[76:79], a[176:179], v[192:195], v[76:79]\n" \
    "v_mul_f32 v50, v50, v33\n" \
    "v_mul_f32 v51, v51, v33\n" \
    "v_mfma_f32_16x16x32_bf16 v[76:79], a[180:183], v[196:199], v[76:79]\n" \
    "v_exp_f32 v50, v50\n" \
    "v_mfma_f32_16x16x32_bf16 v[76:79], a[184:187], v[200:203], v[76:79]\n" \
    "v_mul_u32_u24 v47, 9, v47\n" \
    "v_mfma_f32_16x16x32_bf16 v[76:79], a[188:191], v[204:207], v[76:79]\n" \
    "s_waitcnt lgkmcnt(0)\n" \
    "v_mfma_f32_16x16x32_bf16 v[64:67], a[144:147], a[24:27], v[64:67]\n" \
    "ds_read_b64_tr_b16 a[168:169], v17 offset:24576\n" \
    "v_mfma_f32_16x16x32_bf16 v[64:67], a[148:151], a[28:31], v[64:67]\n" \
    "ds_read_b64_tr_b16 a[170:171], v17 offset:25600\n" \
    "v_mfma_f32_16x16x32_bf16 v[64:67], a[152:155], a[32:35], v[64:67]\n" \
    "v_exp_f32 v51, v51\n" \
    "v_mfma_f32_16x16x32_bf16 v[64:67], a[156:159], a[36:39], v[64:67]\n" \
    "ds_read_b64_tr_b16 a[172:173], v17 offset:26624\n" \
    "v_fma_f32 v50, v50, v29, v29\n" \
    "v_mfma_f32_16x16x32_bf16 v[64:67], a[160:163], a[40:43], v[64:67]\n" \
    "ds_read_b64_tr_b16 a[174:175], v17 offset:27648\n" \
    "v_fma_f32 v51, v51, v29, v29\n" \
    "v_mfma_f32_16x16x32_bf16 v[64:67], a[164:167], a[44:47], v[64:67]\n" \
    "global_load_dword a251, v47, s[88:89]\n" \
    "v_mfma_f32_16x16x32_bf16 v[68:71], a[144:147], a[96:99], v[68:71]\n" \
    "ds_read_b64_tr_b16 a[176:177], v17 offset:28672\n" \
    "v_rcp_f32 v50, v50\n" \
    "v_mfma_f32_16x16x32_bf16 v[68:71], a[148:151], a[100:103], v[68:71]\n" \
    "ds_read_b64_tr_b16 a[178:179], v17 offset:29696\n" \
    "v_readlane_b32 s88, v21, s87\n" \
    "v_mfma_f32_16x16x32_bf16 v[68:71], a[152:155], a[104:107], v[68:71]\n" \
    "v_rcp_f32 v51, v51\n" \
    "v_mfma_f32_16x16x32_bf16 v[68:71], a[156:159], a[108:111], v[68:71]\n" \
    "ds_read_b64_tr_b16 a[180:181], v17 offset:30720\n" \
    "v_mul_f32 v52, v52, v34\n" \
    "v_mfma_f32_16x16x32_bf16 v[68:71], a[160:163], a[112:115], v[68:71]\n" \
    "ds_read_b64_tr_b16 a[182:183], v17 offset:31744\n" \
    "v_readlane_b32 s89, v22, s87\n" \
    "v_mfma_f32_16x16x32_bf16 v[68:71], a[164:167], a[116:119], v[68:71]\n" \
    "v_mul_f32 v53, v53, v34\n" \
    "v_exp_f32 v52, v52\n" \
    "v_mfma_f32_16x16x32_bf16 v[72:75], a[144:147], v[136:139], v[72:75]\n" \
    "ds_read_b64_tr_b16 a[184:185], v17 offset:32768\n" \
    "s_add_u32 s87, s87, 1\n" \
    "v_mfma_f32_16x16x32_bf16 v[72:75], a[148:151], v[140:143], v[72:75]\n" \
    "ds_read_b64_tr_b16 a[186:187], v17 offset:33792\n" \
    "v_lshrrev_b32 v47, 2, v8\n" \
    "v_mfma_f32_16x16x32_bf16 v[72:75], a[152:155], v[144:147], v[72:75]\n" \
    "v_exp_f32 v53, v53\n" \
    "v_mfma_f32_16x16x32_bf16 v[72:75], a[156:159], v[148:151], v[72:75]\n" \
    "ds_read_b64_tr_b16 a[188:189], v17 offset:34816\n" \
    "v_fma_f32 v52, v52, v30, v30\n" \
    "v_mfma_f32_16x16x32_bf16 v[72:75], a[160:163], v[152:155], v[72:75]\n" \
    "ds_read_b64_tr_b16 a[190:191], v17 offset:35840\n" \
    "v_fma_f32 v53, v53, v30, v30\n" \
    "v_mfma_f32_16x16x32_bf16 v[72:75], a[164:167], v[156:159], v[72:75]\n" \
    "buffer_store_dwordx4 v[88:91], v8, s[40:43], s83 offen sc1\n" \
    "s_add_u32 s83, s83, 147456\n" \
    "v_mfma_f32_16x16x32_bf16 v[76:79], a[144:147], v[208:211], v[76:79]\n" \
    "v_rcp_f32 v52, v52\n" \
    "v_mfma_f32_16x16x32_bf16 v[76:79], a[148:151], v[212:215], v[76:79]\n" \
    "v_rcp_f32 v53, v53\n" \
    "v_mfma_f32_16x16x32_bf16 v[76:79], a[152:155], v[216:219], v[76:79]\n" \
    "v_mul_u32_u24 v47, 9, v47\n" \
    "v_mul_f32 v54, v54, v34\n" \
    "v_mfma_f32_16x16x32_bf16 v[76:79], a[156:159], v[220:223], v[76:79]\n" \
    "v_mul_f32 v55, v55, v34\n" \
    "v_exp_f32 v54, v54\n" \
    "v_mfma_f32_16x16x32_bf16 v[76:79], a[160:163], v[224:227], v[76:79]\n" \
    "global_load_dword a251, v47, s[88:89]\n" \
    "v_mfma_f32_16x16x32_bf16 v[76:79], a[164:167], v[228:231], v[76:79]\n" \
    "s_sub_u32 s84, s68, 1\n" \
    "s_or_b32 s84, s84, %6\n" \
    "s_cmp_lg_u32 s84, 0\n" \
    "s_cbranch_scc1 K1_FA2%=\n" \
    "s_mov_b64 s[60:61], %14\n" \
    "s_bfe_u32 s86, s60, 0x30014\n" \
    "s_lshl_b32 s86, s86, 11\n" \
    "s_add_u32 s86, s86, 41951232\n" \
    "v_lshlrev_b32 v39, 1, v8\n" \
    "buffer_load_dwordx4 v[40:43], v39, s[40:43], s86 offen sc1\n" \
    "buffer_load_dwordx4 v[10:13], v39, s[40:43], s86 offen offset:16 sc1\n" \
    "K1_FA2%=:\n" \
    "s_waitcnt lgkmcnt(0)\n" \
    "v_mfma_f32_16x16x32_bf16 v[64:67], a[168:171], a[48:51], v[64:67]\n" \
    "ds_read_b64_tr_b16 a[144:145], v18 offset:0\n" \
    "v_exp_f32 v55, v55\n" \
    "v_mfma_f32_16x16x32_bf16 v[64:67], a[172:175], a[52:55], v[64:67]\n" \
    "ds_read_b64_tr_b16 a[146:147], v18 offset:1024\n" \
    "s_add_u32 s52, s52, 36864\n" \
    "v_mfma_f32_16x16x32_bf16 v[64:67], a[176:179], a[56:59], v[64:67]\n" \
    "v_fma_f32 v54, v54, v30, v30\n" \
    "v_fma_f32 v55, v55, v30, v30\n" \
    "v_mfma_f32_16x16x32_bf16 v[64:67], a[180:183], a[60:63], v[64:67]\n" \
    "ds_read_b64_tr_b16 a[148:149], v18 offset:2048\n" \
    "s_cmp_eq_u32 s52, 147456\n" \
    "s_cselect_b32 s52, 0, s52\n" \
    "v_mfma_f32_16x16x32_bf16 v[64:67], a[184:187], a[64:67], v[64:67]\n" \
    "ds_read_b64_tr_b16 a[150:151], v18 offset:3072\n" \
    "v_mov_b32 v16, v18\n" \
    "v_mfma_f32_16x16x32_bf16 v[64:67], a[188:191], a[68:71], v[64:67]\n" \
    "v_rcp_f32 v54, v54\n" \
    "v_mfma_f32_16x16x32_bf16 v[68:71], a[168:171], a[120:123], v[68:71]\n" \
    "ds_read_b64_tr_b16 a[152:153], v18 offset:4096\n" \
    "v_rcp_f32 v55, v55\n" \
    "v_mfma_f32_16x16x32_bf16 v[68:71], a[172:175], a[124:127], v[68:71]\n" \
    "ds_read_b64_tr_b16 a[154:155], v18 offset:5120\n" \
    "v_mul_f32 v56, v56, v35\n" \
    "v_mfma_f32_16x16x32_bf16 v[68:71], a[176:179], a[128:131], v[68:71]\n" \
    "v_mul_f32 v57, v57, v35\n" \
    "v_exp_f32 v56, v56\n" \
    "v_mfma_f32_16x16x32_bf16 v[68:71], a[180:183], a[132:135], v[68:71]\n" \
    "ds_read_b64_tr_b16 a[156:157], v18 offset:6144\n" \
    "v_xor_b32 v17, 32, v18\n" \
    "v_mfma_f32_16x16x32_bf16 v[68:71], a[184:187], a[136:139], v[68:71]\n" \
    "ds_read_b64_tr_b16 a[158:159], v18 offset:7168\n" \
    "v_mfma_f32_16x16x32_bf16 v[68:71], a[188:191], a[140:143], v[68:71]\n" \
    "v_exp_f32 v57, v57\n" \
    "v_mfma_f32_16x16x32_bf16 v[72:75], a[168:171], v[160:163], v[72:75]\n" \
    "ds_read_b64_tr_b16 a[160:161], v18 offset:8192\n" \
    "v_fma_f32 v56, v56, v31, v31\n" \
    "v_mfma_f32_16x16x32_bf16 v[72:75], a[172:175], v[164:167], v[72:75]\n" \
    "ds_read_b64_tr_b16 a[162:163], v18 offset:9216\n" \
    "v_fma_f32 v57, v57, v31, v31\n" \
    "v_mfma_f32_16x16x32_bf16 v[72:75], a[176:179], v[168:171], v[72:75]\n" \
    "v_rcp_f32 v56, v56\n" \
    "v_mfma_f32_16x16x32_bf16 v[72:75], a[180:183], v[172:175], v[72:75]\n" \
    "ds_read_b64_tr_b16 a[164:165], v18 offset:10240\n" \
    "v_rcp_f32 v57, v57\n" \
    "v_mfma_f32_16x16x32_bf16 v[72:75], a[184:187], v[176:179], v[72:75]\n" \
    "ds_read_b64_tr_b16 a[166:167], v18 offset:11264\n" \
    "v_mul_f32 v58, v58, v35\n" \
    "v_mfma_f32_16x16x32_bf16 v[72:75], a[188:191], v[180:183], v[72:75]\n" \
    "buffer_store_dwordx4 v[92:95], v8, s[40:43], s83 offen sc1\n" \
    "s_add_u32 s83, s83, 147456\n" \
    "v_add_u32 v18, s52, v19\n" \
    "v_mfma_f32_16x16x32_bf16 v[76:79], a[168:171], v[232:235], v[76:79]\n" \
    "v_readlane_b32 s83, v25, s51\n" \
    "v_mul_f32 v59, v59, v35\n" \
    "v_mfma_f32_16x16x32_bf16 v[76:79], a[172:175], v[236:239], v[76:79]\n" \
    "s_add_u32 s51, s51, 1\n" \
    "v_mfma_f32_16x16x32_bf16 v[76:79], a[176:179], v[240:243], v[76:79]\n" \
    "v_exp_f32 v58, v58\n" \
    "v_mfma_f32_16x16x32_bf16 v[76:79], a[180:183], v[244:247], v[76:79]\n" \
    "v_exp_f32 v59, v59\n" \
    "v_mfma_f32_16x16x32_bf16 v[76:79], a[184:187], v[248:251], v[76:79]\n" \
    "v_fma_f32 v58, v58, v31, v31\n" \
    "v_fma_f32 v59, v59, v31, v31\n" \
    "v_mfma_f32_16x16x32_bf16 v[76:79], a[188:191], v[252:255], v[76:79]\n" \
    "v_rcp_f32 v58, v58\n" \
    "v_rcp_f32 v59, v59\n" \
    "s_cmp_lg_u32 s82, 0\n" \
    "s_cbranch_scc1 K1_FA0%=\n" \
    "s_waitcnt vmcnt(15)\n" \
    "K1_FA0%=:\n" \
    "s_waitcnt vmcnt(17)\n" \
    "s_barrier\n" \
    "s_or_b32 s84, s82, %6\n" \
    "s_cmp_lg_u32 s84, 0\n" \
    "s_cbranch_scc1 K1_FA1%=\n" \
    "s_mov_b64 s[84:85], %15\n" \
    "v_lshlrev_b32 v39, 7, v8\n" \
    "s_mov_b64 exec, 0xff\n" \
    "buffer_store_dwordx2 %0, v39, s[40:43], s84 offen sc1\n" \
    "s_mov_b64 exec, -1\n" \
    "K1_FA1%=:\n" \
    "s_mov_b32 s82, 1\n" \
    "s_waitcnt lgkmcnt(0)\n" \
    "v_mfma_f32_16x16x32_bf16 v[80:83], a[144:147], a[0:3], 0\n" \
    "ds_read_b64_tr_b16 a[168:169], v16 offset:12288\n" \
    "v_mfma_f32_16x16x32_bf16 v[80:83], a[148:151], a[4:7], v[80:83]\n" \
    "ds_read_b64_tr_b16 a[170:171], v16 offset:13312\n" \
    "v_readlane_b32 s44, v21, s50\n" \
    "v_mfma_f32_16x16x32_bf16 v[80:83], a[152:155], a[8:11], v[80:83]\n" \
    "v_readlane_b32 s45, v22, s50\n" \
    "v_readlane_b32 s46, v23, s50\n" \
    "v_mfma_f32_16x16x32_bf16 v[80:83], a[156:159], a[12:15], v[80:83]\n" \
    "ds_read_b64_tr_b16 a[172:173], v16 offset:14336\n" \
    "v_readlane_b32 s57, v24, s50\n" \
    "v_mfma_f32_16x16x32_bf16 v[80:83], a[160:163], a[16:19], v[80:83]\n" \
    "ds_read_b64_tr_b16 a[174:175], v16 offset:15360\n" \
    "s_add_u32 m0, s69, s53\n" \
    "v_mfma_f32_16x16x32_bf16 v[80:83], a[164:167], a[20:23], v[80:83]\n" \
    "s_add_u32 s50, s50, 1\n" \
    "v_mfma_f32_16x16x32_bf16 v[84:87], a[144:147], a[72:75], 0\n" \
    "ds_read_b64_tr_b16 a[176:177], v16 offset:16384\n" \
    "v_mul_f32 v60, v60, v36\n" \
    "v_mfma_f32_16x16x32_bf16 v[84:87], a[148:151], a[76:79], v[84:87]\n" \
    "ds_read_b64_tr_b16 a[178:179], v16 offset:17408\n" \
    "v_mul_f32 v61, v61, v36\n" \
    "v_mfma_f32_16x16x32_bf16 v[84:87], a[152:155], a[80:83], v[84:87]\n" \
    "v_exp_f32 v60, v60\n" \
    "v_mfma_f32_16x16x32_bf16 v[84:87], a[156:159], a[84:87], v[84:87]\n" \
    "ds_read_b64_tr_b16 a[180:181], v16 offset:18432\n" \
    "v_mad_u32_u24 v20, v14, s57, v15\n" \
    "v_mfma_f32_16x16x32_bf16 v[84:87], a[160:163], a[88:91], v[84:87]\n" \
    "ds_read_b64_tr_b16 a[182:183], v16 offset:19456\n" \
    "s_mul_i32 s48, s57, s56\n" \
    "v_mfma_f32_16x16x32_bf16 v[84:87], a[164:167], a[92:95], v[84:87]\n" \
    "v_exp_f32 v61, v61\n" \
    "v_mfma_f32_16x16x32_bf16 v[88:91], a[144:147], v[112:115], 0\n" \
    "ds_read_b64_tr_b16 a[184:185], v16 offset:20480\n" \
    "v_fma_f32 v60, v60, v32, v32\n" \
    "v_mfma_f32_16x16x32_bf16 v[88:91], a[148:151], v[116:119], v[88:91]\n" \
    "ds_read_b64_tr_b16 a[186:187], v16 offset:21504\n" \
    "v_fma_f32 v61, v61, v32, v32\n" \
    "v_mfma_f32_16x16x32_bf16 v[88:91], a[152:155], v[120:123], v[88:91]\n" \
    "v_rcp_f32 v60, v60\n" \
    "v_mfma_f32_16x16x32_bf16 v[88:91], a[156:159], v[124:127], v[88:91]\n" \
    "ds_read_b64_tr_b16 a[188:189], v16 offset:22528\n" \
    "s_lshl_b32 s49, s57, 4\n" \
    "v_mfma_f32_16x16x32_bf16 v[88:91], a[160:163], v[128:131], v[88:91]\n" \
    "ds_read_b64_tr_b16 a[190:191], v16 offset:23552\n" \
    "s_sub_u32 s49, s49, 0x400\n" \
    "v_mfma_f32_16x16x32_bf16 v[88:91], a[164:167], v[132:135], v[88:91]\n" \
    "v_rcp_f32 v61, v61\n" \
    "v_mfma_f32_16x16x32_bf16 v[92:95], a[144:147], v[184:187], 0\n" \
    "v_mul_f32 v62, v62, v36\n" \
    "v_mul_f32 v63, v63, v36\n" \
    "v_mfma_f32_16x16x32_bf16 v[92:95], a[148:151], v[188:191], v[92:95]\n" \
    "s_add_u32 s53, s53, 36864\n" \
    "v_mfma_f32_16x16x32_bf16 v[92:95], a[152:155], v[192:195], v[92:95]\n" \
    "v_exp_f32 v62, v62\n" \
    "v_mfma_f32_16x16x32_bf16 v[92:95], a[156:159], v[196:199], v[92:95]\n" \
    "v_exp_f32 v63, v63\n" \
    "v_mfma_f32_16x16x32_bf16 v[92:95], a[160:163], v[200:203], v[92:95]\n" \
    "s_cmp_eq_u32 s53, 147456\n" \
    "s_cselect_b32 s53, 0, s53\n" \
    "v_mfma_f32_16x16x32_bf16 v[92:95], a[164:167], v[204:207], v[92:95]\n" \
    "v_fma_f32 v62, v62, v32, v32\n" \
    "s_waitcnt lgkmcnt(0)\n" \
    "v_mfma_f32_16x16x32_bf16 v[80:83], a[168:171], a[24:27], v[80:83]\n" \
    "ds_read_b64_tr_b16 a[144:145], v16 offset:24576\n" \
    "v_fma_f32 v63, v63, v32, v32\n" \
    "v_mfma_f32_16x16x32_bf16 v[80:83], a[172:175], a[28:31], v[80:83]\n" \
    "ds_read_b64_tr_b16 a[146:147], v16 offset:25600\n" \
    "v_mfma_f32_16x16x32_bf16 v[80:83], a[176:179], a[32:35], v[80:83]\n" \
    "buffer_load_dwordx4 v20, s[44:47], s48 offen lds\n" \
    "v_mfma_f32_16x16x32_bf16 v[80:83], a[180:183], a[36:39], v[80:83]\n" \
    "ds_read_b64_tr_b16 a[148:149], v16 offset:26624\n" \
    "s_add_u32 s48, s48, s49\n" \
    "v_mfma_f32_16x16x32_bf16 v[80:83], a[184:187], a[40:43], v[80:83]\n" \
    "ds_read_b64_tr_b16 a[150:151], v16 offset:27648\n" \
    "buffer_load_dwordx4 v20, s[44:47], s48 offen offset:1024 lds\n" \
    "v_mfma_f32_16x16x32_bf16 v[80:83], a[188:191], a[44:47], v[80:83]\n" \
    "v_rcp_f32 v62, v62\n" \
    "v_mfma_f32_16x16x32_bf16 v[84:87], a[168:171], a[96:99], v[84:87]\n" \
    "ds_read_b64_tr_b16 a[152:153], v16 offset:28672\n" \
    "s_add_u32 s48, s48, s49\n" \
    "v_mfma_f32_16x16x32_bf16 v[84:87], a[172:175], a[100:103], v[84:87]\n" \
    "ds_read_b64_tr_b16 a[154:155], v16 offset:29696\n" \
    "v_mfma_f32_16x16x32_bf16 v[84:87], a[176:179], a[104:107], v[84:87]\n" \
    "v_rcp_f32 v63, v63\n" \
    "v_mfma_f32_16x16x32_bf16 v[84:87], a[180:183], a[108:111], v[84:87]\n" \
    "ds_read_b64_tr_b16 a[156:157], v16 offset:30720\n" \
    "v_mul_f32 v64, v64, v33\n" \
    "v_mfma_f32_16x16x32_bf16 v[84:87], a[184:187], a[112:115], v[84:87]\n" \
    "ds_read_b64_tr_b16 a[158:159], v16 offset:31744\n" \
    "v_mul_f32 v65, v65, v33\n" \
    "v_mfma_f32_16x16x32_bf16 v[84:87], a[188:191], a[116:119], v[84:87]\n" \
    "buffer_load_dwordx4 v20, s[44:47], s48 offen offset:2048 lds\n" \
    "v_mfma_f32_16x16x32_bf16 v[88:91], a[168:171], v[136:139], v[88:91]\n" \
    "ds_read_b64_tr_b16 a[160:161], v16 offset:32768\n" \
    "s_add_u32 s48, s48, s49\n" \
    "v_mfma_f32_16x16x32_bf16 v[88:91], a[172:175], v[140:143], v[88:91]\n" \
    "ds_read_b64_tr_b16 a[162:163], v16 offset:33792\n" \
    "v_mfma_f32_16x16x32_bf16 v[88:91], a[176:179], v[144:147], v[88:91]\n" \
    "v_exp_f32 v64, v64\n" \
    "v_mfma_f32_16x16x32_bf16 v[88:91], a[180:183], v[148:151], v[88:91]\n" \
    "ds_read_b64_tr_b16 a[164:165], v16 offset:34816\n" \
    "v_exp_f32 v65, v65\n" \
    "v_mfma_f32_16x16x32_bf16 v[88:91], a[184:187], v[152:155], v[88:91]\n" \
    "ds_read_b64_tr_b16 a[166:167], v16 offset:35840\n" \
    "v_fma_f32 v64, v64, v29, v29\n" \
    "v_mfma_f32_16x16x32_bf16 v[88:91], a[188:191], v[156:159], v[88:91]\n" \
    "buffer_load_dwordx4 v20, s[44:47], s48 offen offset:3072 lds\n" \
    "v_mfma_f32_16x16x32_bf16 v[92:95], a[168:171], v[208:211], v[92:95]\n" \
    "v_fma_f32 v65, v65, v29, v29\n" \
    "s_add_u32 s48, s48, s49\n" \
    "v_mfma_f32_16x16x32_bf16 v[92:95], a[172:175], v[212:215], v[92:95]\n" \
    "v_rcp_f32 v64, v64\n" \
    "v_mfma_f32_16x16x32_bf16 v[92:95], a[176:179], v[216:219], v[92:95]\n" \
    "s_add_u32 m0, m0, 0x1000\n" \
    "s_add_u32 s48, s48, 0x1000\n" \
    "v_mfma_f32_16x16x32_bf16 v[92:95], a[180:183], v[220:223], v[92:95]\n" \
    "v_rcp_f32 v65, v65\n" \
    "v_mfma_f32_16x16x32_bf16 v[92:95], a[184:187], v[224:227], v[92:95]\n" \
    "v_mul_f32 v66, v66, v33\n" \
    "v_mul_f32 v67, v67, v33\n" \
    "v_mfma_f32_16x16x32_bf16 v[92:95], a[188:191], v[228:231], v[92:95]\n" \
    "v_exp_f32 v66, v66\n" \
    "s_waitcnt lgkmcnt(0)\n" \
    "v_mfma_f32_16x16x32_bf16 v[80:83], a[144:147], a[48:51], v[80:83]\n" \
    "ds_read_b64_tr_b16 a[168:169], v17 offset:0\n" \
    "v_exp_f32 v67, v67\n" \
    "v_mfma_f32_16x16x32_bf16 v[80:83], a[148:151], a[52:55], v[80:83]\n" \
    "ds_read_b64_tr_b16 a[170:171], v17 offset:1024\n" \
    "v_fma_f32 v66, v66, v29, v29\n" \
    "v_mfma_f32_16x16x32_bf16 v[80:83], a[152:155], a[56:59], v[80:83]\n" \
    "buffer_load_dwordx4 v20, s[44:47], s48 offen lds\n" \
    "v_mfma_f32_16x16x32_bf16 v[80:83], a[156:159], a[60:63], v[80:83]\n" \
    "ds_read_b64_tr_b16 a[172:173], v17 offset:2048\n" \
    "s_add_u32 s48, s48, s49\n" \
    "v_mfma_f32_16x16x32_bf16 v[80:83], a[160:163], a[64:67], v[80:83]\n" \
    "ds_read_b64_tr_b16 a[174:175], v17 offset:3072\n" \
    "v_fma_f32 v67, v67, v29, v29\n" \
    "v_mfma_f32_16x16x32_bf16 v[80:83], a[164:167], a[68:71], v[80:83]\n" \
    "buffer_load_dwordx4 v20, s[44:47], s48 offen offset:1024 lds\n" \
    "v_mfma_f32_16x16x32_bf16 v[84:87], a[144:147], a[120:123], v[84:87]\n" \
    "ds_read_b64_tr_b16 a[176:177], v17 offset:4096\n" \
    "s_add_u32 s48, s48, s49\n" \
    "v_mfma_f32_16x16x32_bf16 v[84:87], a[148:151], a[124:127], v[84:87]\n" \
    "ds_read_b64_tr_b16 a[178:179], v17 offset:5120\n" \
    "buffer_load_dwordx4 v20, s[44:47], s48 offen offset:2048 lds\n" \
    "v_mfma_f32_16x16x32_bf16 v[84:87], a[152:155], a[128:131], v[84:87]\n" \
    "v_rcp_f32 v66, v66\n" \
    "v_mfma_f32_16x16x32_bf16 v[84:87], a[156:159], a[132:135], v[84:87]\n" \
    "ds_read_b64_tr_b16 a[180:181], v17 offset:6144\n" \
    "v_rcp_f32 v67, v67\n" \
    "v_mfma_f32_16x16x32_bf16 v[84:87], a[160:163], a[136:139], v[84:87]\n" \
    "ds_read_b64_tr_b16 a[182:183], v17 offset:7168\n" \
    "v_mul_f32 v68, v68, v34\n" \
    "v_mfma_f32_16x16x32_bf16 v[84:87], a[164:167], a[140:143], v[84:87]\n" \
    "s_add_u32 s48, s48, s49\n" \
    "v_mul_f32 v69, v69, v34\n" \
    "v_mfma_f32_16x16x32_bf16 v[88:91], a[144:147], v[160:163], v[88:91]\n" \
    "ds_read_b64_tr_b16 a[184:185], v17 offset:8192\n" \
    "v_exp_f32 v68, v68\n" \
    "v_mfma_f32_16x16x32_bf16 v[88:91], a[148:151], v[164:167], v[88:91]\n" \
    "ds_read_b64_tr_b16 a[186:187], v17 offset:9216\n" \
    "v_exp_f32 v69, v69\n" \
    "v_mfma_f32_16x16x32_bf16 v[88:91], a[152:155], v[168:171], v[88:91]\n" \
    "buffer_load_dwordx4 v20, s[44:47], s48 offen offset:3072 lds\n" \
    "v_mfma_f32_16x16x32_bf16 v[88:91], a[156:159], v[172:175], v[88:91]\n" \
    "ds_read_b64_tr_b16 a[188:189], v17 offset:10240\n" \
    "s_add_u32 s48, s48, s49\n" \
    "v_mfma_f32_16x16x32_bf16 v[88:91], a[160:163], v[176:179], v[88:91]\n" \
    "ds_read_b64_tr_b16 a[190:191], v17 offset:11264\n" \
    "v_fma_f32 v68, v68, v30, v30\n" \
    "v_mfma_f32_16x16x32_bf16 v[88:91], a[164:167], v[180:183], v[88:91]\n" \
    "s_add_u32 m0, m0, 0x1000\n" \
    "v_fma_f32 v69, v69, v30, v30\n" \
    "v_mfma_f32_16x16x32_bf16 v[92:95], a[144:147], v[232:235], v[92:95]\n" \
    "s_add_u32 s48, s48, 0x1000\n" \
    "v_mfma_f32_16x16x32_bf16 v[92:95], a[148:151], v[236:239], v[92:95]\n" \
    "v_rcp_f32 v68, v68\n" \
    "v_mfma_f32_16x16x32_bf16 v[92:95], a[152:155], v[240:243], v[92:95]\n" \
    "buffer_load_dwordx4 v20, s[44:47], s48 offen lds\n" \
    "v_mfma_f32_16x16x32_bf16 v[92:95], a[156:159], v[244:247], v[92:95]\n" \
    "v_rcp_f32 v69, v69\n" \
    "v_mfma_f32_16x16x32_bf16 v[92:95], a[160:163], v[248:251], v[92:95]\n" \
    "v_mul_f32 v70, v70, v34\n" \
    "v_mul_f32 v71, v71, v34\n" \
    "v_mfma_f32_16x16x32_bf16 v[92:95], a[164:167], v[252:255], v[92:95]\n" \
    "v_exp_f32 v70, v70\n" \
    "v_exp_f32 v71, v71\n" \
    "s_waitcnt lgkmcnt(0)\n" \
    "v_mfma_f32_16x16x32_bf16 v[96:99], a[168:171], a[0:3], 0\n" \
    "ds_read_b64_tr_b16 a[144:145], v17 offset:12288\n" \
    "v_mfma_f32_16x16x32_bf16 v[96:99], a[172:175], a[4:7], v[96:99]\n" \
    "ds_read_b64_tr_b16 a[146:147], v17 offset:13312\n" \
    "v_mfma_f32_16x16x32_bf16 v[96:99], a[176:179], a[8:11], v[96:99]\n" \
    "v_mfma_f32_16x16x32_bf16 v[96:99], a[180:183], a[12:15], v[96:99]\n" \
    "ds_read_b64_tr_b16 a[148:149], v17 offset:14336\n" \
    "v_mfma_f32_16x16x32_bf16 v[96:99], a[184:187], a[16:19], v[96:99]\n" \
    "ds_read_b64_tr_b16 a[150:151], v17 offset:15360\n" \
    "v_mfma_f32_16x16x32_bf16 v[96:99], a[188:191], a[20:23], v[96:99]\n" \
    "v_mfma_f32_16x16x32_bf16 v[100:103], a[168:171], a[72:75], 0\n" \
    "ds_read_b64_tr_b16 a[152:153], v17 offset:16384\n" \
    "v_mul_f32 v48, v48, v80\n" \
    "v_mfma_f32_16x16x32_bf16 v[100:103], a[172:175], a[76:79], v[100:103]\n" \
    "ds_read_b64_tr_b16 a[154:155], v17 offset:17408\n" \
    "v_mul_f32 v49, v49, v81\n" \
    "v_mfma_f32_16x16x32_bf16 v[100:103], a[176:179], a[80:83], v[100:103]\n" \
    "v_readlane_b32 s88, v21, s87\n" \
    "v_mul_f32 v50, v50, v82\n" \
    "v_mfma_f32_16x16x32_bf16 v[100:103], a[180:183], a[84:87], v[100:103]\n" \
    "ds_read_b64_tr_b16 a[156:157], v17 offset:18432\n" \
    "v_mul_f32 v51, v51, v83\n" \
    "v_mfma_f32_16x16x32_bf16 v[100:103], a[184:187], a[88:91], v[100:103]\n" \
    "ds_read_b64_tr_b16 a[158:159], v17 offset:19456\n" \
    "v_readlane_b32 s89, v22, s87\n" \
    "v_mfma_f32_16x16x32_bf16 v[100:103], a[188:191], a[92:95], v[100:103]\n" \
    "v_cvt_pk_bf16_f32 v80, v48, v49\n" \
    "v_cvt_pk_bf16_f32 v81, v50, v51\n" \
    "v_mfma_f32_16x16x32_bf16 v[104:107], a[168:171], v[112:115], 0\n" \
    "ds_read_b64_tr_b16 a[160:161], v17 offset:20480\n" \
    "v_mul_f32 v52, v52, v84\n" \
    "v_mfma_f32_16x16x32_bf16 v[104:107], a[172:175], v[116:119], v[104:107]\n" \
    "ds_read_b64_tr_b16 a[162:163], v17 offset:21504\n" \
    "s_add_u32 s87, s87, 1\n" \
    "v_mfma_f32_16x16x32_bf16 v[104:107], a[176:179], v[120:123], v[104:107]\n" \
    "v_mul_f32 v53, v53, v85\n" \
    "v_mul_f32 v54, v54, v86\n" \
    "v_mfma_f32_16x16x32_bf16 v[104:107], a[180:183], v[124:127], v[104:107]\n" \
    "ds_read_b64_tr_b16 a[164:165], v17 offset:22528\n" \
    "v_lshrrev_b32 v47, 2, v8\n" \
    "v_mfma_f32_16x16x32_bf16 v[104:107], a[184:187], v[128:131], v[104:107]\n" \
    "ds_read_b64_tr_b16 a[166:167], v17 offset:23552\n" \
    "v_mul_f32 v55, v55, v87\n" \
    "v_mfma_f32_16x16x32_bf16 v[104:107], a[188:191], v[132:135], v[104:107]\n" \
    "v_cvt_pk_bf16_f32 v84, v52, v53\n" \
    "v_cvt_pk_bf16_f32 v85, v54, v55\n" \
    "v_mfma_f32_16x16x32_bf16 v[108:111], a[168:171], v[184:187], 0\n" \
    "v_mul_u32_u24 v47, 9, v47\n" \
    "v_mul_f32 v56, v56, v88\n" \
    "v_mfma_f32_16x16x32_bf16 v[108:111], a[172:175], v[188:191], v[108:111]\n" \
    "v_mul_f32 v57, v57, v89\n" \
    "v_mul_f32 v58, v58, v90\n" \
    "v_mfma_f32_16x16x32_bf16 v[108:111], a[176:179], v[192:195], v[108:111]\n" \
    "global_load_dword a251, v47, s[88:89]\n" \
    "v_mfma_f32_16x16x32_bf16 v[108:111], a[180:183], v[196:199], v[108:111]\n" \
    "v_mul_f32 v59, v59, v91\n" \
    "v_readlane_b32 s88, v21, s87\n" \
    "v_mfma_f32_16x16x32_bf16 v[108:111], a[184:187], v[200:203], v[108:111]\n" \
    "v_cvt_pk_bf16_f32 v88, v56, v57\n" \
    "v_cvt_pk_bf16_f32 v89, v58, v59\n" \
    "v_mfma_f32_16x16x32_bf16 v[108:111], a[188:191], v[204:207], v[108:111]\n" \
    "v_mul_f32 v60, v60, v92\n" \
    "s_waitcnt lgkmcnt(0)\n" \
    "v_mfma_f32_16x16x32_bf16 v[96:99], a[144:147], a[24:27], v[96:99]\n" \
    "ds_read_b64_tr_b16 a[168:169], v17 offset:24576\n" \
    "v_readlane_b32 s89, v22, s87\n" \
    "v_mfma_f32_16x16x32_bf16 v[96:99], a[148:151], a[28:31], v[96:99]\n" \
    "ds_read_b64_tr_b16 a[170:171], v17 offset:25600\n" \
    "v_mul_f32 v61, v61, v93\n" \
    "v_mfma_f32_16x16x32_bf16 v[96:99], a[152:155], a[32:35], v[96:99]\n" \
    "v_mul_f32 v62, v62, v94\n" \
    "s_add_u32 s87, s87, 1\n" \
    "v_mfma_f32_16x16x32_bf16 v[96:99], a[156:159], a[36:39], v[96:99]\n" \
    "ds_read_b64_tr_b16 a[172:173], v17 offset:26624\n" \
    "v_mul_f32 v63, v63, v95\n" \
    "v_mfma_f32_16x16x32_bf16 v[96:99], a[160:163], a[40:43], v[96:99]\n" \
    "ds_read_b64_tr_b16 a[174:175], v17 offset:27648\n" \
    "v_cvt_pk_bf16_f32 v92, v60, v61\n" \
    "v_mfma_f32_16x16x32_bf16 v[96:99], a[164:167], a[44:47], v[96:99]\n" \
    "v_lshrrev_b32 v47, 2, v8\n" \
    "v_cvt_pk_bf16_f32 v93, v62, v63\n" \
    "v_mfma_f32_16x16x32_bf16 v[100:103], a[144:147], a[96:99], v[100:103]\n" \
    "ds_read_b64_tr_b16 a[176:177], v17 offset:28672\n" \
    "v_fma_f32 v70, v70, v30, v30\n" \
    "v_mfma_f32_16x16x32_bf16 v[100:103], a[148:151], a[100:103], v[100:103]\n" \
    "ds_read_b64_tr_b16 a[178:179], v17 offset:29696\n" \
    "v_fma_f32 v71, v71, v30, v30\n" \
    "v_mfma_f32_16x16x32_bf16 v[100:103], a[152:155], a[104:107], v[100:103]\n" \
    "v_mul_u32_u24 v47, 9, v47\n" \
    "v_mfma_f32_16x16x32_bf16 v[100:103], a[156:159], a[108:111], v[100:103]\n" \
    "ds_read_b64_tr_b16 a[180:181], v17 offset:30720\n" \
    "v_mfma_f32_16x16x32_bf16 v[100:103], a[160:163], a[112:115], v[100:103]\n" \
    "ds_read_b64_tr_b16 a[182:183], v17 offset:31744\n" \
    "v_mfma_f32_16x16x32_bf16 v[100:103], a[164:167], a[116:119], v[100:103]\n" \
    "v_rcp_f32 v70, v70\n" \
    "v_mfma_f32_16x16x32_bf16 v[104:107], a[144:147], v[136:139], v[104:107]\n" \
    "ds_read_b64_tr_b16 a[184:185], v17 offset:32768\n" \
    "v_mfma_f32_16x16x32_bf16 v[104:107], a[148:151], v[140:143], v[104:107]\n" \
    "ds_read_b64_tr_b16 a[186:187], v17 offset:33792\n" \
    "v_mfma_f32_16x16x32_bf16 v[104:107], a[152:155], v[144:147], v[104:107]\n" \
    "v_rcp_f32 v71, v71\n" \
    "v_mfma_f32_16x16x32_bf16 v[104:107], a[156:159], v[148:151], v[104:107]\n" \
    "ds_read_b64_tr_b16 a[188:189], v17 offset:34816\n" \
    "v_mul_f32 v72, v72, v35\n" \
    "v_mfma_f32_16x16x32_bf16 v[104:107], a[160:163], v[152:155], v[104:107]\n" \
    "ds_read_b64_tr_b16 a[190:191], v17 offset:35840\n" \
    "v_mul_f32 v73, v73, v35\n" \
    "v_mfma_f32_16x16x32_bf16 v[104:107], a[164:167], v[156:159], v[104:107]\n" \
    "global_load_dword a251, v47, s[88:89]\n" \
    "v_mfma_f32_16x16x32_bf16 v[108:111], a[144:147], v[208:211], v[108:111]\n" \
    "v_exp_f32 v72, v72\n" \
    "v_mfma_f32_16x16x32_bf16 v[108:111], a[148:151], v[212:215], v[108:111]\n" \
    "v_exp_f32 v73, v73\n" \
    "v_mfma_f32_16x16x32_bf16 v[108:111], a[152:155], v[216:219], v[108:111]\n" \
    "s_add_u32 s52, s52, 36864\n" \
    "v_fma_f32 v72, v72, v31, v31\n" \
    "v_mfma_f32_16x16x32_bf16 v[108:111], a[156:159], v[220:223], v[108:111]\n" \
    "v_fma_f32 v73, v73, v31, v31\n" \
    "v_mfma_f32_16x16x32_bf16 v[108:111], a[160:163], v[224:227], v[108:111]\n" \
    "s_cmp_eq_u32 s52, 147456\n" \
    "s_cselect_b32 s52, 0, s52\n" \
    "v_mfma_f32_16x16x32_bf16 v[108:111], a[164:167], v[228:231], v[108:111]\n" \
    "v_mov_b32 v16, v18\n" \
    "s_waitcnt lgkmcnt(0)\n" \
    "v_mfma_f32_16x16x32_bf16 v[96:99], a[168:171], a[48:51], v[96:99]\n" \
    "ds_read_b64_tr_b16 a[144:145], v18 offset:0\n" \
    "v_mfma_f32_16x16x32_bf16 v[96:99], a[172:175], a[52:55], v[96:99]\n" \
    "ds_read_b64_tr_b16 a[146:147], v18 offset:1024\n" \
    "v_mfma_f32_16x16x32_bf16 v[96:99], a[176:179], a[56:59], v[96:99]\n" \
    "v_rcp_f32 v72, v72\n" \
    "v_mfma_f32_16x16x32_bf16 v[96:99], a[180:183], a[60:63], v[96:99]\n" \
    "ds_read_b64_tr_b16 a[148:149], v18 offset:2048\n" \
    "v_xor_b32 v17, 32, v18\n" \
    "v_mfma_f32_16x16x32_bf16 v[96:99], a[184:187], a[64:67], v[96:99]\n" \
    "ds_read_b64_tr_b16 a[150:151], v18 offset:3072\n" \
    "v_mfma_f32_16x16x32_bf16 v[96:99], a[188:191], a[68:71], v[96:99]\n" \
    "v_rcp_f32 v73, v73\n" \
    "v_mfma_f32_16x16x32_bf16 v[100:103], a[168:171], a[120:123], v[100:103]\n" \
    "ds_read_b64_tr_b16 a[152:153], v18 offset:4096\n" \
    "v_mul_f32 v74, v74, v35\n" \
    "v_mfma_f32_16x16x32_bf16 v[100:103], a[172:175], a[124:127], v[100:103]\n" \
    "ds_read_b64_tr_b16 a[154:155], v18 offset:5120\n" \
    "v_mul_f32 v75, v75, v35\n" \
    "v_mfma_f32_16x16x32_bf16 v[100:103], a[176:179], a[128:131], v[100:103]\n" \
    "v_exp_f32 v74, v74\n" \
    "v_mfma_f32_16x16x32_bf16 v[100:103], a[180:183], a[132:135], v[100:103]\n" \
    "ds_read_b64_tr_b16 a[156:157], v18 offset:6144\n" \
    "v_mfma_f32_16x16x32_bf16 v[100:103], a[184:187], a[136:139], v[100:103]\n" \
    "ds_read_b64_tr_b16 a[158:159], v18 offset:7168\n" \
    "v_mfma_f32_16x16x32_bf16 v[100:103], a[188:191], a[140:143], v[100:103]\n" \
    "v_exp_f32 v75, v75\n" \
    "v_mfma_f32_16x16x32_bf16 v[104:107], a[168:171], v[160:163], v[104:107]\n" \
    "ds_read_b64_tr_b16 a[160:161], v18 offset:8192\n" \
    "s_sub_u32 s84, s68, 1\n" \
    "s_or_b32 s84, s84, %6\n" \
    "s_cmp_lg_u32 s84, 0\n" \
    "s_cbranch_scc1 K1_FA3%=\n" \
    "s_waitcnt vmcnt(9)\n" \
    "v_cmp_ne_u64 s[60:61], v[40:41], %0\n" \
    "v_cmp_ne_u64 s[62:63], v[42:43], %0\n" \
    "v_cmp_ne_u64 s[64:65], v[10:11], %0\n" \
    "v_cmp_ne_u64 vcc, v[12:13], %0\n" \
    "s_or_b64 s[60:61], s[60:61], s[62:63]\n" \
    "s_or_b64 s[64:65], s[64:65], vcc\n" \
    "s_or_b64 s[60:61], s[60:61], s[64:65]\n" \
    "s_cbranch_scc1 FA_SPIN%=\n" \
    "FA_OK%=:\n" \
    "K1_FA3%=:\n" \
    "v_mfma_f32_16x16x32_bf16 v[104:107], a[172:175], v[164:167], v[104:107]\n" \
    "ds_read_b64_tr_b16 a[162:163], v18 offset:9216\n" \
    "v_fma_f32 v74, v74, v31, v31\n" \
    "v_mfma_f32_16x16x32_bf16 v[104:107], a[176:179], v[168:171], v[104:107]\n" \
    "v_fma_f32 v75, v75, v31, v31\n" \
    "v_rcp_f32 v74, v74\n" \
    "v_mfma_f32_16x16x32_bf16 v[104:107], a[180:183], v[172:175], v[104:107]\n" \
    "ds_read_b64_tr_b16 a[164:165], v18 offset:10240\n" \
    "v_mfma_f32_16x16x32_bf16 v[104:107], a[184:187], v[176:179], v[104:107]\n" \
    "ds_read_b64_tr_b16 a[166:167], v18 offset:11264\n" \
    "v_mfma_f32_16x16x32_bf16 v[104:107], a[188:191], v[180:183], v[104:107]\n" \
    "v_add_u32 v18, s52, v19\n" \
    "v_mfma_f32_16x16x32_bf16 v[108:111], a[168:171], v[232:235], v[108:111]\n" \
    "v_rcp_f32 v75, v75\n" \
    "v_mfma_f32_16x16x32_bf16 v[108:111], a[172:175], v[236:239], v[108:111]\n" \
    "s_add_u32 s51, s51, 1\n" \
    "v_mul_f32 v76, v76, v36\n" \
    "v_mfma_f32_16x16x32_bf16 v[108:111], a[176:179], v[240:243], v[108:111]\n" \
    "v_mul_f32 v77, v77, v36\n" \
    "v_mfma_f32_16x16x32_bf16 v[108:111], a[180:183], v[244:247], v[108:111]\n" \
    "v_exp_f32 v76, v76\n" \
    "v_mfma_f32_16x16x32_bf16 v[108:111], a[184:187], v[248:251], v[108:111]\n" \
    "v_exp_f32 v77, v77\n" \
    "v_mfma_f32_16x16x32_bf16 v[108:111], a[188:191], v[252:255], v[108:111]\n" \
    "v_fma_f32 v76, v76, v32, v32\n" \
    "v_fma_f32 v77, v77, v32, v32\n" \
    "v_rcp_f32 v76, v76\n" \
    "v_rcp_f32 v77, v77\n" \
    "s_sub_u32 s68, s68, 1\n" \
    "s_cmp_lg_u32 s68, 0\n" \
    "s_cbranch_scc1 K1_NST%=\n" \
    "s_waitcnt vmcnt(11)\n" \
    "K1_NST%=:\n" \
    "s_waitcnt vmcnt(17)\n" \
    "s_barrier\n" \
    "s_cmp_lg_u32 s68, 0\n" \
    "s_cbranch_scc1 K1_GU%=\n" \
    "s_mov_b64 s[98:99], %15\n" \
    "s_add_u32 s98, s99, 2048\n" \
    "s_cmp_lg_u32 %6, 0\n" \
    "s_cbranch_scc1 K1_P1%=\n" \
    "s_mov_b64 exec, 1\n" \
    "buffer_store_dwordx2 %0, off, s[40:43], s98 sc1\n" \
    "s_mov_b64 exec, -1\n" \
    "K1_P1%=:\n" \
    "s_waitcnt lgkmcnt(0)\n" \
    "v_mfma_f32_16x16x32_bf16 v[48:51], a[144:147], a[0:3], 0\n" \
    "ds_read_b64_tr_b16 a[168:169], v16 offset:12288\n" \
    "v_mfma_f32_16x16x32_bf16 v[48:51], a[148:151], a[4:7], v[48:51]\n" \
    "ds_read_b64_tr_b16 a[170:171], v16 offset:13312\n" \
    "v_mfma_f32_16x16x32_bf16 v[48:51], a[152:155], a[8:11], v[48:51]\n" \
    "s_mov_b64 s[98:99], %12\n" \
    "v_mfma_f32_16x16x32_bf16 v[48:51], a[156:159], a[12:15], v[48:51]\n" \
    "ds_read_b64_tr_b16 a[172:173], v16 offset:14336\n" \
    "v_mfma_f32_16x16x32_bf16 v[48:51], a[160:163], a[16:19], v[48:51]\n" \
    "ds_read_b64_tr_b16 a[174:175], v16 offset:15360\n" \
    "v_mfma_f32_16x16x32_bf16 v[48:51], a[164:167], a[20:23], v[48:51]\n" \
    "s_add_u32 s84, s40, s98\n" \
    "s_addc_u32 s85, s41, 0\n" \
    "v_mfma_f32_16x16x32_bf16 v[52:55], a[144:147], a[72:75], 0\n" \
    "ds_read_b64_tr_b16 a[176:177], v16 offset:16384\n" \
    "s_mov_b32 s86, 81920\n" \
    "v_mfma_f32_16x16x32_bf16 v[52:55], a[148:151], a[76:79], v[52:55]\n" \
    "ds_read_b64_tr_b16 a[178:179], v16 offset:17408\n" \
    "s_mov_b32 s87, 0x20000\n" \
    "v_mfma_f32_16x16x32_bf16 v[52:55], a[152:155], a[80:83], v[52:55]\n" \
    "s_mov_b64 s[98:99], %13\n" \
    "s_mov_b64 s[88:89], %4\n" \
    "v_mfma_f32_16x16x32_bf16 v[52:55], a[156:159], a[84:87], v[52:55]\n" \
    "ds_read_b64_tr_b16 a[180:181], v16 offset:18432\n" \
    "v_mul_f32 v78, v78, v36\n" \
    "v_mfma_f32_16x16x32_bf16 v[52:55], a[160:163], a[88:91], v[52:55]\n" \
    "ds_read_b64_tr_b16 a[182:183], v16 offset:19456\n" \
    "v_mul_f32 v79, v79, v36\n" \
    "v_mfma_f32_16x16x32_bf16 v[52:55], a[164:167], a[92:95], v[52:55]\n" \
    "s_add_u32 s88, s88, s99\n" \
    "s_addc_u32 s89, s89, 0\n" \
    "v_mfma_f32_16x16x32_bf16 v[56:59], a[144:147], v[112:115], 0\n" \
    "ds_read_b64_tr_b16 a[184:185], v16 offset:20480\n" \
    "s_sub_u32 s88, s88, 2048\n" \
    "s_subb_u32 s89, s89, 0\n" \
    "v_mfma_f32_16x16x32_bf16 v[56:59], a[148:151], v[116:119], v[56:59]\n" \
    "ds_read_b64_tr_b16 a[186:187], v16 offset:21504\n" \
    "s_mul_i32 s90, s98, 368640\n" \
    "v_mfma_f32_16x16x32_bf16 v[56:59], a[152:155], v[120:123], v[56:59]\n" \
    "s_sub_u32 s90, s90, 293824\n" \
    "s_cmp_eq_u32 s98, 0\n" \
    "s_cselect_b32 s90, 0, s90\n" \
    "v_mfma_f32_16x16x32_bf16 v[56:59], a[156:159], v[124:127], v[56:59]\n" \
    "ds_read_b64_tr_b16 a[188:189], v16 offset:22528\n" \
    "s_mov_b32 s91, 0x20000\n" \
    "s_and_b32 s92, %6, 1\n" \
    "v_mfma_f32_16x16x32_bf16 v[56:59], a[160:163], v[128:131], v[56:59]\n" \
    "ds_read_b64_tr_b16 a[190:191], v16 offset:23552\n" \
    "v_exp_f32 v78, v78\n" \
    "v_mfma_f32_16x16x32_bf16 v[56:59], a[164:167], v[132:135], v[56:59]\n" \
    "s_mul_i32 s92, s92, 5120\n" \
    "s_mul_i32 s95, %6, 5120\n" \
    "s_add_u32 s95, s95, %7\n" \
    "v_mfma_f32_16x16x32_bf16 v[60:63], a[144:147], v[184:187], 0\n" \
    "v_exp_f32 v79, v79\n" \
    "v_mfma_f32_16x16x32_bf16 v[60:63], a[148:151], v[188:191], v[60:63]\n" \
    "s_lshr_b32 s93, %6, 1\n" \
    "v_fma_f32 v78, v78, v32, v32\n" \
    "v_mfma_f32_16x16x32_bf16 v[60:63], a[152:155], v[192:195], v[60:63]\n" \
    "s_lshl_b32 s93, s93, 3\n" \
    "s_add_u32 s93, s93, %6\n" \
    "v_mfma_f32_16x16x32_bf16 v[60:63], a[156:159], v[196:199], v[60:63]\n" \
    "s_mul_i32 s93, s93, 36864\n" \
    "v_fma_f32 v79, v79, v32, v32\n" \
    "v_mfma_f32_16x16x32_bf16 v[60:63], a[160:163], v[200:203], v[60:63]\n" \
    "s_add_u32 s94, s93, 18432\n" \
    "s_mul_i32 s96, %6, 6144\n" \
    "v_mfma_f32_16x16x32_bf16 v[60:63], a[164:167], v[204:207], v[60:63]\n" \
    "s_add_u32 s96, s96, %7\n" \
    "s_add_u32 s96, s96, 20480\n" \
    "s_waitcnt lgkmcnt(0)\n" \
    "v_mfma_f32_16x16x32_bf16 v[48:51], a[168:171], a[24:27], v[48:51]\n" \
    "ds_read_b64_tr_b16 a[144:145], v16 offset:24576\n" \
    "v_rcp_f32 v78, v78\n" \
    "v_mfma_f32_16x16x32_bf16 v[48:51], a[172:175], a[28:31], v[48:51]\n" \
    "ds_read_b64_tr_b16 a[146:147], v16 offset:25600\n" \
    "s_mov_b32 s97, 0\n" \
    "v_lshrrev_b32 v10, 2, v8\n" \
    "v_mfma_f32_16x16x32_bf16 v[48:51], a[176:179], a[32:35], v[48:51]\n" \
    "v_rcp_f32 v79, v79\n" \
    "v_and_b32 v10, 0x7c, v10\n" \
    "v_mfma_f32_16x16x32_bf16 v[48:51], a[180:183], a[36:39], v[48:51]\n" \
    "ds_read_b64_tr_b16 a[148:149], v16 offset:26624\n" \
    "v_add_u32 v9, 0x1000, v8\n" \
    "v_accvgpr_read_b32 v11, a232\n" \
    "v_mfma_f32_16x16x32_bf16 v[48:51], a[184:187], a[40:43], v[48:51]\n" \
    "ds_read_b64_tr_b16 a[150:151], v16 offset:27648\n" \
    "v_mul_f32 v64, v64, v96\n" \
    "v_accvgpr_read_b32 v12, a233\n" \
    "v_mfma_f32_16x16x32_bf16 v[48:51], a[188:191], a[44:47], v[48:51]\n" \
    "v_accvgpr_read_b32 v13, a234\n" \
    "v_mul_f32 v65, v65, v97\n" \
    "v_mfma_f32_16x16x32_bf16 v[52:55], a[168:171], a[96:99], v[52:55]\n" \
    "ds_read_b64_tr_b16 a[152:153], v16 offset:28672\n" \
    "v_mul_f32 v66, v66, v98\n" \
    "v_mfma_f32_16x16x32_bf16 v[52:55], a[172:175], a[100:103], v[52:55]\n" \
    "ds_read_b64_tr_b16 a[154:155], v16 offset:29696\n" \
    "v_mul_f32 v67, v67, v99\n" \
    "v_mfma_f32_16x16x32_bf16 v[52:55], a[176:179], a[104:107], v[52:55]\n" \
    "s_add_u32 m0, s95, s97\n" \
    "s_nop 0\n" \
    "buffer_load_dwordx4 v8, s[84:87], s92 offen sc1 lds\n" \
    "v_mfma_f32_16x16x32_bf16 v[52:55], a[180:183], a[108:111], v[52:55]\n" \
    "ds_read_b64_tr_b16 a[156:157], v16 offset:30720\n" \
    "v_cvt_pk_bf16_f32 v82, v64, v65\n" \
    "v_mfma_f32_16x16x32_bf16 v[52:55], a[184:187], a[112:115], v[52:55]\n" \
    "ds_read_b64_tr_b16 a[158:159], v16 offset:31744\n" \
    "v_cvt_pk_bf16_f32 v83, v66, v67\n" \
    "v_mfma_f32_16x16x32_bf16 v[52:55], a[188:191], a[116:119], v[52:55]\n" \
    "buffer_load_dwordx4 v8, s[84:87], s92 offen offset:1024 sc1 lds\n" \
    "v_mfma_f32_16x16x32_bf16 v[56:59], a[168:171], v[136:139], v[56:59]\n" \
    "ds_read_b64_tr_b16 a[160:161], v16 offset:32768\n" \
    "v_mul_f32 v68, v68, v100\n" \
    "v_mfma_f32_16x16x32_bf16 v[56:59], a[172:175], v[140:143], v[56:59]\n" \
    "ds_read_b64_tr_b16 a[162:163], v16 offset:33792\n" \
    "v_mul_f32 v69, v69, v101\n" \
    "v_mfma_f32_16x16x32_bf16 v[56:59], a[176:179], v[144:147], v[56:59]\n" \
    "v_mul_f32 v70, v70, v102\n" \
    "v_mul_f32 v71, v71, v103\n" \
    "v_mfma_f32_16x16x32_bf16 v[56:59], a[180:183], v[148:151], v[56:59]\n" \
    "ds_read_b64_tr_b16 a[164:165], v16 offset:34816\n" \
    "v_cvt_pk_bf16_f32 v86, v68, v69\n" \
    "v_mfma_f32_16x16x32_bf16 v[56:59], a[184:187], v[152:155], v[56:59]\n" \
    "ds_read_b64_tr_b16 a[166:167], v16 offset:35840\n" \
    "v_cvt_pk_bf16_f32 v87, v70, v71\n" \
    "v_mfma_f32_16x16x32_bf16 v[56:59], a[188:191], v[156:159], v[56:59]\n" \
    "buffer_load_dwordx4 v8, s[84:87], s92 offen offset:2048 sc1 lds\n" \
    "v_mfma_f32_16x16x32_bf16 v[60:63], a[168:171], v[208:211], v[60:63]\n" \
    "buffer_load_dwordx4 v8, s[84:87], s92 offen offset:3072 sc1 lds\n" \
    "v_mfma_f32_16x16x32_bf16 v[60:63], a[172:175], v[212:215], v[60:63]\n" \
    "v_mul_f32 v72, v72, v104\n" \
    "v_mul_f32 v73, v73, v105\n" \
    "v_mfma_f32_16x16x32_bf16 v[60:63], a[176:179], v[216:219], v[60:63]\n" \
    "v_mul_f32 v74, v74, v106\n" \
    "v_mfma_f32_16x16x32_bf16 v[60:63], a[180:183], v[220:223], v[60:63]\n" \
    "s_add_u32 m0, m0, 0x1000\n" \
    "s_nop 0\n" \
    "buffer_load_dwordx4 v9, s[84:87], s92 offen sc1 lds\n" \
    "s_add_u32 s92, s92, 10240\n" \
    "v_mfma_f32_16x16x32_bf16 v[60:63], a[184:187], v[224:227], v[60:63]\n" \
    "v_mul_f32 v75, v75, v107\n" \
    "v_mfma_f32_16x16x32_bf16 v[60:63], a[188:191], v[228:231], v[60:63]\n" \
    "s_waitcnt lgkmcnt(0)\n" \
    "v_mfma_f32_16x16x32_bf16 v[48:51], a[144:147], a[48:51], v[48:51]\n" \
    "ds_read_b64_tr_b16 a[168:169], v17 offset:0\n" \
    "v_cvt_pk_bf16_f32 v90, v72, v73\n" \
    "v_mfma_f32_16x16x32_bf16 v[48:51], a[148:151], a[52:55], v[48:51]\n" \
    "ds_read_b64_tr_b16 a[170:171], v17 offset:1024\n" \
    "v_mfma_f32_16x16x32_bf16 v[48:51], a[152:155], a[56:59], v[48:51]\n" \
    "v_cvt_pk_bf16_f32 v91, v74, v75\n" \
    "v_mfma_f32_16x16x32_bf16 v[48:51], a[156:159], a[60:63], v[48:51]\n" \
    "ds_read_b64_tr_b16 a[172:173], v17 offset:2048\n" \
    "v_mfma_f32_16x16x32_bf16 v[48:51], a[160:163], a[64:67], v[48:51]\n" \
    "ds_read_b64_tr_b16 a[174:175], v17 offset:3072\n" \
    "v_mul_f32 v76, v76, v108\n" \
    "v_mfma_f32_16x16x32_bf16 v[48:51], a[164:167], a[68:71], v[48:51]\n" \
    "s_add_u32 m0, s96, s97\n" \
    "s_nop 0\n" \
    "buffer_load_dwordx4 v11, s[88:91], s93 offen lds\n" \
    "v_mfma_f32_16x16x32_bf16 v[52:55], a[144:147], a[120:123], v[52:55]\n" \
    "ds_read_b64_tr_b16 a[176:177], v17 offset:4096\n" \
    "v_mul_f32 v77, v77, v109\n" \
    "v_mfma_f32_16x16x32_bf16 v[52:55], a[148:151], a[124:127], v[52:55]\n" \
    "ds_read_b64_tr_b16 a[178:179], v17 offset:5120\n" \
    "v_mfma_f32_16x16x32_bf16 v[52:55], a[152:155], a[128:131], v[52:55]\n" \
    "v_mul_f32 v78, v78, v110\n" \
    "v_mfma_f32_16x16x32_bf16 v[52:55], a[156:159], a[132:135], v[52:55]\n" \
    "ds_read_b64_tr_b16 a[180:181], v17 offset:6144\n" \
    "v_mfma_f32_16x16x32_bf16 v[52:55], a[160:163], a[136:139], v[52:55]\n" \
    "ds_read_b64_tr_b16 a[182:183], v17 offset:7168\n" \
    "v_mul_f32 v79, v79, v111\n" \
    "v_mfma_f32_16x16x32_bf16 v[52:55], a[164:167], a[140:143], v[52:55]\n" \
    "buffer_load_dwordx4 v12, s[88:91], s93 offen offset:1024 lds\n" \
    "v_mfma_f32_16x16x32_bf16 v[56:59], a[144:147], v[160:163], v[56:59]\n" \
    "ds_read_b64_tr_b16 a[184:185], v17 offset:8192\n" \
    "v_cvt_pk_bf16_f32 v94, v76, v77\n" \
    "v_mfma_f32_16x16x32_bf16 v[56:59], a[148:151], v[164:167], v[56:59]\n" \
    "ds_read_b64_tr_b16 a[186:187], v17 offset:9216\n" \
    "v_mfma_f32_16x16x32_bf16 v[56:59], a[152:155], v[168:171], v[56:59]\n" \
    "v_cvt_pk_bf16_f32 v95, v78, v79\n" \
    "v_mfma_f32_16x16x32_bf16 v[56:59], a[156:159], v[172:175], v[56:59]\n" \
    "ds_read_b64_tr_b16 a[188:189], v17 offset:10240\n" \
    "v_mfma_f32_16x16x32_bf16 v[56:59], a[160:163], v[176:179], v[56:59]\n" \
    "ds_read_b64_tr_b16 a[190:191], v17 offset:11264\n" \
    "v_mfma_f32_16x16x32_bf16 v[56:59], a[164:167], v[180:183], v[56:59]\n" \
    "buffer_store_dwordx4 v[80:83], v8, s[40:43], s83 offen sc1\n" \
    "s_add_u32 s83, s83, 147456\n" \
    "v_mfma_f32_16x16x32_bf16 v[60:63], a[144:147], v[232:235], v[60:63]\n" \
    "v_mfma_f32_16x16x32_bf16 v[60:63], a[148:151], v[236:239], v[60:63]\n" \
    "v_mfma_f32_16x16x32_bf16 v[60:63], a[152:155], v[240:243], v[60:63]\n" \
    "v_mfma_f32_16x16x32_bf16 v[60:63], a[156:159], v[244:247], v[60:63]\n" \
    "v_mfma_f32_16x16x32_bf16 v[60:63], a[160:163], v[248:251], v[60:63]\n" \
    "v_mfma_f32_16x16x32_bf16 v[60:63], a[164:167], v[252:255], v[60:63]\n" \
    "s_waitcnt lgkmcnt(0)\n" \
    "v_mfma_f32_16x16x32_bf16 v[64:67], a[168:171], a[0:3], 0\n" \
    "ds_read_b64_tr_b16 a[144:145], v17 offset:12288\n" \
    "v_mfma_f32_16x16x32_bf16 v[64:67], a[172:175], a[4:7], v[64:67]\n" \
    "ds_read_b64_tr_b16 a[146:147], v17 offset:13312\n" \
    "v_mfma_f32_16x16x32_bf16 v[64:67], a[176:179], a[8:11], v[64:67]\n" \
    "buffer_load_dwordx4 v13, s[88:91], s93 offen offset:2048 lds\n" \
    "s_add_u32 s93, s93, 737280\n" \
    "v_mfma_f32_16x16x32_bf16 v[64:67], a[180:183], a[12:15], v[64:67]\n" \
    "ds_read_b64_tr_b16 a[148:149], v17 offset:14336\n" \
    "v_mfma_f32_16x16x32_bf16 v[64:67], a[184:187], a[16:19], v[64:67]\n" \
    "ds_read_b64_tr_b16 a[150:151], v17 offset:15360\n" \
    "v_mfma_f32_16x16x32_bf16 v[64:67], a[188:191], a[20:23], v[64:67]\n" \
    "s_add_u32 m0, m0, 0xc00\n" \
    "s_nop 0\n" \
    "buffer_load_dwordx4 v11, s[88:91], s94 offen lds\n" \
    "v_mfma_f32_16x16x32_bf16 v[68:71], a[168:171], a[72:75], 0\n" \
    "ds_read_b64_tr_b16 a[152:153], v17 offset:16384\n" \
    "v_mul_f32 v48, v48, v33\n" \
    "v_mfma_f32_16x16x32_bf16 v[68:71], a[172:175], a[76:79], v[68:71]\n" \
    "ds_read_b64_tr_b16 a[154:155], v17 offset:17408\n" \
    "v_mul_f32 v49, v49, v33\n" \
    "v_mfma_f32_16x16x32_bf16 v[68:71], a[176:179], a[80:83], v[68:71]\n" \
    "v_exp_f32 v48, v48\n" \
    "v_mfma_f32_16x16x32_bf16 v[68:71], a[180:183], a[84:87], v[68:71]\n" \
    "ds_read_b64_tr_b16 a[156:157], v17 offset:18432\n" \
    "v_mfma_f32_16x16x32_bf16 v[68:71], a[184:187], a[88:91], v[68:71]\n" \
    "ds_read_b64_tr_b16 a[158:159], v17 offset:19456\n" \
    "v_mfma_f32_16x16x32_bf16 v[68:71], a[188:191], a[92:95], v[68:71]\n" \
    "v_exp_f32 v49, v49\n" \
    "v_mfma_f32_16x16x32_bf16 v[72:75], a[168:171], v[112:115], 0\n" \
    "ds_read_b64_tr_b16 a[160:161], v17 offset:20480\n" \
    "v_fma_f32 v48, v48, v29, v29\n" \
    "v_mfma_f32_16x16x32_bf16 v[72:75], a[172:175], v[116:119], v[72:75]\n" \
    "ds_read_b64_tr_b16 a[162:163], v17 offset:21504\n" \
    "v_fma_f32 v49, v49, v29, v29\n" \
    "v_mfma_f32_16x16x32_bf16 v[72:75], a[176:179], v[120:123], v[72:75]\n" \
    "v_rcp_f32 v48, v48\n" \
    "v_mfma_f32_16x16x32_bf16 v[72:75], a[180:183], v[124:127], v[72:75]\n" \
    "ds_read_b64_tr_b16 a[164:165], v17 offset:22528\n" \
    "v_mfma_f32_16x16x32_bf16 v[72:75], a[184:187], v[128:131], v[72:75]\n" \
    "ds_read_b64_tr_b16 a[166:167], v17 offset:23552\n" \
    "v_mfma_f32_16x16x32_bf16 v[72:75], a[188:191], v[132:135], v[72:75]\n" \
    "buffer_store_dwordx4 v[84:87], v8, s[40:43], s83 offen sc1\n" \
    "s_add_u32 s83, s83, 147456\n" \
    "v_mfma_f32_16x16x32_bf16 v[76:79], a[168:171], v[184:187], 0\n" \
    "buffer_load_dwordx4 v12, s[88:91], s94 offen offset:1024 lds\n" \
    "v_mfma_f32_16x16x32_bf16 v[76:79], a[172:175], v[188:191], v[76:79]\n" \
    "v_rcp_f32 v49, v49\n" \
    "v_mfma_f32_16x16x32_bf16 v[76:79], a[176:179], v[192:195], v[76:79]\n" \
    "v_mul_f32 v50, v50, v33\n" \
    "v_mul_f32 v51, v51, v33\n" \
    "v_mfma_f32_16x16x32_bf16 v[76:79], a[180:183], v[196:199], v[76:79]\n" \
    "v_exp_f32 v50, v50\n" \
    "v_mfma_f32_16x16x32_bf16 v[76:79], a[184:187], v[200:203], v[76:79]\n" \
    "buffer_load_dwordx4 v13, s[88:91], s94 offen offset:2048 lds\n" \
    "s_add_u32 s94, s94, 737280\n" \
    "v_mfma_f32_16x16x32_bf16 v[76:79], a[188:191], v[204:207], v[76:79]\n" \
    "s_add_u32 s97, s97, 45056\n" \
    "s_waitcnt lgkmcnt(0)\n" \
    "v_mfma_f32_16x16x32_bf16 v[64:67], a[144:147], a[24:27], v[64:67]\n" \
    "ds_read_b64_tr_b16 a[168:169], v17 offset:24576\n" \
    "v_mfma_f32_16x16x32_bf16 v[64:67], a[148:151], a[28:31], v[64:67]\n" \
    "ds_read_b64_tr_b16 a[170:171], v17 offset:25600\n" \
    "v_mfma_f32_16x16x32_bf16 v[64:67], a[152:155], a[32:35], v[64:67]\n" \
    "v_exp_f32 v51, v51\n" \
    "v_mfma_f32_16x16x32_bf16 v[64:67], a[156:159], a[36:39], v[64:67]\n" \
    "ds_read_b64_tr_b16 a[172:173], v17 offset:26624\n" \
    "v_fma_f32 v50, v50, v29, v29\n" \
    "v_mfma_f32_16x16x32_bf16 v[64:67], a[160:163], a[40:43], v[64:67]\n" \
    "ds_read_b64_tr_b16 a[174:175], v17 offset:27648\n" \
    "v_fma_f32 v51, v51, v29, v29\n" \
    "v_mfma_f32_16x16x32_bf16 v[64:67], a[164:167], a[44:47], v[64:67]\n" \
    "v_rcp_f32 v50, v50\n" \
    "v_mfma_f32_16x16x32_bf16 v[68:71], a[144:147], a[96:99], v[68:71]\n" \
    "ds_read_b64_tr_b16 a[176:177], v17 offset:28672\n" \
    "s_cmp_eq_u32 s97, 135168\n" \
    "s_cselect_b32 s97, 0, s97\n" \
    "v_mfma_f32_16x16x32_bf16 v[68:71], a[148:151], a[100:103], v[68:71]\n" \
    "ds_read_b64_tr_b16 a[178:179], v17 offset:29696\n" \
    "v_mfma_f32_16x16x32_bf16 v[68:71], a[152:155], a[104:107], v[68:71]\n" \
    "v_rcp_f32 v51, v51\n" \
    "v_mfma_f32_16x16x32_bf16 v[68:71], a[156:159], a[108:111], v[68:71]\n" \
    "ds_read_b64_tr_b16 a[180:181], v17 offset:30720\n" \
    "v_mul_f32 v52, v52, v34\n" \
    "v_mfma_f32_16x16x32_bf16 v[68:71], a[160:163], a[112:115], v[68:71]\n" \
    "ds_read_b64_tr_b16 a[182:183], v17 offset:31744\n" \
    "v_mul_f32 v53, v53, v34\n" \
    "v_mfma_f32_16x16x32_bf16 v[68:71], a[164:167], a[116:119], v[68:71]\n" \
    "v_exp_f32 v52, v52\n" \
    "v_mfma_f32_16x16x32_bf16 v[72:75], a[144:147], v[136:139], v[72:75]\n" \
    "ds_read_b64_tr_b16 a[184:185], v17 offset:32768\n" \
    "v_mfma_f32_16x16x32_bf16 v[72:75], a[148:151], v[140:143], v[72:75]\n" \
    "ds_read_b64_tr_b16 a[186:187], v17 offset:33792\n" \
    "v_mfma_f32_16x16x32_bf16 v[72:75], a[152:155], v[144:147], v[72:75]\n" \
    "v_exp_f32 v53, v53\n" \
    "v_mfma_f32_16x16x32_bf16 v[72:75], a[156:159], v[148:151], v[72:75]\n" \
    "ds_read_b64_tr_b16 a[188:189], v17 offset:34816\n" \
    "v_fma_f32 v52, v52, v30, v30\n" \
    "v_mfma_f32_16x16x32_bf16 v[72:75], a[160:163], v[152:155], v[72:75]\n" \
    "ds_read_b64_tr_b16 a[190:191], v17 offset:35840\n" \
    "v_fma_f32 v53, v53, v30, v30\n" \
    "v_mfma_f32_16x16x32_bf16 v[72:75], a[164:167], v[156:159], v[72:75]\n" \
    "buffer_store_dwordx4 v[88:91], v8, s[40:43], s83 offen sc1\n" \
    "s_add_u32 s83, s83, 147456\n" \
    "v_mfma_f32_16x16x32_bf16 v[76:79], a[144:147], v[208:211], v[76:79]\n" \
    "v_rcp_f32 v52, v52\n" \
    "v_mfma_f32_16x16x32_bf16 v[76:79], a[148:151], v[212:215], v[76:79]\n" \
    "v_rcp_f32 v53, v53\n" \
    "v_mfma_f32_16x16x32_bf16 v[76:79], a[152:155], v[216:219], v[76:79]\n" \
    "v_mul_f32 v54, v54, v34\n" \
    "v_mul_f32 v55, v55, v34\n" \
    "v_mfma_f32_16x16x32_bf16 v[76:79], a[156:159], v[220:223], v[76:79]\n" \
    "v_exp_f32 v54, v54\n" \
    "v_mfma_f32_16x16x32_bf16 v[76:79], a[160:163], v[224:227], v[76:79]\n" \
    "s_mov_b64 s[98:99], %11\n" \
    "v_mfma_f32_16x16x32_bf16 v[76:79], a[164:167], v[228:231], v[76:79]\n" \
    "v_exp_f32 v55, v55\n" \
    "s_waitcnt lgkmcnt(0)\n" \
    "v_mfma_f32_16x16x32_bf16 v[64:67], a[168:171], a[48:51], v[64:67]\n" \
    "ds_read_b64_tr_b16 a[144:145], v18 offset:0\n" \
    "s_add_u32 s52, s52, 36864\n" \
    "v_mfma_f32_16x16x32_bf16 v[64:67], a[172:175], a[52:55], v[64:67]\n" \
    "ds_read_b64_tr_b16 a[146:147], v18 offset:1024\n" \
    "v_fma_f32 v54, v54, v30, v30\n" \
    "v_mfma_f32_16x16x32_bf16 v[64:67], a[176:179], a[56:59], v[64:67]\n" \
    "v_fma_f32 v55, v55, v30, v30\n" \
    "s_cmp_eq_u32 s52, 147456\n" \
    "s_cselect_b32 s52, 0, s52\n" \
    "v_mfma_f32_16x16x32_bf16 v[64:67], a[180:183], a[60:63], v[64:67]\n" \
    "ds_read_b64_tr_b16 a[148:149], v18 offset:2048\n" \
    "buffer_load_dwordx4 a[192:195], v8, s[40:43], s98 offen sc1\n" \
    "buffer_load_dword a228, v10, s[40:43], s98 offen offset:1024 sc1\n" \
    "s_add_u32 s98, s98, 1280\n" \
    "v_mfma_f32_16x16x32_bf16 v[64:67], a[184:187], a[64:67], v[64:67]\n" \
    "ds_read_b64_tr_b16 a[150:151], v18 offset:3072\n" \
    "v_mov_b32 v16, v18\n" \
    "v_mfma_f32_16x16x32_bf16 v[64:67], a[188:191], a[68:71], v[64:67]\n" \
    "v_rcp_f32 v54, v54\n" \
    "v_mfma_f32_16x16x32_bf16 v[68:71], a[168:171], a[120:123], v[68:71]\n" \
    "ds_read_b64_tr_b16 a[152:153], v18 offset:4096\n" \
    "v_rcp_f32 v55, v55\n" \
    "v_mfma_f32_16x16x32_bf16 v[68:71], a[172:175], a[124:127], v[68:71]\n" \
    "ds_read_b64_tr_b16 a[154:155], v18 offset:5120\n" \
    "v_mul_f32 v56, v56, v35\n" \
    "v_mfma_f32_16x16x32_bf16 v[68:71], a[176:179], a[128:131], v[68:71]\n" \
    "v_mul_f32 v57, v57, v35\n" \
    "v_exp_f32 v56, v56\n" \
    "v_mfma_f32_16x16x32_bf16 v[68:71], a[180:183], a[132:135], v[68:71]\n" \
    "ds_read_b64_tr_b16 a[156:157], v18 offset:6144\n" \
    "v_xor_b32 v17, 32, v18\n" \
    "v_mfma_f32_16x16x32_bf16 v[68:71], a[184:187], a[136:139], v[68:71]\n" \
    "ds_read_b64_tr_b16 a[158:159], v18 offset:7168\n" \
    "v_mfma_f32_16x16x32_bf16 v[68:71], a[188:191], a[140:143], v[68:71]\n" \
    "v_exp_f32 v57, v57\n" \
    "v_mfma_f32_16x16x32_bf16 v[72:75], a[168:171], v[160:163], v[72:75]\n" \
    "ds_read_b64_tr_b16 a[160:161], v18 offset:8192\n" \
    "v_fma_f32 v56, v56, v31, v31\n" \
    "v_mfma_f32_16x16x32_bf16 v[72:75], a[172:175], v[164:167], v[72:75]\n" \
    "ds_read_b64_tr_b16 a[162:163], v18 offset:9216\n" \
    "v_fma_f32 v57, v57, v31, v31\n" \
    "v_mfma_f32_16x16x32_bf16 v[72:75], a[176:179], v[168:171], v[72:75]\n" \
    "buffer_load_dwordx4 a[196:199], v8, s[40:43], s98 offen sc1\n" \
    "buffer_load_dword a229, v10, s[40:43], s98 offen offset:1024 sc1\n" \
    "s_add_u32 s98, s98, 1280\n" \
    "v_mfma_f32_16x16x32_bf16 v[72:75], a[180:183], v[172:175], v[72:75]\n" \
    "ds_read_b64_tr_b16 a[164:165], v18 offset:10240\n" \
    "v_rcp_f32 v56, v56\n" \
    "v_mfma_f32_16x16x32_bf16 v[72:75], a[184:187], v[176:179], v[72:75]\n" \
    "ds_read_b64_tr_b16 a[166:167], v18 offset:11264\n" \
    "v_rcp_f32 v57, v57\n" \
    "v_mfma_f32_16x16x32_bf16 v[72:75], a[188:191], v[180:183], v[72:75]\n" \
    "buffer_store_dwordx4 v[92:95], v8, s[40:43], s83 offen sc1\n" \
    "s_add_u32 s83, s83, 147456\n" \
    "v_add_u32 v18, s52, v19\n" \
    "v_mfma_f32_16x16x32_bf16 v[76:79], a[168:171], v[232:235], v[76:79]\n" \
    "v_readlane_b32 s83, v25, s51\n" \
    "v_mul_f32 v58, v58, v35\n" \
    "v_mfma_f32_16x16x32_bf16 v[76:79], a[172:175], v[236:239], v[76:79]\n" \
    "s_add_u32 s51, s51, 1\n" \
    "v_mul_f32 v59, v59, v35\n" \
    "v_mfma_f32_16x16x32_bf16 v[76:79], a[176:179], v[240:243], v[76:79]\n" \
    "buffer_load_dwordx4 a[200:203], v8, s[40:43], s98 offen sc1\n" \
    "buffer_load_dword a230, v10, s[40:43], s98 offen offset:1024 sc1\n" \
    "s_add_u32 s98, s98, 1280\n" \
    "v_mfma_f32_16x16x32_bf16 v[76:79], a[180:183], v[244:247], v[76:79]\n" \
    "v_exp_f32 v58, v58\n" \
    "v_mfma_f32_16x16x32_bf16 v[76:79], a[184:187], v[248:251], v[76:79]\n" \
    "v_exp_f32 v59, v59\n" \
    "v_mfma_f32_16x16x32_bf16 v[76:79], a[188:191], v[252:255], v[76:79]\n" \
    "v_fma_f32 v58, v58, v31, v31\n" \
    "v_fma_f32 v59, v59, v31, v31\n" \
    "v_rcp_f32 v58, v58\n" \
    "v_rcp_f32 v59, v59\n" \
    "s_waitcnt vmcnt(23)\n" \
    "s_barrier\n" \
    "s_waitcnt lgkmcnt(0)\n" \
    "v_mfma_f32_16x16x32_bf16 v[80:83], a[144:147], a[0:3], 0\n" \
    "ds_read_b64_tr_b16 a[168:169], v16 offset:12288\n" \
    "v_mfma_f32_16x16x32_bf16 v[80:83], a[148:151], a[4:7], v[80:83]\n" \
    "ds_read_b64_tr_b16 a[170:171], v16 offset:13312\n" \
    "v_mfma_f32_16x16x32_bf16 v[80:83], a[152:155], a[8:11], v[80:83]\n" \
    "buffer_load_dwordx4 a[204:207], v8, s[40:43], s98 offen sc1\n" \
    "buffer_load_dword a231, v10, s[40:43], s98 offen offset:1024 sc1\n" \
    "s_add_u32 s98, s98, 1280\n" \
    "v_mfma_f32_16x16x32_bf16 v[80:83], a[156:159], a[12:15], v[80:83]\n" \
    "ds_read_b64_tr_b16 a[172:173], v16 offset:14336\n" \
    "v_mfma_f32_16x16x32_bf16 v[80:83], a[160:163], a[16:19], v[80:83]\n" \
    "ds_read_b64_tr_b16 a[174:175], v16 offset:15360\n" \
    "v_mfma_f32_16x16x32_bf16 v[80:83], a[164:167], a[20:23], v[80:83]\n" \
    "s_add_u32 m0, s95, s97\n" \
    "s_nop 0\n" \
    "buffer_load_dwordx4 v8, s[84:87], s92 offen sc1 lds\n" \
    "v_mfma_f32_16x16x32_bf16 v[84:87], a[144:147], a[72:75], 0\n" \
    "ds_read_b64_tr_b16 a[176:177], v16 offset:16384\n" \
    "v_mul_f32 v60, v60, v36\n" \
    "v_mfma_f32_16x16x32_bf16 v[84:87], a[148:151], a[76:79], v[84:87]\n" \
    "ds_read_b64_tr_b16 a[178:179], v16 offset:17408\n" \
    "v_mul_f32 v61, v61, v36\n" \
    "v_mfma_f32_16x16x32_bf16 v[84:87], a[152:155], a[80:83], v[84:87]\n" \
    "buffer_load_dwordx4 v8, s[84:87], s92 offen offset:1024 sc1 lds\n" \
    "v_mfma_f32_16x16x32_bf16 v[84:87], a[156:159], a[84:87], v[84:87]\n" \
    "ds_read_b64_tr_b16 a[180:181], v16 offset:18432\n" \
    "v_mfma_f32_16x16x32_bf16 v[84:87], a[160:163], a[88:91], v[84:87]\n" \
    "ds_read_b64_tr_b16 a[182:183], v16 offset:19456\n" \
    "v_mfma_f32_16x16x32_bf16 v[84:87], a[164:167], a[92:95], v[84:87]\n" \
    "v_exp_f32 v60, v60\n" \
    "v_mfma_f32_16x16x32_bf16 v[88:91], a[144:147], v[112:115], 0\n" \
    "ds_read_b64_tr_b16 a[184:185], v16 offset:20480\n" \
    "v_mfma_f32_16x16x32_bf16 v[88:91], a[148:151], v[116:119], v[88:91]\n" \
    "ds_read_b64_tr_b16 a[186:187], v16 offset:21504\n" \
    "v_mfma_f32_16x16x32_bf16 v[88:91], a[152:155], v[120:123], v[88:91]\n" \
    "v_exp_f32 v61, v61\n" \
    "v_mfma_f32_16x16x32_bf16 v[88:91], a[156:159], v[124:127], v[88:91]\n" \
    "ds_read_b64_tr_b16 a[188:189], v16 offset:22528\n" \
    "v_fma_f32 v60, v60, v32, v32\n" \
    "v_mfma_f32_16x16x32_bf16 v[88:91], a[160:163], v[128:131], v[88:91]\n" \
    "ds_read_b64_tr_b16 a[190:191], v16 offset:23552\n" \
    "v_fma_f32 v61, v61, v32, v32\n" \
    "v_mfma_f32_16x16x32_bf16 v[88:91], a[164:167], v[132:135], v[88:91]\n" \
    "buffer_load_dwordx4 a[216:219], v8, s[40:43], s99 offen sc1\n" \
    "buffer_load_dword a224, v10, s[40:43], s99 offen offset:2304 sc1\n" \
    "s_add_u32 s99, s99, 81920\n" \
    "v_mfma_f32_16x16x32_bf16 v[92:95], a[144:147], v[184:187], 0\n" \
    "buffer_load_dwordx4 v8, s[84:87], s92 offen offset:2048 sc1 lds\n" \
    "v_mfma_f32_16x16x32_bf16 v[92:95], a[148:151], v[188:191], v[92:95]\n" \
    "v_rcp_f32 v60, v60\n" \
    "v_mfma_f32_16x16x32_bf16 v[92:95], a[152:155], v[192:195], v[92:95]\n" \
    "v_rcp_f32 v61, v61\n" \
    "v_mfma_f32_16x16x32_bf16 v[92:95], a[156:159], v[196:199], v[92:95]\n" \
    "buffer_load_dwordx4 v8, s[84:87], s92 offen offset:3072 sc1 lds\n" \
    "v_mfma_f32_16x16x32_bf16 v[92:95], a[160:163], v[200:203], v[92:95]\n" \
    "v_mul_f32 v62, v62, v36\n" \
    "v_mul_f32 v63, v63, v36\n" \
    "v_mfma_f32_16x16x32_bf16 v[92:95], a[164:167], v[204:207], v[92:95]\n" \
    "v_exp_f32 v62, v62\n" \
    "s_waitcnt lgkmcnt(0)\n" \
    "v_mfma_f32_16x16x32_bf16 v[80:83], a[168:171], a[24:27], v[80:83]\n" \
    "ds_read_b64_tr_b16 a[144:145], v16 offset:24576\n" \
    "v_exp_f32 v63, v63\n" \
    "v_mfma_f32_16x16x32_bf16 v[80:83], a[172:175], a[28:31], v[80:83]\n" \
    "ds_read_b64_tr_b16 a[146:147], v16 offset:25600\n" \
    "v_fma_f32 v62, v62, v32, v32\n" \
    "v_mfma_f32_16x16x32_bf16 v[80:83], a[176:179], a[32:35], v[80:83]\n" \
    "buffer_load_dwordx4 a[220:223], v8, s[40:43], s99 offen sc1\n" \
    "buffer_load_dword a226, v10, s[40:43], s99 offen offset:2304 sc1\n" \
    "s_add_u32 s99, s99, 81920\n" \
    "v_mfma_f32_16x16x32_bf16 v[80:83], a[180:183], a[36:39], v[80:83]\n" \
    "ds_read_b64_tr_b16 a[148:149], v16 offset:26624\n" \
    "v_fma_f32 v63, v63, v32, v32\n" \
    "v_mfma_f32_16x16x32_bf16 v[80:83], a[184:187], a[40:43], v[80:83]\n" \
    "ds_read_b64_tr_b16 a[150:151], v16 offset:27648\n" \
    "v_mfma_f32_16x16x32_bf16 v[80:83], a[188:191], a[44:47], v[80:83]\n" \
    "s_add_u32 m0, m0, 0x1000\n" \
    "s_nop 0\n" \
    "buffer_load_dwordx4 v9, s[84:87], s92 offen sc1 lds\n" \
    "s_add_u32 s92, s92, 10240\n" \
    "v_mfma_f32_16x16x32_bf16 v[84:87], a[168:171], a[96:99], v[84:87]\n" \
    "ds_read_b64_tr_b16 a[152:153], v16 offset:28672\n" \
    "s_add_u32 m0, s96, s97\n" \
    "s_nop 0\n" \
    "buffer_load_dwordx4 v11, s[88:91], s93 offen lds\n" \
    "v_mfma_f32_16x16x32_bf16 v[84:87], a[172:175], a[100:103], v[84:87]\n" \
    "ds_read_b64_tr_b16 a[154:155], v16 offset:29696\n" \
    "buffer_load_dwordx4 v12, s[88:91], s93 offen offset:1024 lds\n" \
    "v_mfma_f32_16x16x32_bf16 v[84:87], a[176:179], a[104:107], v[84:87]\n" \
    "v_rcp_f32 v62, v62\n" \
    "v_mfma_f32_16x16x32_bf16 v[84:87], a[180:183], a[108:111], v[84:87]\n" \
    "ds_read_b64_tr_b16 a[156:157], v16 offset:30720\n" \
    "v_rcp_f32 v63, v63\n" \
    "v_mfma_f32_16x16x32_bf16 v[84:87], a[184:187], a[112:115], v[84:87]\n" \
    "ds_read_b64_tr_b16 a[158:159], v16 offset:31744\n" \
    "v_mul_f32 v64, v64, v33\n" \
    "v_mfma_f32_16x16x32_bf16 v[84:87], a[188:191], a[116:119], v[84:87]\n" \
    "v_mul_f32 v65, v65, v33\n" \
    "v_mfma_f32_16x16x32_bf16 v[88:91], a[168:171], v[136:139], v[88:91]\n" \
    "ds_read_b64_tr_b16 a[160:161], v16 offset:32768\n" \
    "v_mfma_f32_16x16x32_bf16 v[88:91], a[172:175], v[140:143], v[88:91]\n" \
    "ds_read_b64_tr_b16 a[162:163], v16 offset:33792\n" \
    "v_mfma_f32_16x16x32_bf16 v[88:91], a[176:179], v[144:147], v[88:91]\n" \
    "v_exp_f32 v64, v64\n" \
    "v_mfma_f32_16x16x32_bf16 v[88:91], a[180:183], v[148:151], v[88:91]\n" \
    "ds_read_b64_tr_b16 a[164:165], v16 offset:34816\n" \
    "v_exp_f32 v65, v65\n" \
    "v_mfma_f32_16x16x32_bf16 v[88:91], a[184:187], v[152:155], v[88:91]\n" \
    "ds_read_b64_tr_b16 a[166:167], v16 offset:35840\n" \
    "v_fma_f32 v64, v64, v29, v29\n" \
    "v_mfma_f32_16x16x32_bf16 v[88:91], a[188:191], v[156:159], v[88:91]\n" \
    "v_fma_f32 v65, v65, v29, v29\n" \
    "v_mfma_f32_16x16x32_bf16 v[92:95], a[168:171], v[208:211], v[92:95]\n" \
    "v_rcp_f32 v64, v64\n" \
    "v_mfma_f32_16x16x32_bf16 v[92:95], a[172:175], v[212:215], v[92:95]\n" \
    "buffer_load_dwordx4 v13, s[88:91], s93 offen offset:2048 lds\n" \
    "s_add_u32 s93, s93, 737280\n" \
    "v_mfma_f32_16x16x32_bf16 v[92:95], a[176:179], v[216:219], v[92:95]\n" \
    "v_rcp_f32 v65, v65\n" \
    "v_mfma_f32_16x16x32_bf16 v[92:95], a[180:183], v[220:223], v[92:95]\n" \
    "v_mul_f32 v66, v66, v33\n" \
    "v_mul_f32 v67, v67, v33\n" \
    "v_mfma_f32_16x16x32_bf16 v[92:95], a[184:187], v[224:227], v[92:95]\n" \
    "s_add_u32 m0, m0, 0xc00\n" \
    "s_nop 0\n" \
    "buffer_load_dwordx4 v11, s[88:91], s94 offen lds\n" \
    "v_mfma_f32_16x16x32_bf16 v[92:95], a[188:191], v[228:231], v[92:95]\n" \
    "buffer_load_dwordx4 v12, s[88:91], s94 offen offset:1024 lds\n" \
    "s_waitcnt lgkmcnt(0)\n" \
    "v_mfma_f32_16x16x32_bf16 v[80:83], a[144:147], a[48:51], v[80:83]\n" \
    "ds_read_b64_tr_b16 a[168:169], v17 offset:0\n" \
    "v_mfma_f32_16x16x32_bf16 v[80:83], a[148:151], a[52:55], v[80:83]\n" \
    "ds_read_b64_tr_b16 a[170:171], v17 offset:1024\n" \
    "v_mfma_f32_16x16x32_bf16 v[80:83], a[152:155], a[56:59], v[80:83]\n" \
    "v_exp_f32 v66, v66\n" \
    "v_mfma_f32_16x16x32_bf16 v[80:83], a[156:159], a[60:63], v[80:83]\n" \
    "ds_read_b64_tr_b16 a[172:173], v17 offset:2048\n" \
    "v_exp_f32 v67, v67\n" \
    "v_mfma_f32_16x16x32_bf16 v[80:83], a[160:163], a[64:67], v[80:83]\n" \
    "ds_read_b64_tr_b16 a[174:175], v17 offset:3072\n" \
    "v_fma_f32 v66, v66, v29, v29\n" \
    "v_mfma_f32_16x16x32_bf16 v[80:83], a[164:167], a[68:71], v[80:83]\n" \
    "v_fma_f32 v67, v67, v29, v29\n" \
    "v_mfma_f32_16x16x32_bf16 v[84:87], a[144:147], a[120:123], v[84:87]\n" \
    "ds_read_b64_tr_b16 a[176:177], v17 offset:4096\n" \
    "v_mfma_f32_16x16x32_bf16 v[84:87], a[148:151], a[124:127], v[84:87]\n" \
    "ds_read_b64_tr_b16 a[178:179], v17 offset:5120\n" \
    "v_mfma_f32_16x16x32_bf16 v[84:87], a[152:155], a[128:131], v[84:87]\n" \
    "v_rcp_f32 v66, v66\n" \
    "v_mfma_f32_16x16x32_bf16 v[84:87], a[156:159], a[132:135], v[84:87]\n" \
    "ds_read_b64_tr_b16 a[180:181], v17 offset:6144\n" \
    "v_rcp_f32 v67, v67\n" \
    "v_mfma_f32_16x16x32_bf16 v[84:87], a[160:163], a[136:139], v[84:87]\n" \
    "ds_read_b64_tr_b16 a[182:183], v17 offset:7168\n" \
    "v_mul_f32 v68, v68, v34\n" \
    "v_mfma_f32_16x16x32_bf16 v[84:87], a[164:167], a[140:143], v[84:87]\n" \
    "buffer_load_dwordx4 v13, s[88:91], s94 offen offset:2048 lds\n" \
    "s_add_u32 s94, s94, 737280\n" \
    "v_mfma_f32_16x16x32_bf16 v[88:91], a[144:147], v[160:163], v[88:91]\n" \
    "ds_read_b64_tr_b16 a[184:185], v17 offset:8192\n" \
    "v_mul_f32 v69, v69, v34\n" \
    "v_mfma_f32_16x16x32_bf16 v[88:91], a[148:151], v[164:167], v[88:91]\n" \
    "ds_read_b64_tr_b16 a[186:187], v17 offset:9216\n" \
    "s_add_u32 s97, s97, 45056\n" \
    "v_mfma_f32_16x16x32_bf16 v[88:91], a[152:155], v[168:171], v[88:91]\n" \
    "v_exp_f32 v68, v68\n" \
    "v_mfma_f32_16x16x32_bf16 v[88:91], a[156:159], v[172:175], v[88:91]\n" \
    "ds_read_b64_tr_b16 a[188:189], v17 offset:10240\n" \
    "v_exp_f32 v69, v69\n" \
    "v_mfma_f32_16x16x32_bf16 v[88:91], a[160:163], v[176:179], v[88:91]\n" \
    "ds_read_b64_tr_b16 a[190:191], v17 offset:11264\n" \
    "v_fma_f32 v68, v68, v30, v30\n" \
    "v_mfma_f32_16x16x32_bf16 v[88:91], a[164:167], v[180:183], v[88:91]\n" \
    "v_fma_f32 v69, v69, v30, v30\n" \
    "v_mfma_f32_16x16x32_bf16 v[92:95], a[144:147], v[232:235], v[92:95]\n" \
    "v_rcp_f32 v68, v68\n" \
    "v_mfma_f32_16x16x32_bf16 v[92:95], a[148:151], v[236:239], v[92:95]\n" \
    "v_rcp_f32 v69, v69\n" \
    "v_mfma_f32_16x16x32_bf16 v[92:95], a[152:155], v[240:243], v[92:95]\n" \
    "s_cmp_eq_u32 s97, 135168\n" \
    "s_cselect_b32 s97, 0, s97\n" \
    "v_mfma_f32_16x16x32_bf16 v[92:95], a[156:159], v[244:247], v[92:95]\n" \
    "v_mul_f32 v70, v70, v34\n" \
    "v_mul_f32 v71, v71, v34\n" \
    "v_mfma_f32_16x16x32_bf16 v[92:95], a[160:163], v[248:251], v[92:95]\n" \
    "v_exp_f32 v70, v70\n" \
    "v_mfma_f32_16x16x32_bf16 v[92:95], a[164:167], v[252:255], v[92:95]\n" \
    "v_exp_f32 v71, v71\n" \
    "s_waitcnt lgkmcnt(0)\n" \
    "v_mfma_f32_16x16x32_bf16 v[96:99], a[168:171], a[0:3], 0\n" \
    "ds_read_b64_tr_b16 a[144:145], v17 offset:12288\n" \
    "v_mfma_f32_16x16x32_bf16 v[96:99], a[172:175], a[4:7], v[96:99]\n" \
    "ds_read_b64_tr_b16 a[146:147], v17 offset:13312\n" \
    "v_mfma_f32_16x16x32_bf16 v[96:99], a[176:179], a[8:11], v[96:99]\n" \
    "s_mov_b64 s[98:99], %12\n" \
    "s_mov_b64 s[64:65], %13\n" \
    "v_mfma_f32_16x16x32_bf16 v[96:99], a[180:183], a[12:15], v[96:99]\n" \
    "ds_read_b64_tr_b16 a[148:149], v17 offset:14336\n" \
    "v_mfma_f32_16x16x32_bf16 v[96:99], a[184:187], a[16:19], v[96:99]\n" \
    "ds_read_b64_tr_b16 a[150:151], v17 offset:15360\n" \
    "v_mfma_f32_16x16x32_bf16 v[96:99], a[188:191], a[20:23], v[96:99]\n" \
    "s_add_u32 s44, s40, s99\n" \
    "s_addc_u32 s45, s41, 0\n" \
    "v_mfma_f32_16x16x32_bf16 v[100:103], a[168:171], a[72:75], 0\n" \
    "ds_read_b64_tr_b16 a[152:153], v17 offset:16384\n" \
    "v_mul_f32 v48, v48, v80\n" \
    "v_mfma_f32_16x16x32_bf16 v[100:103], a[172:175], a[76:79], v[100:103]\n" \
    "ds_read_b64_tr_b16 a[154:155], v17 offset:17408\n" \
    "v_mul_f32 v49, v49, v81\n" \
    "v_mfma_f32_16x16x32_bf16 v[100:103], a[176:179], a[80:83], v[100:103]\n" \
    "s_mul_i32 s46, s64, 10240\n" \
    "v_mul_f32 v50, v50, v82\n" \
    "v_mfma_f32_16x16x32_bf16 v[100:103], a[180:183], a[84:87], v[100:103]\n" \
    "ds_read_b64_tr_b16 a[156:157], v17 offset:18432\n" \
    "s_sub_u32 s46, s46, 8192\n" \
    "v_mfma_f32_16x16x32_bf16 v[100:103], a[184:187], a[88:91], v[100:103]\n" \
    "ds_read_b64_tr_b16 a[158:159], v17 offset:19456\n" \
    "v_mul_f32 v51, v51, v83\n" \
    "v_mfma_f32_16x16x32_bf16 v[100:103], a[188:191], a[92:95], v[100:103]\n" \
    "s_cmp_eq_u32 s64, 0\n" \
    "s_cselect_b32 s46, 0, s46\n" \
    "v_mfma_f32_16x16x32_bf16 v[104:107], a[168:171], v[112:115], 0\n" \
    "ds_read_b64_tr_b16 a[160:161], v17 offset:20480\n" \
    "v_cvt_pk_bf16_f32 v80, v48, v49\n" \
    "v_mfma_f32_16x16x32_bf16 v[104:107], a[172:175], v[116:119], v[104:107]\n" \
    "ds_read_b64_tr_b16 a[162:163], v17 offset:21504\n" \
    "v_cvt_pk_bf16_f32 v81, v50, v51\n" \
    "v_mfma_f32_16x16x32_bf16 v[104:107], a[176:179], v[120:123], v[104:107]\n" \
    "s_mov_b32 s47, 0x20000\n" \
    "v_mul_f32 v52, v52, v84\n" \
    "v_mfma_f32_16x16x32_bf16 v[104:107], a[180:183], v[124:127], v[104:107]\n" \
    "ds_read_b64_tr_b16 a[164:165], v17 offset:22528\n" \
    "v_mul_f32 v53, v53, v85\n" \
    "v_mfma_f32_16x16x32_bf16 v[104:107], a[184:187], v[128:131], v[104:107]\n" \
    "ds_read_b64_tr_b16 a[166:167], v17 offset:23552\n" \
    "v_mul_f32 v54, v54, v86\n" \
    "v_mfma_f32_16x16x32_bf16 v[104:107], a[188:191], v[132:135], v[104:107]\n" \
    "s_add_u32 s48, s44, 147456\n" \
    "s_addc_u32 s49, s45, 0\n" \
    "v_mfma_f32_16x16x32_bf16 v[108:111], a[168:171], v[184:187], 0\n" \
    "s_mov_b32 s50, s46\n" \
    "v_mul_f32 v55, v55, v87\n" \
    "v_mfma_f32_16x16x32_bf16 v[108:111], a[172:175], v[188:191], v[108:111]\n" \
    "s_mov_b32 s51, 0x20000\n" \
    "v_cvt_pk_bf16_f32 v84, v52, v53\n" \
    "v_mfma_f32_16x16x32_bf16 v[108:111], a[176:179], v[192:195], v[108:111]\n" \
    "v_cvt_pk_bf16_f32 v85, v54, v55\n" \
    "v_mul_f32 v56, v56, v88\n" \
    "v_mfma_f32_16x16x32_bf16 v[108:111], a[180:183], v[196:199], v[108:111]\n" \
    "s_add_u32 s52, s48, 147456\n" \
    "s_addc_u32 s53, s49, 0\n" \
    "v_mfma_f32_16x16x32_bf16 v[108:111], a[184:187], v[200:203], v[108:111]\n" \
    "s_mov_b32 s54, s46\n" \
    "v_mul_f32 v57, v57, v89\n" \
    "v_mfma_f32_16x16x32_bf16 v[108:111], a[188:191], v[204:207], v[108:111]\n" \
    "s_mov_b32 s55, 0x20000\n" \
    "s_waitcnt lgkmcnt(0)\n" \
    "v_mfma_f32_16x16x32_bf16 v[96:99], a[144:147], a[24:27], v[96:99]\n" \
    "ds_read_b64_tr_b16 a[168:169], v17 offset:24576\n" \
    "v_mul_f32 v58, v58, v90\n" \
    "v_mfma_f32_16x16x32_bf16 v[96:99], a[148:151], a[28:31], v[96:99]\n" \
    "ds_read_b64_tr_b16 a[170:171], v17 offset:25600\n" \
    "v_mul_f32 v59, v59, v91\n" \
    "v_mfma_f32_16x16x32_bf16 v[96:99], a[152:155], a[32:35], v[96:99]\n" \
    "s_add_u32 s56, s52, 147456\n" \
    "s_addc_u32 s57, s53, 0\n" \
    "v_mfma_f32_16x16x32_bf16 v[96:99], a[156:159], a[36:39], v[96:99]\n" \
    "ds_read_b64_tr_b16 a[172:173], v17 offset:26624\n" \
    "v_cvt_pk_bf16_f32 v88, v56, v57\n" \
    "v_mfma_f32_16x16x32_bf16 v[96:99], a[160:163], a[40:43], v[96:99]\n" \
    "ds_read_b64_tr_b16 a[174:175], v17 offset:27648\n" \
    "s_mov_b32 s58, s46\n" \
    "v_mfma_f32_16x16x32_bf16 v[96:99], a[164:167], a[44:47], v[96:99]\n" \
    "v_cvt_pk_bf16_f32 v89, v58, v59\n" \
    "v_mul_f32 v60, v60, v92\n" \
    "v_mfma_f32_16x16x32_bf16 v[100:103], a[144:147], a[96:99], v[100:103]\n" \
    "ds_read_b64_tr_b16 a[176:177], v17 offset:28672\n" \
    "s_mov_b32 s59, 0x20000\n" \
    "v_mfma_f32_16x16x32_bf16 v[100:103], a[148:151], a[100:103], v[100:103]\n" \
    "ds_read_b64_tr_b16 a[178:179], v17 offset:29696\n" \
    "v_mul_f32 v61, v61, v93\n" \
    "v_mfma_f32_16x16x32_bf16 v[100:103], a[152:155], a[104:107], v[100:103]\n" \
    "s_mov_b32 s64, 0\n" \
    "v_mul_f32 v62, v62, v94\n" \
    "v_mfma_f32_16x16x32_bf16 v[100:103], a[156:159], a[108:111], v[100:103]\n" \
    "ds_read_b64_tr_b16 a[180:181], v17 offset:30720\n" \
    "v_mul_f32 v63, v63, v95\n" \
    "v_mfma_f32_16x16x32_bf16 v[100:103], a[160:163], a[112:115], v[100:103]\n" \
    "ds_read_b64_tr_b16 a[182:183], v17 offset:31744\n" \
    "s_mov_b64 s[98:99], %14\n" \
    "s_bfe_u32 s71, s98, 0x30010\n" \
    "s_lshl_b32 s71, s71, 8\n" \
    "s_add_u32 s71, s71, 41943296\n" \
    "v_lshlrev_b32 v39, 1, v10\n" \
    "buffer_load_dwordx2 v[40:41], v39, s[40:43], s71 offen offset:2048 sc1\n" \
    "v_mfma_f32_16x16x32_bf16 v[100:103], a[164:167], a[116:119], v[100:103]\n" \
    "s_mov_b64 s[98:99], %14\n" \
    "v_cvt_pk_bf16_f32 v92, v60, v61\n" \
    "v_mfma_f32_16x16x32_bf16 v[104:107], a[144:147], v[136:139], v[104:107]\n" \
    "ds_read_b64_tr_b16 a[184:185], v17 offset:32768\n" \
    "s_mov_b64 s[60:61], %5\n" \
    "v_mfma_f32_16x16x32_bf16 v[104:107], a[148:151], v[140:143], v[104:107]\n" \
    "ds_read_b64_tr_b16 a[186:187], v17 offset:33792\n" \
    "v_cvt_pk_bf16_f32 v93, v62, v63\n" \
    "v_mfma_f32_16x16x32_bf16 v[104:107], a[152:155], v[144:147], v[104:107]\n" \
    "s_and_b32 s69, s98, 0xffff\n" \
    "v_fma_f32 v70, v70, v30, v30\n" \
    "v_mfma_f32_16x16x32_bf16 v[104:107], a[156:159], v[148:151], v[104:107]\n" \
    "ds_read_b64_tr_b16 a[188:189], v17 offset:34816\n" \
    "v_fma_f32 v71, v71, v30, v30\n" \
    "v_mfma_f32_16x16x32_bf16 v[104:107], a[160:163], v[152:155], v[104:107]\n" \
    "ds_read_b64_tr_b16 a[190:191], v17 offset:35840\n" \
    "v_mfma_f32_16x16x32_bf16 v[104:107], a[164:167], v[156:159], v[104:107]\n" \
    "s_add_u32 s60, s60, s69\n" \
    "s_addc_u32 s61, s61, 0\n" \
    "v_mfma_f32_16x16x32_bf16 v[108:111], a[144:147], v[208:211], v[108:111]\n" \
    "v_rcp_f32 v70, v70\n" \
    "v_mfma_f32_16x16x32_bf16 v[108:111], a[148:151], v[212:215], v[108:111]\n" \
    "s_sub_u32 s60, s60, 2048\n" \
    "s_subb_u32 s61, s61, 0\n" \
    "v_mfma_f32_16x16x32_bf16 v[108:111], a[152:155], v[216:219], v[108:111]\n" \
    "v_rcp_f32 v71, v71\n" \
    "v_mfma_f32_16x16x32_bf16 v[108:111], a[156:159], v[220:223], v[108:111]\n" \
    "v_mul_f32 v72, v72, v35\n" \
    "v_mul_f32 v73, v73, v35\n" \
    "v_mfma_f32_16x16x32_bf16 v[108:111], a[160:163], v[224:227], v[108:111]\n" \
    "s_bitcmp1_b32 s98, 31\n" \
    "s_cselect_b32 s62, 664640, 0\n" \
    "v_mfma_f32_16x16x32_bf16 v[108:111], a[164:167], v[228:231], v[108:111]\n" \
    "s_mov_b32 s63, 0x20000\n" \
    "s_waitcnt lgkmcnt(0)\n" \
    "v_mfma_f32_16x16x32_bf16 v[96:99], a[168:171], a[48:51], v[96:99]\n" \
    "v_exp_f32 v72, v72\n" \
    "v_mfma_f32_16x16x32_bf16 v[96:99], a[172:175], a[52:55], v[96:99]\n" \
    "v_exp_f32 v73, v73\n" \
    "v_mfma_f32_16x16x32_bf16 v[96:99], a[176:179], a[56:59], v[96:99]\n" \
    "s_mov_b32 s68, s99\n" \
    "v_fma_f32 v72, v72, v31, v31\n" \
    "v_mfma_f32_16x16x32_bf16 v[96:99], a[180:183], a[60:63], v[96:99]\n" \
    "s_lshl_b32 s70, %6, 1\n" \
    "v_fma_f32 v73, v73, v31, v31\n" \
    "v_mfma_f32_16x16x32_bf16 v[96:99], a[184:187], a[64:67], v[96:99]\n" \
    "s_add_u32 s70, s70, 16\n" \
    "s_mul_i32 s67, s70, 18432\n" \
    "v_mfma_f32_16x16x32_bf16 v[96:99], a[188:191], a[68:71], v[96:99]\n" \
    "v_rcp_f32 v72, v72\n" \
    "v_mfma_f32_16x16x32_bf16 v[100:103], a[168:171], a[120:123], v[100:103]\n" \
    "v_rcp_f32 v73, v73\n" \
    "v_mfma_f32_16x16x32_bf16 v[100:103], a[172:175], a[124:127], v[100:103]\n" \
    "v_mul_f32 v74, v74, v35\n" \
    "s_mul_i32 s69, s70, 3072\n" \
    "v_mfma_f32_16x16x32_bf16 v[100:103], a[176:179], a[128:131], v[100:103]\n" \
    "v_mul_f32 v75, v75, v35\n" \
    "s_add_u32 s69, s69, %7\n" \
    "v_mfma_f32_16x16x32_bf16 v[100:103], a[180:183], a[132:135], v[100:103]\n" \
    "v_exp_f32 v74, v74\n" \
    "v_mfma_f32_16x16x32_bf16 v[100:103], a[184:187], a[136:139], v[100:103]\n" \
    "v_exp_f32 v75, v75\n" \
    "v_mfma_f32_16x16x32_bf16 v[100:103], a[188:191], a[140:143], v[100:103]\n" \
    "s_add_u32 s69, s69, 90112\n" \
    "v_fma_f32 v74, v74, v31, v31\n" \
    "v_mfma_f32_16x16x32_bf16 v[104:107], a[168:171], v[160:163], v[104:107]\n" \
    "v_fma_f32 v75, v75, v31, v31\n" \
    "s_mov_b32 s66, 45056\n" \
    "v_mfma_f32_16x16x32_bf16 v[104:107], a[172:175], v[164:167], v[104:107]\n" \
    "v_rcp_f32 v74, v74\n" \
    "v_mfma_f32_16x16x32_bf16 v[104:107], a[176:179], v[168:171], v[104:107]\n" \
    "v_rcp_f32 v75, v75\n" \
    "v_mfma_f32_16x16x32_bf16 v[104:107], a[180:183], v[172:175], v[104:107]\n" \
    "v_mul_f32 v76, v76, v36\n" \
    "v_mul_f32 v77, v77, v36\n" \
    "v_mfma_f32_16x16x32_bf16 v[104:107], a[184:187], v[176:179], v[104:107]\n" \
    "v_exp_f32 v76, v76\n" \
    "v_mfma_f32_16x16x32_bf16 v[104:107], a[188:191], v[180:183], v[104:107]\n" \
    "v_exp_f32 v77, v77\n" \
    "v_mfma_f32_16x16x32_bf16 v[108:111], a[168:171], v[232:235], v[108:111]\n" \
    "v_fma_f32 v76, v76, v32, v32\n" \
    "v_fma_f32 v77, v77, v32, v32\n" \
    "v_mfma_f32_16x16x32_bf16 v[108:111], a[172:175], v[236:239], v[108:111]\n" \
    "v_rcp_f32 v76, v76\n" \
    "v_mfma_f32_16x16x32_bf16 v[108:111], a[176:179], v[240:243], v[108:111]\n" \
    "v_rcp_f32 v77, v77\n" \
    "v_mfma_f32_16x16x32_bf16 v[108:111], a[180:183], v[244:247], v[108:111]\n" \
    "v_mfma_f32_16x16x32_bf16 v[108:111], a[184:187], v[248:251], v[108:111]\n" \
    "v_mfma_f32_16x16x32_bf16 v[108:111], a[188:191], v[252:255], v[108:111]\n" \
    "s_waitcnt vmcnt(0)\n" \
    "v_cmp_ne_u64 vcc, v[40:41], %0\n" \
    "s_cmp_lg_u64 vcc, 0\n" \
    "s_cbranch_scc1 F1_SPIN%=\n" \
    "F1_OK%=:\n" \
    "v_cmp_gt_u32 vcc, 0x200, v8\n" \
    "v_accvgpr_write_b32 a225, 0\n" \
    "v_accvgpr_write_b32 a227, 0\n" \
    "v_accvgpr_read_b32 v226, a228\n" \
    "v_mov_b32 v227, 0\n" \
    "v_cndmask_b32 v226, 0, v226, vcc\n" \
    "v_accvgpr_read_b32 v228, a229\n" \
    "v_mov_b32 v229, 0\n" \
    "v_cndmask_b32 v228, 0, v228, vcc\n" \
    "v_accvgpr_read_b32 v230, a230\n" \
    "v_mov_b32 v231, 0\n" \
    "v_cndmask_b32 v230, 0, v230, vcc\n" \
    "v_accvgpr_read_b32 v232, a231\n" \
    "v_mov_b32 v233, 0\n" \
    "v_cndmask_b32 v232, 0, v232, vcc\n" \
    "v_mfma_f32_32x32x8_bf16 v[112:127], a[224:225], v[226:227], 0\n" \
    "v_mul_f32 v78, v78, v36\n" \
    "v_mul_f32 v79, v79, v36\n" \
    "v_exp_f32 v78, v78\n" \
    "v_exp_f32 v79, v79\n" \
    "v_fma_f32 v78, v78, v32, v32\n" \
    "v_fma_f32 v79, v79, v32, v32\n" \
    "v_mfma_f32_32x32x8_bf16 v[128:143], a[226:227], v[226:227], 0\n" \
    "v_rcp_f32 v78, v78\n" \
    "v_rcp_f32 v79, v79\n" \
    "buffer_load_dwordx4 v[176:179], v8, s[44:47], s64 offen offset:0 sc1\n" \
    "buffer_load_dwordx4 v[180:183], v8, s[44:47], s64 offen offset:1024 sc1\n" \
    "buffer_load_dwordx4 v[184:187], v8, s[48:51], s64 offen offset:0 sc1\n" \
    "buffer_load_dwordx4 v[188:191], v8, s[48:51], s64 offen offset:1024 sc1\n" \
    "v_mfma_f32_32x32x16_bf16 v[112:127], a[216:219], a[192:195], v[112:127]\n" \
    "buffer_load_dwordx4 v[192:195], v8, s[52:55], s64 offen offset:0 sc1\n" \
    "buffer_load_dwordx4 v[196:199], v8, s[52:55], s64 offen offset:1024 sc1\n" \
    "buffer_load_dwordx4 v[200:203], v8, s[56:59], s64 offen offset:0 sc1\n" \
    "buffer_load_dwordx4 v[204:207], v8, s[56:59], s64 offen offset:1024 sc1\n" \
    "s_add_u32 s64, s64, 10240\n" \
    "v_mul_f32 v64, v64, v96\n" \
    "v_mfma_f32_32x32x16_bf16 v[128:143], a[220:223], a[192:195], v[128:143]\n" \
    "v_mul_f32 v65, v65, v97\n" \
    "v_mul_f32 v66, v66, v98\n" \
    "v_mul_f32 v67, v67, v99\n" \
    "v_cvt_pk_bf16_f32 v82, v64, v65\n" \
    "v_cvt_pk_bf16_f32 v83, v66, v67\n" \
    "buffer_store_dwordx4 v[80:83], v8, s[40:43], s83 offen sc1\n" \
    "v_mfma_f32_32x32x8_bf16 v[144:159], a[224:225], v[228:229], 0\n" \
    "s_add_u32 s83, s83, 147456\n" \
    "v_mul_f32 v68, v68, v100\n" \
    "v_mul_f32 v69, v69, v101\n" \
    "v_mul_f32 v70, v70, v102\n" \
    "v_mul_f32 v71, v71, v103\n" \
    "v_cvt_pk_bf16_f32 v86, v68, v69\n" \
    "v_mfma_f32_32x32x8_bf16 v[160:175], a[226:227], v[228:229], 0\n" \
    "v_cvt_pk_bf16_f32 v87, v70, v71\n" \
    "buffer_store_dwordx4 v[84:87], v8, s[40:43], s83 offen sc1\n" \
    "s_add_u32 s83, s83, 147456\n" \
    "v_mul_f32 v72, v72, v104\n" \
    "v_mul_f32 v73, v73, v105\n" \
    "v_mul_f32 v74, v74, v106\n" \
    "v_mfma_f32_32x32x16_bf16 v[144:159], a[216:219], a[196:199], v[144:159]\n" \
    "v_mul_f32 v75, v75, v107\n" \
    "v_cvt_pk_bf16_f32 v90, v72, v73\n" \
    "v_cvt_pk_bf16_f32 v91, v74, v75\n" \
    "buffer_store_dwordx4 v[88:91], v8, s[40:43], s83 offen sc1\n" \
    "s_add_u32 s83, s83, 147456\n" \
    "v_mul_f32 v76, v76, v108\n" \
    "v_mfma_f32_32x32x16_bf16 v[160:175], a[220:223], a[196:199], v[160:175]\n" \
    "v_mul_f32 v77, v77, v109\n" \
    "v_mul_f32 v78, v78, v110\n" \
    "v_mul_f32 v79, v79, v111\n" \
    "v_cvt_pk_bf16_f32 v94, v76, v77\n" \
    "v_cvt_pk_bf16_f32 v95, v78, v79\n" \
    "buffer_store_dwordx4 v[92:95], v8, s[40:43], s83 offen sc1\n" \
    "s_add_u32 s83, s83, 147456\n" \
    "v_accvgpr_read_b32 v217, a235\n" \
    "v_accvgpr_read_b32 v218, a236\n" \
    "v_accvgpr_read_b32 v219, a237\n" \
    "v_max3_f32 v234, v112, v113, v114\n" \
    "v_max3_f32 v234, v234, v115, v116\n" \
    "v_max3_f32 v234, v234, v117, v118\n" \
    "v_max3_f32 v234, v234, v119, v120\n" \
    "v_max3_f32 v234, v234, v121, v122\n" \
    "v_max3_f32 v234, v234, v123, v124\n" \
    "v_max3_f32 v234, v234, v125, v126\n" \
    "v_max3_f32 v234, v234, v127, v128\n" \
    "v_max3_f32 v234, v234, v129, v130\n" \
    "v_max3_f32 v234, v234, v131, v132\n" \
    "v_max3_f32 v234, v234, v133, v134\n" \
    "v_max3_f32 v234, v234, v135, v136\n" \
    "v_max3_f32 v234, v234, v137, v138\n" \
    "v_max3_f32 v234, v234, v139, v140\n" \
    "v_max3_f32 v234, v234, v141, v142\n" \
    "v_max_f32 v234, v234, v143\n" \
    "v_mov_b32 v238, v234\n" \
    "v_mfma_f32_32x32x8_bf16 v[112:127], a[224:225], v[230:231], 0\n" \
    "v_max3_f32 v235, v144, v145, v146\n" \
    "v_max3_f32 v235, v235, v147, v148\n" \
    "v_max3_f32 v235, v235, v149, v150\n" \
    "v_max3_f32 v235, v235, v151, v152\n" \
    "v_max3_f32 v235, v235, v153, v154\n" \
    "v_mfma_f32_32x32x8_bf16 v[128:143], a[226:227], v[230:231], 0\n" \
    "v_max3_f32 v235, v235, v155, v156\n" \
    "v_max3_f32 v235, v235, v157, v158\n" \
    "v_max3_f32 v235, v235, v159, v160\n" \
    "v_max3_f32 v235, v235, v161, v162\n" \
    "v_max3_f32 v235, v235, v163, v164\n" \
    "v_mfma_f32_32x32x16_bf16 v[112:127], a[216:219], a[200:203], v[112:127]\n" \
    "v_max3_f32 v235, v235, v165, v166\n" \
    "v_max3_f32 v235, v235, v167, v168\n" \
    "v_max3_f32 v235, v235, v169, v170\n" \
    "v_max3_f32 v235, v235, v171, v172\n" \
    "v_max3_f32 v235, v235, v173, v174\n" \
    "v_mfma_f32_32x32x16_bf16 v[128:143], a[220:223], a[200:203], v[128:143]\n" \
    "v_max_f32 v235, v235, v175\n" \
    "v_mov_b32 v239, v235\n" \
    "v_accvgpr_read_b32 v220, a238\n" \
    "v_mov_b32 v208, v217\n" \
    "v_mov_b32 v209, v218\n" \
    "v_mfma_f32_32x32x8_bf16 v[144:159], a[224:225], v[232:233], 0\n" \
    "v_accvgpr_write_b32 a208, v226\n" \
    "v_accvgpr_write_b32 a210, v228\n" \
    "v_accvgpr_write_b32 a212, v230\n" \
    "v_accvgpr_write_b32 a214, v232\n" \
    "v_mov_b32 v210, v219\n" \
    "v_mov_b32 v211, v220\n" \
    "v_mfma_f32_32x32x8_bf16 v[160:175], a[226:227], v[232:233], 0\n" \
    "v_xor_b32 v212, 32, v220\n" \
    "v_lshlrev_b32 v221, 1, v10\n" \
    "v_mfma_f32_32x32x16_bf16 v[144:159], a[216:219], a[204:207], v[144:159]\n" \
    "v_mfma_f32_32x32x16_bf16 v[160:175], a[220:223], a[204:207], v[160:175]\n" \
    "v_max3_f32 v236, v112, v113, v114\n" \
    "v_max3_f32 v236, v236, v115, v116\n" \
    "v_max3_f32 v236, v236, v117, v118\n" \
    "v_max3_f32 v236, v236, v119, v120\n" \
    "v_max3_f32 v236, v236, v121, v122\n" \
    "v_max3_f32 v236, v236, v123, v124\n" \
    "v_max3_f32 v236, v236, v125, v126\n" \
    "v_max3_f32 v236, v236, v127, v128\n" \
    "v_max3_f32 v236, v236, v129, v130\n" \
    "v_max3_f32 v236, v236, v131, v132\n" \
    "v_max3_f32 v236, v236, v133, v134\n" \
    "v_max3_f32 v236, v236, v135, v136\n" \
    "v_max3_f32 v236, v236, v137, v138\n" \
    "v_max3_f32 v236, v236, v139, v140\n" \
    "v_max3_f32 v236, v236, v141, v142\n" \
    "v_max_f32 v236, v236, v143\n" \
    "v_mov_b32 v240, v236\n" \
    "v_max3_f32 v237, v144, v145, v146\n" \
    "v_max3_f32 v237, v237, v147, v148\n" \
    "v_max3_f32 v237, v237, v149, v150\n" \
    "v_max3_f32 v237, v237, v151, v152\n" \
    "v_max3_f32 v237, v237, v153, v154\n" \
    "v_max3_f32 v237, v237, v155, v156\n" \
    "v_max3_f32 v237, v237, v157, v158\n" \
    "v_max3_f32 v237, v237, v159, v160\n" \
    "v_max3_f32 v237, v237, v161, v162\n" \
    "v_max3_f32 v237, v237, v163, v164\n" \
    "v_max3_f32 v237, v237, v165, v166\n" \
    "v_max3_f32 v237, v237, v167, v168\n" \
    "v_max3_f32 v237, v237, v169, v170\n" \
    "v_max3_f32 v237, v237, v171, v172\n" \
    "v_max3_f32 v237, v237, v173, v174\n" \
    "v_max_f32 v237, v237, v175\n" \
    "v_mov_b32 v241, v237\n" \
    "s_nop 1\n" \
    "v_permlane32_swap_b32 v234, v238\n" \
    "v_permlane32_swap_b32 v235, v239\n" \
    "v_permlane32_swap_b32 v236, v240\n" \
    "v_permlane32_swap_b32 v237, v241\n" \
    "s_nop 1\n" \
    "v_max_f32 v234, v234, v238\n" \
    "v_max_f32 v235, v235, v239\n" \
    "v_max_f32 v236, v236, v240\n" \
    "v_max_f32 v237, v237, v241\n" \
    "v_sub_f32 v234, 0xc2a00000, v234\n" \
    "v_sub_f32 v235, 0xc2a00000, v235\n" \
    "v_sub_f32 v236, 0xc2a00000, v236\n" \
    "v_sub_f32 v237, 0xc2a00000, v237\n" \
    "v_cvt_pk_bf16_f32 v234, v234, 0\n" \
    "v_cvt_pk_bf16_f32 v235, v235, 0\n" \
    "v_cvt_pk_bf16_f32 v236, v236, 0\n" \
    "v_cvt_pk_bf16_f32 v237, v237, 0\n" \
    "v_cndmask_b32 v234, 0, v234, vcc\n" \
    "v_cndmask_b32 v235, 0, v235, vcc\n" \
    "v_cndmask_b32 v236, 0, v236, vcc\n" \
    "v_cndmask_b32 v237, 0, v237, vcc\n" \
    "v_accvgpr_write_b32 a209, v234\n" \
    "v_accvgpr_write_b32 a211, v235\n" \
    "v_accvgpr_write_b32 a213, v236\n" \
    "v_accvgpr_write_b32 a215, v237\n" \
    "v_mov_b32 v77, 0x3f80\n" \
    "s_mov_b32 s74, 0x07060302\n" \
    "s_barrier\n" \
    "ds_read_b128 v[64:67], v208\n" \
    "ds_read_b32 v76, v209\n" \
    "ds_read_b64_tr_b16 v[80:81], v211 offset:0\n" \
    "ds_read_b64_tr_b16 v[82:83], v211 offset:3072\n" \
    "ds_read_b64_tr_b16 v[84:85], v211 offset:6144\n" \
    "ds_read_b64_tr_b16 v[86:87], v211 offset:9216\n" \
    "ds_read_b64_tr_b16 v[88:89], v212 offset:0\n" \
    "ds_read_b64_tr_b16 v[90:91], v212 offset:3072\n" \
    "ds_read_b64_tr_b16 v[92:93], v212 offset:6144\n" \
    "ds_read_b64_tr_b16 v[94:95], v212 offset:9216\n" \
    "ds_read_b64_tr_b16 v[96:97], v211 offset:64\n" \
    "ds_read_b64_tr_b16 v[98:99], v211 offset:3136\n" \
    "ds_read_b64_tr_b16 v[100:101], v211 offset:6208\n" \
    "ds_read_b64_tr_b16 v[102:103], v211 offset:9280\n" \
    "s_waitcnt lgkmcnt(12)\n" \
    "v_mfma_f32_32x32x8_bf16 v[16:31], v[76:77], a[208:209], 0\n" \
    "v_mfma_f32_32x32x16_bf16 v[16:31], v[64:67], a[192:195], v[16:31]\n" \
    "ds_read_b64_tr_b16 v[104:105], v212 offset:64\n" \
    "ds_read_b64_tr_b16 v[106:107], v212 offset:3136\n" \
    "ds_read_b64_tr_b16 v[108:109], v212 offset:6208\n" \
    "ds_read_b64_tr_b16 v[110:111], v212 offset:9280\n" \
    "ds_read_b64_tr_b16 v[112:113], v211 offset:128\n" \
    "ds_read_b64_tr_b16 v[114:115], v211 offset:3200\n" \
    "ds_read_b64_tr_b16 v[116:117], v211 offset:6272\n" \
    "ds_read_b64_tr_b16 v[118:119], v211 offset:9344\n" \
    "ds_read_b64_tr_b16 v[120:121], v212 offset:128\n" \
    "ds_read_b64_tr_b16 v[122:123], v212 offset:3200\n" \
    "ds_read_b64_tr_b16 v[124:125], v212 offset:6272\n" \
    "ds_read_b64_tr_b16 v[126:127], v212 offset:9344\n" \
    "ds_read_b64_tr_b16 v[128:129], v211 offset:12288\n" \
    "v_exp_f32 v16, v16\n" \
    "v_exp_f32 v48, v17\n" \
    "v_exp_f32 v18, v18\n" \
    "ds_read_b128 v[68:71], v210 offset:0\n" \
    "v_exp_f32 v49, v19\n" \
    "v_exp_f32 v20, v20\n" \
    "v_mov_b32_sdwa v48, v16 dst_sel:WORD_0 dst_unused:UNUSED_PRESERVE src0_sel:WORD_1\n" \
    "v_mfma_f32_32x32x8_bf16 v[32:47], v[76:77], a[210:211], 0\n" \
    "ds_read_b128 v[72:75], v210 offset:640\n" \
    "ds_read_b64_tr_b16 v[130:131], v211 offset:15360\n" \
    "v_exp_f32 v50, v21\n" \
    "v_mov_b32_sdwa v49, v18 dst_sel:WORD_0 dst_unused:UNUSED_PRESERVE src0_sel:WORD_1\n" \
    "s_add_u32 m0, s95, s97\n" \
    "v_exp_f32 v22, v22\n" \
    "v_mov_b32_sdwa v50, v20 dst_sel:WORD_0 dst_unused:UNUSED_PRESERVE src0_sel:WORD_1\n" \
    "s_waitcnt vmcnt(10) lgkmcnt(4)\n" \
    "v_mfma_f32_16x16x32_bf16 a[64:67], v[80:83], v[176:179], 0\n" \
    "v_exp_f32 v51, v23\n" \
    "v_mfma_f32_16x16x32_bf16 a[64:67], v[84:87], v[180:183], a[64:67]\n" \
    "v_exp_f32 v24, v24\n" \
    "v_mfma_f32_32x32x16_bf16 v[32:47], v[64:67], a[196:199], v[32:47]\n" \
    "buffer_load_dwordx4 v8, s[84:87], s92 offen sc1 lds\n" \
    "ds_read_b64_tr_b16 v[132:133], v211 offset:18432\n" \
    "v_exp_f32 v52, v25\n" \
    "v_mov_b32_sdwa v51, v22 dst_sel:WORD_0 dst_unused:UNUSED_PRESERVE src0_sel:WORD_1\n" \
    "v_exp_f32 v26, v26\n" \
    "v_mov_b32_sdwa v52, v24 dst_sel:WORD_0 dst_unused:UNUSED_PRESERVE src0_sel:WORD_1\n" \
    "v_mfma_f32_16x16x32_bf16 a[68:71], v[88:91], v[176:179], 0\n" \
    "v_exp_f32 v53, v27\n" \
    "v_mfma_f32_16x16x32_bf16 a[68:71], v[92:95], v[180:183], a[68:71]\n" \
    "ds_read_b64_tr_b16 v[134:135], v211 offset:21504\n" \
    "v_exp_f32 v28, v28\n" \
    "v_mfma_f32_16x16x32_bf16 a[72:75], v[96:99], v[176:179], 0\n" \
    "v_exp_f32 v54, v29\n" \
    "v_mov_b32_sdwa v53, v26 dst_sel:WORD_0 dst_unused:UNUSED_PRESERVE src0_sel:WORD_1\n" \
    "v_mfma_f32_16x16x32_bf16 a[72:75], v[100:103], v[180:183], a[72:75]\n" \
    "v_exp_f32 v30, v30\n" \
    "v_exp_f32 v55, v31\n" \
    "v_mov_b32_sdwa v54, v28 dst_sel:WORD_0 dst_unused:UNUSED_PRESERVE src0_sel:WORD_1\n" \
    "v_mov_b32_sdwa v55, v30 dst_sel:WORD_0 dst_unused:UNUSED_PRESERVE src0_sel:WORD_1\n" \
    "s_waitcnt vmcnt(9) lgkmcnt(3)\n" \
    "v_mfma_f32_32x32x16_bf16 a[0:15], v[68:71], v[48:51], 0\n" \
    "ds_read_b64_tr_b16 v[136:137], v212 offset:12288\n" \
    "v_exp_f32 v32, v32\n" \
    "v_exp_f32 v56, v33\n" \
    "v_exp_f32 v34, v34\n" \
    "v_mfma_f32_32x32x16_bf16 a[0:15], v[72:75], v[52:55], a[0:15]\n" \
    "buffer_load_dwordx4 v8, s[84:87], s92 offen offset:1024 sc1 lds\n" \
    "v_exp_f32 v57, v35\n" \
    "v_exp_f32 v36, v36\n" \
    "v_mov_b32_sdwa v56, v32 dst_sel:WORD_0 dst_unused:UNUSED_PRESERVE src0_sel:WORD_1\n" \
    "v_mfma_f32_32x32x8_bf16 v[16:31], v[76:77], a[212:213], 0\n" \
    "ds_read_b64_tr_b16 v[138:139], v212 offset:15360\n" \
    "v_exp_f32 v58, v37\n" \
    "v_mov_b32_sdwa v57, v34 dst_sel:WORD_0 dst_unused:UNUSED_PRESERVE src0_sel:WORD_1\n" \
    "v_exp_f32 v38, v38\n" \
    "v_mov_b32_sdwa v58, v36 dst_sel:WORD_0 dst_unused:UNUSED_PRESERVE src0_sel:WORD_1\n" \
    "v_mfma_f32_16x16x32_bf16 a[76:79], v[104:107], v[176:179], 0\n" \
    "v_exp_f32 v59, v39\n" \
    "v_mfma_f32_16x16x32_bf16 a[76:79], v[108:111], v[180:183], a[76:79]\n" \
    "v_exp_f32 v40, v40\n" \
    "v_mfma_f32_32x32x16_bf16 v[16:31], v[64:67], a[200:203], v[16:31]\n" \
    "buffer_load_dwordx4 v8, s[84:87], s92 offen offset:2048 sc1 lds\n" \
    "ds_read_b64_tr_b16 v[140:141], v212 offset:18432\n" \
    "v_exp_f32 v60, v41\n" \
    "v_mov_b32_sdwa v59, v38 dst_sel:WORD_0 dst_unused:UNUSED_PRESERVE src0_sel:WORD_1\n" \
    "v_exp_f32 v42, v42\n" \
    "v_mov_b32_sdwa v60, v40 dst_sel:WORD_0 dst_unused:UNUSED_PRESERVE src0_sel:WORD_1\n" \
    "v_mfma_f32_16x16x32_bf16 a[80:83], v[112:115], v[176:179], 0\n" \
    "v_exp_f32 v61, v43\n" \
    "v_mfma_f32_16x16x32_bf16 a[80:83], v[116:119], v[180:183], a[80:83]\n" \
    "ds_read_b64_tr_b16 v[142:143], v212 offset:21504\n" \
    "v_exp_f32 v44, v44\n" \
    "v_mfma_f32_16x16x32_bf16 a[84:87], v[120:123], v[176:179], 0\n" \
    "v_exp_f32 v62, v45\n" \
    "v_mov_b32_sdwa v61, v42 dst_sel:WORD_0 dst_unused:UNUSED_PRESERVE src0_sel:WORD_1\n" \
    "v_mfma_f32_16x16x32_bf16 a[84:87], v[124:127], v[180:183], a[84:87]\n" \
    "buffer_load_dwordx4 v[176:179], v8, s[44:47], s64 offen offset:0 sc1\n" \
    "buffer_load_dwordx4 v[180:183], v8, s[44:47], s64 offen offset:1024 sc1\n" \
    "v_exp_f32 v46, v46\n" \
    "v_exp_f32 v63, v47\n" \
    "v_mov_b32_sdwa v62, v44 dst_sel:WORD_0 dst_unused:UNUSED_PRESERVE src0_sel:WORD_1\n" \
    "v_mov_b32_sdwa v63, v46 dst_sel:WORD_0 dst_unused:UNUSED_PRESERVE src0_sel:WORD_1\n" \
    "v_mfma_f32_32x32x16_bf16 a[16:31], v[68:71], v[56:59], 0\n" \
    "ds_read_b64_tr_b16 v[144:145], v211 offset:12352\n" \
    "v_exp_f32 v16, v16\n" \
    "v_exp_f32 v48, v17\n" \
    "v_exp_f32 v18, v18\n" \
    "v_mfma_f32_32x32x16_bf16 a[16:31], v[72:75], v[60:63], a[16:31]\n" \
    "buffer_load_dwordx4 v8, s[84:87], s92 offen offset:3072 sc1 lds\n" \
    "v_exp_f32 v49, v19\n" \
    "v_exp_f32 v20, v20\n" \
    "v_mov_b32_sdwa v48, v16 dst_sel:WORD_0 dst_unused:UNUSED_PRESERVE src0_sel:WORD_1\n" \
    "v_mfma_f32_32x32x8_bf16 v[32:47], v[76:77], a[214:215], 0\n" \
    "ds_read_b64_tr_b16 v[146:147], v211 offset:15424\n" \
    "v_exp_f32 v50, v21\n" \
    "v_mov_b32_sdwa v49, v18 dst_sel:WORD_0 dst_unused:UNUSED_PRESERVE src0_sel:WORD_1\n" \
    "v_exp_f32 v22, v22\n" \
    "v_mov_b32_sdwa v50, v20 dst_sel:WORD_0 dst_unused:UNUSED_PRESERVE src0_sel:WORD_1\n" \
    "v_mfma_f32_16x16x32_bf16 a[88:91], v[80:83], v[184:187], 0\n" \
    "v_exp_f32 v51, v23\n" \
    "v_mfma_f32_16x16x32_bf16 a[88:91], v[84:87], v[188:191], a[88:91]\n" \
    "v_exp_f32 v24, v24\n" \
    "v_mfma_f32_32x32x16_bf16 v[32:47], v[64:67], a[204:207], v[32:47]\n" \
    "ds_read_b128 v[64:67], v208 offset:2560\n" \
    "ds_read_b32 v76, v209 offset:2560\n" \
    "ds_read_b64_tr_b16 v[148:149], v211 offset:18496\n" \
    "v_exp_f32 v52, v25\n" \
    "v_mov_b32_sdwa v51, v22 dst_sel:WORD_0 dst_unused:UNUSED_PRESERVE src0_sel:WORD_1\n" \
    "v_exp_f32 v26, v26\n" \
    "v_mov_b32_sdwa v52, v24 dst_sel:WORD_0 dst_unused:UNUSED_PRESERVE src0_sel:WORD_1\n" \
    "v_mfma_f32_16x16x32_bf16 a[92:95], v[88:91], v[184:187], 0\n" \
    "v_exp_f32 v53, v27\n" \
    "v_mfma_f32_16x16x32_bf16 a[92:95], v[92:95], v[188:191], a[92:95]\n" \
    "ds_read_b64_tr_b16 v[150:151], v211 offset:21568\n" \
    "v_exp_f32 v28, v28\n" \
    "v_mfma_f32_16x16x32_bf16 a[96:99], v[96:99], v[184:187], 0\n" \
    "v_exp_f32 v54, v29\n" \
    "v_mov_b32_sdwa v53, v26 dst_sel:WORD_0 dst_unused:UNUSED_PRESERVE src0_sel:WORD_1\n" \
    "v_mfma_f32_16x16x32_bf16 a[96:99], v[100:103], v[188:191], a[96:99]\n" \
    "v_exp_f32 v30, v30\n" \
    "v_exp_f32 v55, v31\n" \
    "v_mov_b32_sdwa v54, v28 dst_sel:WORD_0 dst_unused:UNUSED_PRESERVE src0_sel:WORD_1\n" \
    "v_mov_b32_sdwa v55, v30 dst_sel:WORD_0 dst_unused:UNUSED_PRESERVE src0_sel:WORD_1\n" \
    "v_mfma_f32_32x32x16_bf16 a[32:47], v[68:71], v[48:51], 0\n" \
    "ds_read_b64_tr_b16 v[152:153], v212 offset:12352\n" \
    "v_exp_f32 v32, v32\n" \
    "v_exp_f32 v56, v33\n" \
    "s_add_u32 m0, m0, 0x1000\n" \
    "v_exp_f32 v34, v34\n" \
    "v_mfma_f32_32x32x16_bf16 a[32:47], v[72:75], v[52:55], a[32:47]\n" \
    "buffer_load_dwordx4 v9, s[84:87], s92 offen sc1 lds\n" \
    "s_add_u32 s92, s92, 10240\n" \
    "v_exp_f32 v57, v35\n" \
    "v_exp_f32 v36, v36\n" \
    "v_mov_b32_sdwa v56, v32 dst_sel:WORD_0 dst_unused:UNUSED_PRESERVE src0_sel:WORD_1\n" \
    "s_waitcnt vmcnt(13) lgkmcnt(3)\n" \
    "v_mfma_f32_32x32x8_bf16 v[16:31], v[76:77], a[208:209], 0\n" \
    "ds_read_b64_tr_b16 v[154:155], v212 offset:15424\n" \
    "v_exp_f32 v58, v37\n" \
    "v_mov_b32_sdwa v57, v34 dst_sel:WORD_0 dst_unused:UNUSED_PRESERVE src0_sel:WORD_1\n" \
    "s_add_u32 m0, s96, s97\n" \
    "v_exp_f32 v38, v38\n" \
    "v_mov_b32_sdwa v58, v36 dst_sel:WORD_0 dst_unused:UNUSED_PRESERVE src0_sel:WORD_1\n" \
    "v_mfma_f32_16x16x32_bf16 a[100:103], v[104:107], v[184:187], 0\n" \
    "v_exp_f32 v59, v39\n" \
    "v_mfma_f32_16x16x32_bf16 a[100:103], v[108:111], v[188:191], a[100:103]\n" \
    "v_exp_f32 v40, v40\n" \
    "v_mfma_f32_32x32x16_bf16 v[16:31], v[64:67], a[192:195], v[16:31]\n" \
    "buffer_load_dwordx4 v11, s[88:91], s93 offen lds\n" \
    "v_exp_f32 v60, v41\n" \
    "v_mov_b32_sdwa v59, v38 dst_sel:WORD_0 dst_unused:UNUSED_PRESERVE src0_sel:WORD_1\n" \
    "v_exp_f32 v42, v42\n" \
    "v_mov_b32_sdwa v60, v40 dst_sel:WORD_0 dst_unused:UNUSED_PRESERVE src0_sel:WORD_1\n" \
    "v_mfma_f32_16x16x32_bf16 a[104:107], v[112:115], v[184:187], 0\n" \
    "v_exp_f32 v61, v43\n" \
    "v_mfma_f32_16x16x32_bf16 a[104:107], v[116:119], v[188:191], a[104:107]\n" \
    "ds_read_b64_tr_b16 v[156:157], v212 offset:18496\n" \
    "v_exp_f32 v44, v44\n" \
    "v_mfma_f32_16x16x32_bf16 a[108:111], v[120:123], v[184:187], 0\n" \
    "v_exp_f32 v62, v45\n" \
    "v_mov_b32_sdwa v61, v42 dst_sel:WORD_0 dst_unused:UNUSED_PRESERVE src0_sel:WORD_1\n" \
    "v_mfma_f32_16x16x32_bf16 a[108:111], v[124:127], v[188:191], a[108:111]\n" \
    "buffer_load_dwordx4 v[184:187], v8, s[48:51], s64 offen offset:0 sc1\n" \
    "buffer_load_dwordx4 v[188:191], v8, s[48:51], s64 offen offset:1024 sc1\n" \
    "v_exp_f32 v46, v46\n" \
    "v_exp_f32 v63, v47\n" \
    "v_mov_b32_sdwa v62, v44 dst_sel:WORD_0 dst_unused:UNUSED_PRESERVE src0_sel:WORD_1\n" \
    "v_mov_b32_sdwa v63, v46 dst_sel:WORD_0 dst_unused:UNUSED_PRESERVE src0_sel:WORD_1\n" \
    "v_mfma_f32_32x32x16_bf16 a[48:63], v[68:71], v[56:59], 0\n" \
    "ds_read_b64_tr_b16 v[158:159], v212 offset:21568\n" \
    "v_exp_f32 v16, v16\n" \
    "v_exp_f32 v48, v17\n" \
    "v_exp_f32 v18, v18\n" \
    "v_mfma_f32_32x32x16_bf16 a[48:63], v[72:75], v[60:63], a[48:63]\n" \
    "ds_read_b128 v[68:71], v210 offset:2560\n" \
    "v_exp_f32 v49, v19\n" \
    "v_exp_f32 v20, v20\n" \
    "v_mov_b32_sdwa v48, v16 dst_sel:WORD_0 dst_unused:UNUSED_PRESERVE src0_sel:WORD_1\n" \
    "v_mfma_f32_32x32x8_bf16 v[32:47], v[76:77], a[210:211], 0\n" \
    "ds_read_b128 v[72:75], v210 offset:3200\n" \
    "ds_read_b64_tr_b16 v[160:161], v211 offset:12416\n" \
    "v_exp_f32 v50, v21\n" \
    "v_mov_b32_sdwa v49, v18 dst_sel:WORD_0 dst_unused:UNUSED_PRESERVE src0_sel:WORD_1\n" \
    "v_exp_f32 v22, v22\n" \
    "v_mov_b32_sdwa v50, v20 dst_sel:WORD_0 dst_unused:UNUSED_PRESERVE src0_sel:WORD_1\n" \
    "v_mfma_f32_16x16x32_bf16 a[112:115], v[80:83], v[192:195], 0\n" \
    "v_exp_f32 v51, v23\n" \
    "v_mfma_f32_16x16x32_bf16 a[112:115], v[84:87], v[196:199], a[112:115]\n" \
    "v_exp_f32 v24, v24\n" \
    "v_mfma_f32_32x32x16_bf16 v[32:47], v[64:67], a[196:199], v[32:47]\n" \
    "buffer_load_dwordx4 v12, s[88:91], s93 offen offset:1024 lds\n" \
    "v_exp_f32 v52, v25\n" \
    "v_mov_b32_sdwa v51, v22 dst_sel:WORD_0 dst_unused:UNUSED_PRESERVE src0_sel:WORD_1\n" \
    "v_exp_f32 v26, v26\n" \
    "v_mov_b32_sdwa v52, v24 dst_sel:WORD_0 dst_unused:UNUSED_PRESERVE src0_sel:WORD_1\n" \
    "v_mfma_f32_16x16x32_bf16 a[116:119], v[88:91], v[192:195], 0\n" \
    "v_exp_f32 v53, v27\n" \
    "v_mfma_f32_16x16x32_bf16 a[116:119], v[92:95], v[196:199], a[116:119]\n" \
    "ds_read_b64_tr_b16 v[162:163], v211 offset:15488\n" \
    "v_exp_f32 v28, v28\n" \
    "v_mfma_f32_16x16x32_bf16 a[120:123], v[96:99], v[192:195], 0\n" \
    "v_exp_f32 v54, v29\n" \
    "v_mov_b32_sdwa v53, v26 dst_sel:WORD_0 dst_unused:UNUSED_PRESERVE src0_sel:WORD_1\n" \
    "v_mfma_f32_16x16x32_bf16 a[120:123], v[100:103], v[196:199], a[120:123]\n" \
    "v_exp_f32 v30, v30\n" \
    "v_exp_f32 v55, v31\n" \
    "v_mov_b32_sdwa v54, v28 dst_sel:WORD_0 dst_unused:UNUSED_PRESERVE src0_sel:WORD_1\n" \
    "v_mov_b32_sdwa v55, v30 dst_sel:WORD_0 dst_unused:UNUSED_PRESERVE src0_sel:WORD_1\n" \
    "s_waitcnt vmcnt(15) lgkmcnt(2)\n" \
    "v_mfma_f32_32x32x16_bf16 a[0:15], v[68:71], v[48:51], a[0:15]\n" \
    "ds_read_b64_tr_b16 v[164:165], v211 offset:18560\n" \
    "v_exp_f32 v32, v32\n" \
    "v_exp_f32 v56, v33\n" \
    "v_exp_f32 v34, v34\n" \
    "v_mfma_f32_32x32x16_bf16 a[0:15], v[72:75], v[52:55], a[0:15]\n" \
    "buffer_load_dwordx4 v13, s[88:91], s93 offen offset:2048 lds\n" \
    "s_add_u32 s93, s93, 737280\n" \
    "v_exp_f32 v57, v35\n" \
    "v_exp_f32 v36, v36\n" \
    "v_mov_b32_sdwa v56, v32 dst_sel:WORD_0 dst_unused:UNUSED_PRESERVE src0_sel:WORD_1\n" \
    "v_mfma_f32_32x32x8_bf16 v[16:31], v[76:77], a[212:213], 0\n" \
    "ds_read_b64_tr_b16 v[166:167], v211 offset:21632\n" \
    "v_exp_f32 v58, v37\n" \
    "v_mov_b32_sdwa v57, v34 dst_sel:WORD_0 dst_unused:UNUSED_PRESERVE src0_sel:WORD_1\n" \
    "s_add_u32 m0, m0, 0xc00\n" \
    "v_exp_f32 v38, v38\n" \
    "v_mov_b32_sdwa v58, v36 dst_sel:WORD_0 dst_unused:UNUSED_PRESERVE src0_sel:WORD_1\n" \
    "v_mfma_f32_16x16x32_bf16 a[124:127], v[104:107], v[192:195], 0\n" \
    "v_exp_f32 v59, v39\n" \
    "v_mfma_f32_16x16x32_bf16 a[124:127], v[108:111], v[196:199], a[124:127]\n" \
    "v_exp_f32 v40, v40\n" \
    "v_mfma_f32_32x32x16_bf16 v[16:31], v[64:67], a[200:203], v[16:31]\n" \
    "buffer_load_dwordx4 v11, s[88:91], s94 offen lds\n" \
    "v_exp_f32 v60, v41\n" \
    "v_mov_b32_sdwa v59, v38 dst_sel:WORD_0 dst_unused:UNUSED_PRESERVE src0_sel:WORD_1\n" \
    "v_exp_f32 v42, v42\n" \
    "v_mov_b32_sdwa v60, v40 dst_sel:WORD_0 dst_unused:UNUSED_PRESERVE src0_sel:WORD_1\n" \
    "v_mfma_f32_16x16x32_bf16 a[128:131], v[112:115], v[192:195], 0\n" \
    "v_exp_f32 v61, v43\n" \
    "v_mfma_f32_16x16x32_bf16 a[128:131], v[116:119], v[196:199], a[128:131]\n" \
    "ds_read_b64_tr_b16 v[168:169], v212 offset:12416\n" \
    "v_exp_f32 v44, v44\n" \
    "v_mfma_f32_16x16x32_bf16 a[132:135], v[120:123], v[192:195], 0\n" \
    "v_exp_f32 v62, v45\n" \
    "v_mov_b32_sdwa v61, v42 dst_sel:WORD_0 dst_unused:UNUSED_PRESERVE src0_sel:WORD_1\n" \
    "v_mfma_f32_16x16x32_bf16 a[132:135], v[124:127], v[196:199], a[132:135]\n" \
    "buffer_load_dwordx4 v[192:195], v8, s[52:55], s64 offen offset:0 sc1\n" \
    "buffer_load_dwordx4 v[196:199], v8, s[52:55], s64 offen offset:1024 sc1\n" \
    "v_exp_f32 v46, v46\n" \
    "v_exp_f32 v63, v47\n" \
    "v_mov_b32_sdwa v62, v44 dst_sel:WORD_0 dst_unused:UNUSED_PRESERVE src0_sel:WORD_1\n" \
    "v_mov_b32_sdwa v63, v46 dst_sel:WORD_0 dst_unused:UNUSED_PRESERVE src0_sel:WORD_1\n" \
    "v_mfma_f32_32x32x16_bf16 a[16:31], v[68:71], v[56:59], a[16:31]\n" \
    "ds_read_b64_tr_b16 v[170:171], v212 offset:15488\n" \
    "v_exp_f32 v16, v16\n" \
    "v_exp_f32 v48, v17\n" \
    "v_exp_f32 v18, v18\n" \
    "v_mfma_f32_32x32x16_bf16 a[16:31], v[72:75], v[60:63], a[16:31]\n" \
    "buffer_load_dwordx4 v12, s[88:91], s94 offen offset:1024 lds\n" \
    "v_exp_f32 v49, v19\n" \
    "v_exp_f32 v20, v20\n" \
    "v_mov_b32_sdwa v48, v16 dst_sel:WORD_0 dst_unused:UNUSED_PRESERVE src0_sel:WORD_1\n" \
    "v_mfma_f32_32x32x8_bf16 v[32:47], v[76:77], a[214:215], 0\n" \
    "ds_read_b64_tr_b16 v[172:173], v212 offset:18560\n" \
    "v_exp_f32 v50, v21\n" \
    "v_mov_b32_sdwa v49, v18 dst_sel:WORD_0 dst_unused:UNUSED_PRESERVE src0_sel:WORD_1\n" \
    "v_exp_f32 v22, v22\n" \
    "v_mov_b32_sdwa v50, v20 dst_sel:WORD_0 dst_unused:UNUSED_PRESERVE src0_sel:WORD_1\n" \
    "v_mfma_f32_16x16x32_bf16 a[136:139], v[80:83], v[200:203], 0\n" \
    "v_exp_f32 v51, v23\n" \
    "v_mfma_f32_16x16x32_bf16 a[136:139], v[84:87], v[204:207], a[136:139]\n" \
    "v_exp_f32 v24, v24\n" \
    "v_mfma_f32_32x32x16_bf16 v[32:47], v[64:67], a[204:207], v[32:47]\n" \
    "ds_read_b128 v[64:67], v208 offset:5120\n" \
    "ds_read_b32 v76, v209 offset:5120\n" \
    "v_exp_f32 v52, v25\n" \
    "v_mov_b32_sdwa v51, v22 dst_sel:WORD_0 dst_unused:UNUSED_PRESERVE src0_sel:WORD_1\n" \
    "v_exp_f32 v26, v26\n" \
    "v_mov_b32_sdwa v52, v24 dst_sel:WORD_0 dst_unused:UNUSED_PRESERVE src0_sel:WORD_1\n" \
    "v_mfma_f32_16x16x32_bf16 a[140:143], v[88:91], v[200:203], 0\n" \
    "v_exp_f32 v53, v27\n" \
    "v_mfma_f32_16x16x32_bf16 a[140:143], v[92:95], v[204:207], a[140:143]\n" \
    "ds_read_b64_tr_b16 v[174:175], v212 offset:21632\n" \
    "v_exp_f32 v28, v28\n" \
    "v_mfma_f32_16x16x32_bf16 a[144:147], v[96:99], v[200:203], 0\n" \
    "v_exp_f32 v54, v29\n" \
    "v_mov_b32_sdwa v53, v26 dst_sel:WORD_0 dst_unused:UNUSED_PRESERVE src0_sel:WORD_1\n" \
    "v_mfma_f32_16x16x32_bf16 a[144:147], v[100:103], v[204:207], a[144:147]\n" \
    "v_exp_f32 v30, v30\n" \
    "v_exp_f32 v55, v31\n" \
    "v_mov_b32_sdwa v54, v28 dst_sel:WORD_0 dst_unused:UNUSED_PRESERVE src0_sel:WORD_1\n" \
    "v_mov_b32_sdwa v55, v30 dst_sel:WORD_0 dst_unused:UNUSED_PRESERVE src0_sel:WORD_1\n" \
    "v_mfma_f32_32x32x16_bf16 a[32:47], v[68:71], v[48:51], a[32:47]\n" \
    "v_exp_f32 v32, v32\n" \
    "v_exp_f32 v56, v33\n" \
    "v_exp_f32 v34, v34\n" \
    "v_mfma_f32_32x32x16_bf16 a[32:47], v[72:75], v[52:55], a[32:47]\n" \
    "buffer_load_dwordx4 v13, s[88:91], s94 offen offset:2048 lds\n" \
    "s_add_u32 s94, s94, 737280\n" \
    "v_exp_f32 v57, v35\n" \
    "v_exp_f32 v36, v36\n" \
    "v_mov_b32_sdwa v56, v32 dst_sel:WORD_0 dst_unused:UNUSED_PRESERVE src0_sel:WORD_1\n" \
    "s_waitcnt vmcnt(12) lgkmcnt(0)\n" \
    "v_mfma_f32_32x32x8_bf16 v[16:31], v[76:77], a[208:209], 0\n" \
    "v_exp_f32 v58, v37\n" \
    "v_mov_b32_sdwa v57, v34 dst_sel:WORD_0 dst_unused:UNUSED_PRESERVE src0_sel:WORD_1\n" \
    "s_mov_b32 m0, s69\n" \
    "v_exp_f32 v38, v38\n" \
    "v_mov_b32_sdwa v58, v36 dst_sel:WORD_0 dst_unused:UNUSED_PRESERVE src0_sel:WORD_1\n" \
    "v_mfma_f32_16x16x32_bf16 a[148:151], v[104:107], v[200:203], 0\n" \
    "v_exp_f32 v59, v39\n" \
    "v_mfma_f32_16x16x32_bf16 a[148:151], v[108:111], v[204:207], a[148:151]\n" \
    "v_exp_f32 v40, v40\n" \
    "v_mfma_f32_32x32x16_bf16 v[16:31], v[64:67], a[192:195], v[16:31]\n" \
    "buffer_load_dwordx4 v11, s[60:63], s67 offen lds\n" \
    "v_exp_f32 v60, v41\n" \
    "v_mov_b32_sdwa v59, v38 dst_sel:WORD_0 dst_unused:UNUSED_PRESERVE src0_sel:WORD_1\n" \
    "v_exp_f32 v42, v42\n" \
    "v_mov_b32_sdwa v60, v40 dst_sel:WORD_0 dst_unused:UNUSED_PRESERVE src0_sel:WORD_1\n" \
    "v_mfma_f32_16x16x32_bf16 a[152:155], v[112:115], v[200:203], 0\n" \
    "v_exp_f32 v61, v43\n" \
    "v_mfma_f32_16x16x32_bf16 a[152:155], v[116:119], v[204:207], a[152:155]\n" \
    "v_exp_f32 v44, v44\n" \
    "v_mfma_f32_16x16x32_bf16 a[156:159], v[120:123], v[200:203], 0\n" \
    "v_exp_f32 v62, v45\n" \
    "v_mov_b32_sdwa v61, v42 dst_sel:WORD_0 dst_unused:UNUSED_PRESERVE src0_sel:WORD_1\n" \
    "v_mfma_f32_16x16x32_bf16 a[156:159], v[124:127], v[204:207], a[156:159]\n" \
    "buffer_load_dwordx4 v[200:203], v8, s[56:59], s64 offen offset:0 sc1\n" \
    "buffer_load_dwordx4 v[204:207], v8, s[56:59], s64 offen offset:1024 sc1\n" \
    "s_add_u32 s64, s64, 10240\n" \
    "v_exp_f32 v46, v46\n" \
    "v_exp_f32 v63, v47\n" \
    "v_mov_b32_sdwa v62, v44 dst_sel:WORD_0 dst_unused:UNUSED_PRESERVE src0_sel:WORD_1\n" \
    "v_mov_b32_sdwa v63, v46 dst_sel:WORD_0 dst_unused:UNUSED_PRESERVE src0_sel:WORD_1\n" \
    "v_add_u32 v211, s66, v220\n" \
    "v_xor_b32 v212, 32, v211\n" \
    "v_mfma_f32_32x32x16_bf16 a[48:63], v[68:71], v[56:59], a[48:63]\n" \
    "ds_read_b64_tr_b16 v[80:81], v211 offset:0\n" \
    "v_exp_f32 v16, v16\n" \
    "v_exp_f32 v48, v17\n" \
    "v_exp_f32 v18, v18\n" \
    "v_mfma_f32_32x32x16_bf16 a[48:63], v[72:75], v[60:63], a[48:63]\n" \
    "ds_read_b128 v[68:71], v210 offset:5120\n" \
    "v_exp_f32 v49, v19\n" \
    "v_exp_f32 v20, v20\n" \
    "v_mov_b32_sdwa v48, v16 dst_sel:WORD_0 dst_unused:UNUSED_PRESERVE src0_sel:WORD_1\n" \
    "v_mfma_f32_32x32x8_bf16 v[32:47], v[76:77], a[210:211], 0\n" \
    "ds_read_b128 v[72:75], v210 offset:5760\n" \
    "ds_read_b64_tr_b16 v[82:83], v211 offset:3072\n" \
    "v_exp_f32 v50, v21\n" \
    "v_mov_b32_sdwa v49, v18 dst_sel:WORD_0 dst_unused:UNUSED_PRESERVE src0_sel:WORD_1\n" \
    "v_exp_f32 v22, v22\n" \
    "v_mov_b32_sdwa v50, v20 dst_sel:WORD_0 dst_unused:UNUSED_PRESERVE src0_sel:WORD_1\n" \
    "v_mfma_f32_16x16x32_bf16 a[64:67], v[128:131], v[176:179], a[64:67]\n" \
    "v_exp_f32 v51, v23\n" \
    "v_mfma_f32_16x16x32_bf16 a[64:67], v[132:135], v[180:183], a[64:67]\n" \
    "v_exp_f32 v24, v24\n" \
    "v_mfma_f32_32x32x16_bf16 v[32:47], v[64:67], a[196:199], v[32:47]\n" \
    "buffer_load_dwordx4 v12, s[60:63], s67 offen offset:1024 lds\n" \
    "s_add_u32 s69, s69, 0xc00\n" \
    "ds_read_b64_tr_b16 v[84:85], v211 offset:6144\n" \
    "v_exp_f32 v52, v25\n" \
    "v_mov_b32_sdwa v51, v22 dst_sel:WORD_0 dst_unused:UNUSED_PRESERVE src0_sel:WORD_1\n" \
    "v_exp_f32 v26, v26\n" \
    "v_mov_b32_sdwa v52, v24 dst_sel:WORD_0 dst_unused:UNUSED_PRESERVE src0_sel:WORD_1\n" \
    "v_mfma_f32_16x16x32_bf16 a[68:71], v[136:139], v[176:179], a[68:71]\n" \
    "v_exp_f32 v53, v27\n" \
    "v_mfma_f32_16x16x32_bf16 a[68:71], v[140:143], v[180:183], a[68:71]\n" \
    "ds_read_b64_tr_b16 v[86:87], v211 offset:9216\n" \
    "v_exp_f32 v28, v28\n" \
    "v_mfma_f32_16x16x32_bf16 a[72:75], v[144:147], v[176:179], a[72:75]\n" \
    "v_exp_f32 v54, v29\n" \
    "v_mov_b32_sdwa v53, v26 dst_sel:WORD_0 dst_unused:UNUSED_PRESERVE src0_sel:WORD_1\n" \
    "v_mfma_f32_16x16x32_bf16 a[72:75], v[148:151], v[180:183], a[72:75]\n" \
    "v_exp_f32 v30, v30\n" \
    "v_exp_f32 v55, v31\n" \
    "v_mov_b32_sdwa v54, v28 dst_sel:WORD_0 dst_unused:UNUSED_PRESERVE src0_sel:WORD_1\n" \
    "v_mov_b32_sdwa v55, v30 dst_sel:WORD_0 dst_unused:UNUSED_PRESERVE src0_sel:WORD_1\n" \
    "s_waitcnt vmcnt(11) lgkmcnt(3)\n" \
    "v_mfma_f32_32x32x16_bf16 a[0:15], v[68:71], v[48:51], a[0:15]\n" \
    "ds_read_b64_tr_b16 v[88:89], v212 offset:0\n" \
    "v_exp_f32 v32, v32\n" \
    "v_exp_f32 v56, v33\n" \
    "v_exp_f32 v34, v34\n" \
    "v_mfma_f32_32x32x16_bf16 a[0:15], v[72:75], v[52:55], a[0:15]\n" \
    "buffer_load_dwordx4 v13, s[60:63], s67 offen offset:2048 lds\n" \
    "s_add_u32 s67, s67, 18432\n" \
    "v_exp_f32 v57, v35\n" \
    "v_exp_f32 v36, v36\n" \
    "v_mov_b32_sdwa v56, v32 dst_sel:WORD_0 dst_unused:UNUSED_PRESERVE src0_sel:WORD_1\n" \
    "v_mfma_f32_32x32x8_bf16 v[16:31], v[76:77], a[212:213], 0\n" \
    "ds_read_b64_tr_b16 v[90:91], v212 offset:3072\n" \
    "v_exp_f32 v58, v37\n" \
    "v_mov_b32_sdwa v57, v34 dst_sel:WORD_0 dst_unused:UNUSED_PRESERVE src0_sel:WORD_1\n" \
    "s_mov_b32 m0, s69\n" \
    "v_exp_f32 v38, v38\n" \
    "v_mov_b32_sdwa v58, v36 dst_sel:WORD_0 dst_unused:UNUSED_PRESERVE src0_sel:WORD_1\n" \
    "v_mfma_f32_16x16x32_bf16 a[76:79], v[152:155], v[176:179], a[76:79]\n" \
    "v_exp_f32 v59, v39\n" \
    "v_mfma_f32_16x16x32_bf16 a[76:79], v[156:159], v[180:183], a[76:79]\n" \
    "v_exp_f32 v40, v40\n" \
    "v_mfma_f32_32x32x16_bf16 v[16:31], v[64:67], a[200:203], v[16:31]\n" \
    "buffer_load_dwordx4 v11, s[60:63], s67 offen lds\n" \
    "ds_read_b64_tr_b16 v[92:93], v212 offset:6144\n" \
    "v_exp_f32 v60, v41\n" \
    "v_mov_b32_sdwa v59, v38 dst_sel:WORD_0 dst_unused:UNUSED_PRESERVE src0_sel:WORD_1\n" \
    "v_exp_f32 v42, v42\n" \
    "v_mov_b32_sdwa v60, v40 dst_sel:WORD_0 dst_unused:UNUSED_PRESERVE src0_sel:WORD_1\n" \
    "v_mfma_f32_16x16x32_bf16 a[80:83], v[160:163], v[176:179], a[80:83]\n" \
    "v_exp_f32 v61, v43\n" \
    "v_mfma_f32_16x16x32_bf16 a[80:83], v[164:167], v[180:183], a[80:83]\n" \
    "ds_read_b64_tr_b16 v[94:95], v212 offset:9216\n" \
    "v_exp_f32 v44, v44\n" \
    "v_mfma_f32_16x16x32_bf16 a[84:87], v[168:171], v[176:179], a[84:87]\n" \
    "v_exp_f32 v62, v45\n" \
    "v_mov_b32_sdwa v61, v42 dst_sel:WORD_0 dst_unused:UNUSED_PRESERVE src0_sel:WORD_1\n" \
    "v_mfma_f32_16x16x32_bf16 a[84:87], v[172:175], v[180:183], a[84:87]\n" \
    "buffer_load_dwordx4 v[176:179], v8, s[44:47], s64 offen offset:0 sc1\n" \
    "buffer_load_dwordx4 v[180:183], v8, s[44:47], s64 offen offset:1024 sc1\n" \
    "v_exp_f32 v46, v46\n" \
    "v_exp_f32 v63, v47\n" \
    "v_mov_b32_sdwa v62, v44 dst_sel:WORD_0 dst_unused:UNUSED_PRESERVE src0_sel:WORD_1\n" \
    "v_mov_b32_sdwa v63, v46 dst_sel:WORD_0 dst_unused:UNUSED_PRESERVE src0_sel:WORD_1\n" \
    "v_mfma_f32_32x32x16_bf16 a[16:31], v[68:71], v[56:59], a[16:31]\n" \
    "ds_read_b64_tr_b16 v[96:97], v211 offset:64\n" \
    "v_exp_f32 v16, v16\n" \
    "v_exp_f32 v48, v17\n" \
    "v_exp_f32 v18, v18\n" \
    "v_mfma_f32_32x32x16_bf16 a[16:31], v[72:75], v[60:63], a[16:31]\n" \
    "buffer_load_dwordx4 v12, s[60:63], s67 offen offset:1024 lds\n" \
    "s_add_u32 s69, s69, 0xc00\n" \
    "v_exp_f32 v49, v19\n" \
    "v_exp_f32 v20, v20\n" \
    "v_mov_b32_sdwa v48, v16 dst_sel:WORD_0 dst_unused:UNUSED_PRESERVE src0_sel:WORD_1\n" \
    "v_mfma_f32_32x32x8_bf16 v[32:47], v[76:77], a[214:215], 0\n" \
    "ds_read_b64_tr_b16 v[98:99], v211 offset:3136\n" \
    "v_exp_f32 v50, v21\n" \
    "v_mov_b32_sdwa v49, v18 dst_sel:WORD_0 dst_unused:UNUSED_PRESERVE src0_sel:WORD_1\n" \
    "v_exp_f32 v22, v22\n" \
    "v_mov_b32_sdwa v50, v20 dst_sel:WORD_0 dst_unused:UNUSED_PRESERVE src0_sel:WORD_1\n" \
    "v_mfma_f32_16x16x32_bf16 a[88:91], v[128:131], v[184:187], a[88:91]\n" \
    "v_exp_f32 v51, v23\n" \
    "v_mfma_f32_16x16x32_bf16 a[88:91], v[132:135], v[188:191], a[88:91]\n" \
    "v_exp_f32 v24, v24\n" \
    "v_mfma_f32_32x32x16_bf16 v[32:47], v[64:67], a[204:207], v[32:47]\n" \
    "ds_read_b128 v[64:67], v208 offset:7680\n" \
    "ds_read_b32 v76, v209 offset:7680\n" \
    "ds_read_b64_tr_b16 v[100:101], v211 offset:6208\n" \
    "v_exp_f32 v52, v25\n" \
    "v_mov_b32_sdwa v51, v22 dst_sel:WORD_0 dst_unused:UNUSED_PRESERVE src0_sel:WORD_1\n" \
    "v_exp_f32 v26, v26\n" \
    "v_mov_b32_sdwa v52, v24 dst_sel:WORD_0 dst_unused:UNUSED_PRESERVE src0_sel:WORD_1\n" \
    "v_mfma_f32_16x16x32_bf16 a[92:95], v[136:139], v[184:187], a[92:95]\n" \
    "v_exp_f32 v53, v27\n" \
    "v_mfma_f32_16x16x32_bf16 a[92:95], v[140:143], v[188:191], a[92:95]\n" \
    "ds_read_b64_tr_b16 v[102:103], v211 offset:9280\n" \
    "v_exp_f32 v28, v28\n" \
    "v_mfma_f32_16x16x32_bf16 a[96:99], v[144:147], v[184:187], a[96:99]\n" \
    "v_exp_f32 v54, v29\n" \
    "v_mov_b32_sdwa v53, v26 dst_sel:WORD_0 dst_unused:UNUSED_PRESERVE src0_sel:WORD_1\n" \
    "v_mfma_f32_16x16x32_bf16 a[96:99], v[148:151], v[188:191], a[96:99]\n" \
    "v_exp_f32 v30, v30\n" \
    "v_exp_f32 v55, v31\n" \
    "v_mov_b32_sdwa v54, v28 dst_sel:WORD_0 dst_unused:UNUSED_PRESERVE src0_sel:WORD_1\n" \
    "v_mov_b32_sdwa v55, v30 dst_sel:WORD_0 dst_unused:UNUSED_PRESERVE src0_sel:WORD_1\n" \
    "v_mfma_f32_32x32x16_bf16 a[32:47], v[68:71], v[48:51], a[32:47]\n" \
    "ds_read_b64_tr_b16 v[104:105], v212 offset:64\n" \
    "v_exp_f32 v32, v32\n" \
    "v_exp_f32 v56, v33\n" \
    "v_exp_f32 v34, v34\n" \
    "v_mfma_f32_32x32x16_bf16 a[32:47], v[72:75], v[52:55], a[32:47]\n" \
    "buffer_load_dwordx4 v13, s[60:63], s67 offen offset:2048 lds\n" \
    "s_add_u32 s67, s67, 18432\n" \
    "v_exp_f32 v57, v35\n" \
    "v_exp_f32 v36, v36\n" \
    "v_mov_b32_sdwa v56, v32 dst_sel:WORD_0 dst_unused:UNUSED_PRESERVE src0_sel:WORD_1\n" \
    "s_waitcnt vmcnt(12) lgkmcnt(3)\n" \
    "v_mfma_f32_32x32x8_bf16 v[16:31], v[76:77], a[208:209], 0\n" \
    "ds_read_b64_tr_b16 v[106:107], v212 offset:3136\n" \
    "v_exp_f32 v58, v37\n" \
    "v_mov_b32_sdwa v57, v34 dst_sel:WORD_0 dst_unused:UNUSED_PRESERVE src0_sel:WORD_1\n" \
    "v_exp_f32 v38, v38\n" \
    "v_mov_b32_sdwa v58, v36 dst_sel:WORD_0 dst_unused:UNUSED_PRESERVE src0_sel:WORD_1\n" \
    "v_mfma_f32_16x16x32_bf16 a[100:103], v[152:155], v[184:187], a[100:103]\n" \
    "v_exp_f32 v59, v39\n" \
    "v_mfma_f32_16x16x32_bf16 a[100:103], v[156:159], v[188:191], a[100:103]\n" \
    "v_exp_f32 v40, v40\n" \
    "v_mfma_f32_32x32x16_bf16 v[16:31], v[64:67], a[192:195], v[16:31]\n" \
    "v_exp_f32 v60, v41\n" \
    "v_mov_b32_sdwa v59, v38 dst_sel:WORD_0 dst_unused:UNUSED_PRESERVE src0_sel:WORD_1\n" \
    "v_exp_f32 v42, v42\n" \
    "v_mov_b32_sdwa v60, v40 dst_sel:WORD_0 dst_unused:UNUSED_PRESERVE src0_sel:WORD_1\n" \
    "v_mfma_f32_16x16x32_bf16 a[104:107], v[160:163], v[184:187], a[104:107]\n" \
    "v_exp_f32 v61, v43\n" \
    "v_mfma_f32_16x16x32_bf16 a[104:107], v[164:167], v[188:191], a[104:107]\n" \
    "ds_read_b64_tr_b16 v[108:109], v212 offset:6208\n" \
    "v_exp_f32 v44, v44\n" \
    "v_mfma_f32_16x16x32_bf16 a[108:111], v[168:171], v[184:187], a[108:111]\n" \
    "v_exp_f32 v62, v45\n" \
    "v_mov_b32_sdwa v61, v42 dst_sel:WORD_0 dst_unused:UNUSED_PRESERVE src0_sel:WORD_1\n" \
    "v_mfma_f32_16x16x32_bf16 a[108:111], v[172:175], v[188:191], a[108:111]\n" \
    "buffer_load_dwordx4 v[184:187], v8, s[48:51], s64 offen offset:0 sc1\n" \
    "buffer_load_dwordx4 v[188:191], v8, s[48:51], s64 offen offset:1024 sc1\n" \
    "v_exp_f32 v46, v46\n" \
    "v_exp_f32 v63, v47\n" \
    "v_mov_b32_sdwa v62, v44 dst_sel:WORD_0 dst_unused:UNUSED_PRESERVE src0_sel:WORD_1\n" \
    "v_mov_b32_sdwa v63, v46 dst_sel:WORD_0 dst_unused:UNUSED_PRESERVE src0_sel:WORD_1\n" \
    "v_mfma_f32_32x32x16_bf16 a[48:63], v[68:71], v[56:59], a[48:63]\n" \
    "ds_read_b64_tr_b16 v[110:111], v212 offset:9280\n" \
    "v_exp_f32 v16, v16\n" \
    "v_exp_f32 v48, v17\n" \
    "v_exp_f32 v18, v18\n" \
    "v_mfma_f32_32x32x16_bf16 a[48:63], v[72:75], v[60:63], a[48:63]\n" \
    "ds_read_b128 v[68:71], v210 offset:7680\n" \
    "v_exp_f32 v49, v19\n" \
    "v_exp_f32 v20, v20\n" \
    "v_mov_b32_sdwa v48, v16 dst_sel:WORD_0 dst_unused:UNUSED_PRESERVE src0_sel:WORD_1\n" \
    "v_mfma_f32_32x32x8_bf16 v[32:47], v[76:77], a[210:211], 0\n" \
    "ds_read_b128 v[72:75], v210 offset:8320\n" \
    "ds_read_b64_tr_b16 v[112:113], v211 offset:128\n" \
    "v_exp_f32 v50, v21\n" \
    "v_mov_b32_sdwa v49, v18 dst_sel:WORD_0 dst_unused:UNUSED_PRESERVE src0_sel:WORD_1\n" \
    "v_exp_f32 v22, v22\n" \
    "v_mov_b32_sdwa v50, v20 dst_sel:WORD_0 dst_unused:UNUSED_PRESERVE src0_sel:WORD_1\n" \
    "v_mfma_f32_16x16x32_bf16 a[112:115], v[128:131], v[192:195], a[112:115]\n" \
    "v_exp_f32 v51, v23\n" \
    "v_mfma_f32_16x16x32_bf16 a[112:115], v[132:135], v[196:199], a[112:115]\n" \
    "v_exp_f32 v24, v24\n" \
    "v_mfma_f32_32x32x16_bf16 v[32:47], v[64:67], a[196:199], v[32:47]\n" \
    "v_exp_f32 v52, v25\n" \
    "v_mov_b32_sdwa v51, v22 dst_sel:WORD_0 dst_unused:UNUSED_PRESERVE src0_sel:WORD_1\n" \
    "v_exp_f32 v26, v26\n" \
    "v_mov_b32_sdwa v52, v24 dst_sel:WORD_0 dst_unused:UNUSED_PRESERVE src0_sel:WORD_1\n" \
    "v_mfma_f32_16x16x32_bf16 a[116:119], v[136:139], v[192:195], a[116:119]\n" \
    "v_exp_f32 v53, v27\n" \
    "v_mfma_f32_16x16x32_bf16 a[116:119], v[140:143], v[196:199], a[116:119]\n" \
    "ds_read_b64_tr_b16 v[114:115], v211 offset:3200\n" \
    "v_exp_f32 v28, v28\n" \
    "v_mfma_f32_16x16x32_bf16 a[120:123], v[144:147], v[192:195], a[120:123]\n" \
    "v_exp_f32 v54, v29\n" \
    "v_mov_b32_sdwa v53, v26 dst_sel:WORD_0 dst_unused:UNUSED_PRESERVE src0_sel:WORD_1\n" \
    "v_mfma_f32_16x16x32_bf16 a[120:123], v[148:151], v[196:199], a[120:123]\n" \
    "v_exp_f32 v30, v30\n" \
    "v_exp_f32 v55, v31\n" \
    "v_mov_b32_sdwa v54, v28 dst_sel:WORD_0 dst_unused:UNUSED_PRESERVE src0_sel:WORD_1\n" \
    "v_mov_b32_sdwa v55, v30 dst_sel:WORD_0 dst_unused:UNUSED_PRESERVE src0_sel:WORD_1\n" \
    "s_waitcnt vmcnt(9) lgkmcnt(2)\n" \
    "v_mfma_f32_32x32x16_bf16 a[0:15], v[68:71], v[48:51], a[0:15]\n" \
    "ds_read_b64_tr_b16 v[116:117], v211 offset:6272\n" \
    "v_exp_f32 v32, v32\n" \
    "v_exp_f32 v56, v33\n" \
    "v_exp_f32 v34, v34\n" \
    "v_mfma_f32_32x32x16_bf16 a[0:15], v[72:75], v[52:55], a[0:15]\n" \
    "v_exp_f32 v57, v35\n" \
    "v_exp_f32 v36, v36\n" \
    "v_mov_b32_sdwa v56, v32 dst_sel:WORD_0 dst_unused:UNUSED_PRESERVE src0_sel:WORD_1\n" \
    "v_mfma_f32_32x32x8_bf16 v[16:31], v[76:77], a[212:213], 0\n" \
    "ds_read_b64_tr_b16 v[118:119], v211 offset:9344\n" \
    "v_exp_f32 v58, v37\n" \
    "v_mov_b32_sdwa v57, v34 dst_sel:WORD_0 dst_unused:UNUSED_PRESERVE src0_sel:WORD_1\n" \
    "v_add_u32 v210, s66, v219\n" \
    "v_exp_f32 v38, v38\n" \
    "v_mov_b32_sdwa v58, v36 dst_sel:WORD_0 dst_unused:UNUSED_PRESERVE src0_sel:WORD_1\n" \
    "v_mfma_f32_16x16x32_bf16 a[124:127], v[152:155], v[192:195], a[124:127]\n" \
    "v_exp_f32 v59, v39\n" \
    "v_mfma_f32_16x16x32_bf16 a[124:127], v[156:159], v[196:199], a[124:127]\n" \
    "v_exp_f32 v40, v40\n" \
    "v_mfma_f32_32x32x16_bf16 v[16:31], v[64:67], a[200:203], v[16:31]\n" \
    "v_exp_f32 v60, v41\n" \
    "v_mov_b32_sdwa v59, v38 dst_sel:WORD_0 dst_unused:UNUSED_PRESERVE src0_sel:WORD_1\n" \
    "v_exp_f32 v42, v42\n" \
    "v_mov_b32_sdwa v60, v40 dst_sel:WORD_0 dst_unused:UNUSED_PRESERVE src0_sel:WORD_1\n" \
    "v_mfma_f32_16x16x32_bf16 a[128:131], v[160:163], v[192:195], a[128:131]\n" \
    "v_exp_f32 v61, v43\n" \
    "v_mfma_f32_16x16x32_bf16 a[128:131], v[164:167], v[196:199], a[128:131]\n" \
    "ds_read_b64_tr_b16 v[120:121], v212 offset:128\n" \
    "v_exp_f32 v44, v44\n" \
    "v_mfma_f32_16x16x32_bf16 a[132:135], v[168:171], v[192:195], a[132:135]\n" \
    "v_exp_f32 v62, v45\n" \
    "v_mov_b32_sdwa v61, v42 dst_sel:WORD_0 dst_unused:UNUSED_PRESERVE src0_sel:WORD_1\n" \
    "v_mfma_f32_16x16x32_bf16 a[132:135], v[172:175], v[196:199], a[132:135]\n" \
    "buffer_load_dwordx4 v[192:195], v8, s[52:55], s64 offen offset:0 sc1\n" \
    "buffer_load_dwordx4 v[196:199], v8, s[52:55], s64 offen offset:1024 sc1\n" \
    "v_exp_f32 v46, v46\n" \
    "v_exp_f32 v63, v47\n" \
    "v_mov_b32_sdwa v62, v44 dst_sel:WORD_0 dst_unused:UNUSED_PRESERVE src0_sel:WORD_1\n" \
    "v_mov_b32_sdwa v63, v46 dst_sel:WORD_0 dst_unused:UNUSED_PRESERVE src0_sel:WORD_1\n" \
    "v_mfma_f32_32x32x16_bf16 a[16:31], v[68:71], v[56:59], a[16:31]\n" \
    "ds_read_b64_tr_b16 v[122:123], v212 offset:3200\n" \
    "v_exp_f32 v16, v16\n" \
    "v_exp_f32 v48, v17\n" \
    "v_exp_f32 v18, v18\n" \
    "v_mfma_f32_32x32x16_bf16 a[16:31], v[72:75], v[60:63], a[16:31]\n" \
    "v_exp_f32 v49, v19\n" \
    "v_exp_f32 v20, v20\n" \
    "v_mov_b32_sdwa v48, v16 dst_sel:WORD_0 dst_unused:UNUSED_PRESERVE src0_sel:WORD_1\n" \
    "v_mfma_f32_32x32x8_bf16 v[32:47], v[76:77], a[214:215], 0\n" \
    "ds_read_b64_tr_b16 v[124:125], v212 offset:6272\n" \
    "v_exp_f32 v50, v21\n" \
    "v_mov_b32_sdwa v49, v18 dst_sel:WORD_0 dst_unused:UNUSED_PRESERVE src0_sel:WORD_1\n" \
    "v_add_u32 v208, s66, v217\n" \
    "v_add_u32 v209, s66, v218\n" \
    "v_exp_f32 v22, v22\n" \
    "v_mov_b32_sdwa v50, v20 dst_sel:WORD_0 dst_unused:UNUSED_PRESERVE src0_sel:WORD_1\n" \
    "v_mfma_f32_16x16x32_bf16 a[136:139], v[128:131], v[200:203], a[136:139]\n" \
    "v_exp_f32 v51, v23\n" \
    "v_mfma_f32_16x16x32_bf16 a[136:139], v[132:135], v[204:207], a[136:139]\n" \
    "v_exp_f32 v24, v24\n" \
    "v_mfma_f32_32x32x16_bf16 v[32:47], v[64:67], a[204:207], v[32:47]\n" \
    "ds_read_b128 v[64:67], v208 offset:0\n" \
    "ds_read_b32 v76, v209 offset:0\n" \
    "v_exp_f32 v52, v25\n" \
    "v_mov_b32_sdwa v51, v22 dst_sel:WORD_0 dst_unused:UNUSED_PRESERVE src0_sel:WORD_1\n" \
    "v_exp_f32 v26, v26\n" \
    "v_mov_b32_sdwa v52, v24 dst_sel:WORD_0 dst_unused:UNUSED_PRESERVE src0_sel:WORD_1\n" \
    "v_mfma_f32_16x16x32_bf16 a[140:143], v[136:139], v[200:203], a[140:143]\n" \
    "v_exp_f32 v53, v27\n" \
    "v_mfma_f32_16x16x32_bf16 a[140:143], v[140:143], v[204:207], a[140:143]\n" \
    "ds_read_b64_tr_b16 v[126:127], v212 offset:9344\n" \
    "v_exp_f32 v28, v28\n" \
    "v_mfma_f32_16x16x32_bf16 a[144:147], v[144:147], v[200:203], a[144:147]\n" \
    "v_exp_f32 v54, v29\n" \
    "v_mov_b32_sdwa v53, v26 dst_sel:WORD_0 dst_unused:UNUSED_PRESERVE src0_sel:WORD_1\n" \
    "v_mfma_f32_16x16x32_bf16 a[144:147], v[148:151], v[204:207], a[144:147]\n" \
    "v_exp_f32 v30, v30\n" \
    "v_exp_f32 v55, v31\n" \
    "v_mov_b32_sdwa v54, v28 dst_sel:WORD_0 dst_unused:UNUSED_PRESERVE src0_sel:WORD_1\n" \
    "v_mov_b32_sdwa v55, v30 dst_sel:WORD_0 dst_unused:UNUSED_PRESERVE src0_sel:WORD_1\n" \
    "v_mfma_f32_32x32x16_bf16 a[32:47], v[68:71], v[48:51], a[32:47]\n" \
    "v_exp_f32 v32, v32\n" \
    "v_exp_f32 v56, v33\n" \
    "v_exp_f32 v34, v34\n" \
    "v_mfma_f32_32x32x16_bf16 a[32:47], v[72:75], v[52:55], a[32:47]\n" \
    "v_exp_f32 v57, v35\n" \
    "v_exp_f32 v36, v36\n" \
    "v_mov_b32_sdwa v56, v32 dst_sel:WORD_0 dst_unused:UNUSED_PRESERVE src0_sel:WORD_1\n" \
    "s_waitcnt vmcnt(6) lgkmcnt(0)\n" \
    "v_mfma_f32_32x32x8_bf16 v[16:31], v[76:77], a[208:209], 0\n" \
    "v_exp_f32 v58, v37\n" \
    "v_mov_b32_sdwa v57, v34 dst_sel:WORD_0 dst_unused:UNUSED_PRESERVE src0_sel:WORD_1\n" \
    "s_mov_b32 s66, s97\n" \
    "s_add_u32 s97, s97, 45056\n" \
    "s_cmp_eq_u32 s97, 135168\n" \
    "s_cselect_b32 s97, 0, s97\n" \
    "v_exp_f32 v38, v38\n" \
    "v_mov_b32_sdwa v58, v36 dst_sel:WORD_0 dst_unused:UNUSED_PRESERVE src0_sel:WORD_1\n" \
    "v_mfma_f32_16x16x32_bf16 a[148:151], v[152:155], v[200:203], a[148:151]\n" \
    "v_exp_f32 v59, v39\n" \
    "v_mfma_f32_16x16x32_bf16 a[148:151], v[156:159], v[204:207], a[148:151]\n" \
    "v_exp_f32 v40, v40\n" \
    "v_mfma_f32_32x32x16_bf16 v[16:31], v[64:67], a[192:195], v[16:31]\n" \
    "v_exp_f32 v60, v41\n" \
    "v_mov_b32_sdwa v59, v38 dst_sel:WORD_0 dst_unused:UNUSED_PRESERVE src0_sel:WORD_1\n" \
    "v_exp_f32 v42, v42\n" \
    "v_mov_b32_sdwa v60, v40 dst_sel:WORD_0 dst_unused:UNUSED_PRESERVE src0_sel:WORD_1\n" \
    "v_mfma_f32_16x16x32_bf16 a[152:155], v[160:163], v[200:203], a[152:155]\n" \
    "v_exp_f32 v61, v43\n" \
    "v_mfma_f32_16x16x32_bf16 a[152:155], v[164:167], v[204:207], a[152:155]\n" \
    "v_exp_f32 v44, v44\n" \
    "v_mfma_f32_16x16x32_bf16 a[156:159], v[168:171], v[200:203], a[156:159]\n" \
    "v_exp_f32 v62, v45\n" \
    "v_mov_b32_sdwa v61, v42 dst_sel:WORD_0 dst_unused:UNUSED_PRESERVE src0_sel:WORD_1\n" \
    "v_mfma_f32_16x16x32_bf16 a[156:159], v[172:175], v[204:207], a[156:159]\n" \
    "buffer_load_dwordx4 v[200:203], v8, s[56:59], s64 offen offset:0 sc1\n" \
    "buffer_load_dwordx4 v[204:207], v8, s[56:59], s64 offen offset:1024 sc1\n" \
    "s_add_u32 s64, s64, 10240\n" \
    "v_exp_f32 v46, v46\n" \
    "v_exp_f32 v63, v47\n" \
    "v_mov_b32_sdwa v62, v44 dst_sel:WORD_0 dst_unused:UNUSED_PRESERVE src0_sel:WORD_1\n" \
    "v_mov_b32_sdwa v63, v46 dst_sel:WORD_0 dst_unused:UNUSED_PRESERVE src0_sel:WORD_1\n" \
    "s_waitcnt vmcnt(6)\n" \
    "s_barrier\n" \
    "s_mov_b64 s[98:99], %15\n" \
    "s_cmp_lg_u32 %6, 0\n" \
    "s_cbranch_scc1 K2_P2%=\n" \
    "s_mov_b64 exec, 1\n" \
    "buffer_store_dwordx2 %0, off, s[40:43], s99 sc1\n" \
    "s_mov_b64 exec, -1\n" \
    "K2_P2%=:\n" \
    "s_mov_b32 s65, 5\n" \
    "K2_LOOP%=:\n" \
    "v_mfma_f32_32x32x16_bf16 a[48:63], v[68:71], v[56:59], a[48:63]\n" \
    "ds_read_b64_tr_b16 v[128:129], v211 offset:12288\n" \
    "v_exp_f32 v16, v16\n" \
    "v_exp_f32 v48, v17\n" \
    "v_exp_f32 v18, v18\n" \
    "v_mfma_f32_32x32x16_bf16 a[48:63], v[72:75], v[60:63], a[48:63]\n" \
    "ds_read_b128 v[68:71], v210 offset:0\n" \
    "v_exp_f32 v49, v19\n" \
    "v_exp_f32 v20, v20\n" \
    "v_mov_b32_sdwa v48, v16 dst_sel:WORD_0 dst_unused:UNUSED_PRESERVE src0_sel:WORD_1\n" \
    "v_mfma_f32_32x32x8_bf16 v[32:47], v[76:77], a[210:211], 0\n" \
    "ds_read_b128 v[72:75], v210 offset:640\n" \
    "ds_read_b64_tr_b16 v[130:131], v211 offset:15360\n" \
    "v_exp_f32 v50, v21\n" \
    "v_mov_b32_sdwa v49, v18 dst_sel:WORD_0 dst_unused:UNUSED_PRESERVE src0_sel:WORD_1\n" \
    "s_add_u32 m0, s95, s97\n" \
    "v_exp_f32 v22, v22\n" \
    "v_mov_b32_sdwa v50, v20 dst_sel:WORD_0 dst_unused:UNUSED_PRESERVE src0_sel:WORD_1\n" \
    "v_mfma_f32_16x16x32_bf16 a[64:67], v[80:83], v[176:179], a[64:67]\n" \
    "v_exp_f32 v51, v23\n" \
    "v_mfma_f32_16x16x32_bf16 a[64:67], v[84:87], v[180:183], a[64:67]\n" \
    "v_exp_f32 v24, v24\n" \
    "v_mfma_f32_32x32x16_bf16 v[32:47], v[64:67], a[196:199], v[32:47]\n" \
    "buffer_load_dwordx4 v8, s[84:87], s92 offen sc1 lds\n" \
    "ds_read_b64_tr_b16 v[132:133], v211 offset:18432\n" \
    "v_exp_f32 v52, v25\n" \
    "v_mov_b32_sdwa v51, v22 dst_sel:WORD_0 dst_unused:UNUSED_PRESERVE src0_sel:WORD_1\n" \
    "v_exp_f32 v26, v26\n" \
    "v_mov_b32_sdwa v52, v24 dst_sel:WORD_0 dst_unused:UNUSED_PRESERVE src0_sel:WORD_1\n" \
    "v_mfma_f32_16x16x32_bf16 a[68:71], v[88:91], v[176:179], a[68:71]\n" \
    "v_exp_f32 v53, v27\n" \
    "v_mfma_f32_16x16x32_bf16 a[68:71], v[92:95], v[180:183], a[68:71]\n" \
    "ds_read_b64_tr_b16 v[134:135], v211 offset:21504\n" \
    "v_exp_f32 v28, v28\n" \
    "v_mfma_f32_16x16x32_bf16 a[72:75], v[96:99], v[176:179], a[72:75]\n" \
    "v_exp_f32 v54, v29\n" \
    "v_mov_b32_sdwa v53, v26 dst_sel:WORD_0 dst_unused:UNUSED_PRESERVE src0_sel:WORD_1\n" \
    "v_mfma_f32_16x16x32_bf16 a[72:75], v[100:103], v[180:183], a[72:75]\n" \
    "v_exp_f32 v30, v30\n" \
    "v_exp_f32 v55, v31\n" \
    "v_mov_b32_sdwa v54, v28 dst_sel:WORD_0 dst_unused:UNUSED_PRESERVE src0_sel:WORD_1\n" \
    "v_mov_b32_sdwa v55, v30 dst_sel:WORD_0 dst_unused:UNUSED_PRESERVE src0_sel:WORD_1\n" \
    "s_waitcnt vmcnt(5) lgkmcnt(3)\n" \
    "v_mfma_f32_32x32x16_bf16 a[0:15], v[68:71], v[48:51], a[0:15]\n" \
    "ds_read_b64_tr_b16 v[136:137], v212 offset:12288\n" \
    "v_exp_f32 v32, v32\n" \
    "v_exp_f32 v56, v33\n" \
    "v_exp_f32 v34, v34\n" \
    "v_mfma_f32_32x32x16_bf16 a[0:15], v[72:75], v[52:55], a[0:15]\n" \
    "buffer_load_dwordx4 v8, s[84:87], s92 offen offset:1024 sc1 lds\n" \
    "v_exp_f32 v57, v35\n" \
    "v_exp_f32 v36, v36\n" \
    "v_mov_b32_sdwa v56, v32 dst_sel:WORD_0 dst_unused:UNUSED_PRESERVE src0_sel:WORD_1\n" \
    "v_mfma_f32_32x32x8_bf16 v[16:31], v[76:77], a[212:213], 0\n" \
    "ds_read_b64_tr_b16 v[138:139], v212 offset:15360\n" \
    "v_exp_f32 v58, v37\n" \
    "v_mov_b32_sdwa v57, v34 dst_sel:WORD_0 dst_unused:UNUSED_PRESERVE src0_sel:WORD_1\n" \
    "v_exp_f32 v38, v38\n" \
    "v_mov_b32_sdwa v58, v36 dst_sel:WORD_0 dst_unused:UNUSED_PRESERVE src0_sel:WORD_1\n" \
    "v_mfma_f32_16x16x32_bf16 a[76:79], v[104:107], v[176:179], a[76:79]\n" \
    "v_exp_f32 v59, v39\n" \
    "v_mfma_f32_16x16x32_bf16 a[76:79], v[108:111], v[180:183], a[76:79]\n" \
    "v_exp_f32 v40, v40\n" \
    "v_mfma_f32_32x32x16_bf16 v[16:31], v[64:67], a[200:203], v[16:31]\n" \
    "ds_read_b64_tr_b16 v[140:141], v212 offset:18432\n" \
    "v_exp_f32 v60, v41\n" \
    "v_mov_b32_sdwa v59, v38 dst_sel:WORD_0 dst_unused:UNUSED_PRESERVE src0_sel:WORD_1\n" \
    "v_exp_f32 v42, v42\n" \
    "v_mov_b32_sdwa v60, v40 dst_sel:WORD_0 dst_unused:UNUSED_PRESERVE src0_sel:WORD_1\n" \
    "v_mfma_f32_16x16x32_bf16 a[80:83], v[112:115], v[176:179], a[80:83]\n" \
    "v_exp_f32 v61, v43\n" \
    "v_mfma_f32_16x16x32_bf16 a[80:83], v[116:119], v[180:183], a[80:83]\n" \
    "ds_read_b64_tr_b16 v[142:143], v212 offset:21504\n" \
    "v_exp_f32 v44, v44\n" \
    "v_mfma_f32_16x16x32_bf16 a[84:87], v[120:123], v[176:179], a[84:87]\n" \
    "v_exp_f32 v62, v45\n" \
    "v_mov_b32_sdwa v61, v42 dst_sel:WORD_0 dst_unused:UNUSED_PRESERVE src0_sel:WORD_1\n" \
    "v_mfma_f32_16x16x32_bf16 a[84:87], v[124:127], v[180:183], a[84:87]\n" \
    "buffer_load_dwordx4 v[176:179], v8, s[44:47], s64 offen offset:0 sc1\n" \
    "buffer_load_dwordx4 v[180:183], v8, s[44:47], s64 offen offset:1024 sc1\n" \
    "buffer_load_dwordx2 v[222:223], v221, s[40:43], s71 offen sc1\n" \
    "v_exp_f32 v46, v46\n" \
    "v_exp_f32 v63, v47\n" \
    "v_mov_b32_sdwa v62, v44 dst_sel:WORD_0 dst_unused:UNUSED_PRESERVE src0_sel:WORD_1\n" \
    "v_mov_b32_sdwa v63, v46 dst_sel:WORD_0 dst_unused:UNUSED_PRESERVE src0_sel:WORD_1\n" \
    "v_mfma_f32_32x32x16_bf16 a[16:31], v[68:71], v[56:59], a[16:31]\n" \
    "ds_read_b64_tr_b16 v[144:145], v211 offset:12352\n" \
    "v_exp_f32 v16, v16\n" \
    "v_exp_f32 v48, v17\n" \
    "v_exp_f32 v18, v18\n" \
    "v_mfma_f32_32x32x16_bf16 a[16:31], v[72:75], v[60:63], a[16:31]\n" \
    "buffer_load_dwordx4 v8, s[84:87], s92 offen offset:2048 sc1 lds\n" \
    "v_exp_f32 v49, v19\n" \
    "v_exp_f32 v20, v20\n" \
    "v_mov_b32_sdwa v48, v16 dst_sel:WORD_0 dst_unused:UNUSED_PRESERVE src0_sel:WORD_1\n" \
    "v_mfma_f32_32x32x8_bf16 v[32:47], v[76:77], a[214:215], 0\n" \
    "ds_read_b64_tr_b16 v[146:147], v211 offset:15424\n" \
    "v_exp_f32 v50, v21\n" \
    "v_mov_b32_sdwa v49, v18 dst_sel:WORD_0 dst_unused:UNUSED_PRESERVE src0_sel:WORD_1\n" \
    "v_exp_f32 v22, v22\n" \
    "v_mov_b32_sdwa v50, v20 dst_sel:WORD_0 dst_unused:UNUSED_PRESERVE src0_sel:WORD_1\n" \
    "v_mfma_f32_16x16x32_bf16 a[88:91], v[80:83], v[184:187], a[88:91]\n" \
    "v_exp_f32 v51, v23\n" \
    "v_mfma_f32_16x16x32_bf16 a[88:91], v[84:87], v[188:191], a[88:91]\n" \
    "v_exp_f32 v24, v24\n" \
    "v_mfma_f32_32x32x16_bf16 v[32:47], v[64:67], a[204:207], v[32:47]\n" \
    "ds_read_b128 v[64:67], v208 offset:2560\n" \
    "ds_read_b32 v76, v209 offset:2560\n" \
    "ds_read_b64_tr_b16 v[148:149], v211 offset:18496\n" \
    "v_exp_f32 v52, v25\n" \
    "v_mov_b32_sdwa v51, v22 dst_sel:WORD_0 dst_unused:UNUSED_PRESERVE src0_sel:WORD_1\n" \
    "v_exp_f32 v26, v26\n" \
    "v_mov_b32_sdwa v52, v24 dst_sel:WORD_0 dst_unused:UNUSED_PRESERVE src0_sel:WORD_1\n" \
    "v_mfma_f32_16x16x32_bf16 a[92:95], v[88:91], v[184:187], a[92:95]\n" \
    "v_exp_f32 v53, v27\n" \
    "v_mfma_f32_16x16x32_bf16 a[92:95], v[92:95], v[188:191], a[92:95]\n" \
    "ds_read_b64_tr_b16 v[150:151], v211 offset:21568\n" \
    "v_exp_f32 v28, v28\n" \
    "v_mfma_f32_16x16x32_bf16 a[96:99], v[96:99], v[184:187], a[96:99]\n" \
    "v_exp_f32 v54, v29\n" \
    "v_mov_b32_sdwa v53, v26 dst_sel:WORD_0 dst_unused:UNUSED_PRESERVE src0_sel:WORD_1\n" \
    "v_mfma_f32_16x16x32_bf16 a[96:99], v[100:103], v[188:191], a[96:99]\n" \
    "v_exp_f32 v30, v30\n" \
    "v_exp_f32 v55, v31\n" \
    "v_mov_b32_sdwa v54, v28 dst_sel:WORD_0 dst_unused:UNUSED_PRESERVE src0_sel:WORD_1\n" \
    "v_mov_b32_sdwa v55, v30 dst_sel:WORD_0 dst_unused:UNUSED_PRESERVE src0_sel:WORD_1\n" \
    "v_mfma_f32_32x32x16_bf16 a[32:47], v[68:71], v[48:51], a[32:47]\n" \
    "ds_read_b64_tr_b16 v[152:153], v212 offset:12352\n" \
    "v_exp_f32 v32, v32\n" \
    "v_exp_f32 v56, v33\n" \
    "v_exp_f32 v34, v34\n" \
    "v_mfma_f32_32x32x16_bf16 a[32:47], v[72:75], v[52:55], a[32:47]\n" \
    "buffer_load_dwordx4 v8, s[84:87], s92 offen offset:3072 sc1 lds\n" \
    "v_exp_f32 v57, v35\n" \
    "v_exp_f32 v36, v36\n" \
    "v_mov_b32_sdwa v56, v32 dst_sel:WORD_0 dst_unused:UNUSED_PRESERVE src0_sel:WORD_1\n" \
    "s_waitcnt vmcnt(9) lgkmcnt(3)\n" \
    "v_mfma_f32_32x32x8_bf16 v[16:31], v[76:77], a[208:209], 0\n" \
    "ds_read_b64_tr_b16 v[154:155], v212 offset:15424\n" \
    "v_exp_f32 v58, v37\n" \
    "v_mov_b32_sdwa v57, v34 dst_sel:WORD_0 dst_unused:UNUSED_PRESERVE src0_sel:WORD_1\n" \
    "v_exp_f32 v38, v38\n" \
    "v_mov_b32_sdwa v58, v36 dst_sel:WORD_0 dst_unused:UNUSED_PRESERVE src0_sel:WORD_1\n" \
    "v_mfma_f32_16x16x32_bf16 a[100:103], v[104:107], v[184:187], a[100:103]\n" \
    "v_exp_f32 v59, v39\n" \
    "v_mfma_f32_16x16x32_bf16 a[100:103], v[108:111], v[188:191], a[100:103]\n" \
    "v_exp_f32 v40, v40\n" \
    "v_mfma_f32_32x32x16_bf16 v[16:31], v[64:67], a[192:195], v[16:31]\n" \
    "v_exp_f32 v60, v41\n" \
    "v_mov_b32_sdwa v59, v38 dst_sel:WORD_0 dst_unused:UNUSED_PRESERVE src0_sel:WORD_1\n" \
    "v_exp_f32 v42, v42\n" \
    "v_mov_b32_sdwa v60, v40 dst_sel:WORD_0 dst_unused:UNUSED_PRESERVE src0_sel:WORD_1\n" \
    "v_mfma_f32_16x16x32_bf16 a[104:107], v[112:115], v[184:187], a[104:107]\n" \
    "v_exp_f32 v61, v43\n" \
    "v_mfma_f32_16x16x32_bf16 a[104:107], v[116:119], v[188:191], a[104:107]\n" \
    "ds_read_b64_tr_b16 v[156:157], v212 offset:18496\n" \
    "v_exp_f32 v44, v44\n" \
    "v_mfma_f32_16x16x32_bf16 a[108:111], v[120:123], v[184:187], a[108:111]\n" \
    "v_exp_f32 v62, v45\n" \
    "v_mov_b32_sdwa v61, v42 dst_sel:WORD_0 dst_unused:UNUSED_PRESERVE src0_sel:WORD_1\n" \
    "v_mfma_f32_16x16x32_bf16 a[108:111], v[124:127], v[188:191], a[108:111]\n" \
    "buffer_load_dwordx4 v[184:187], v8, s[48:51], s64 offen offset:0 sc1\n" \
    "buffer_load_dwordx4 v[188:191], v8, s[48:51], s64 offen offset:1024 sc1\n" \
    "v_exp_f32 v46, v46\n" \
    "v_exp_f32 v63, v47\n" \
    "v_mov_b32_sdwa v62, v44 dst_sel:WORD_0 dst_unused:UNUSED_PRESERVE src0_sel:WORD_1\n" \
    "v_mov_b32_sdwa v63, v46 dst_sel:WORD_0 dst_unused:UNUSED_PRESERVE src0_sel:WORD_1\n" \
    "v_mfma_f32_32x32x16_bf16 a[48:63], v[68:71], v[56:59], a[48:63]\n" \
    "ds_read_b64_tr_b16 v[158:159], v212 offset:21568\n" \
    "v_exp_f32 v16, v16\n" \
    "v_exp_f32 v48, v17\n" \
    "v_exp_f32 v18, v18\n" \
    "v_mfma_f32_32x32x16_bf16 a[48:63], v[72:75], v[60:63], a[48:63]\n" \
    "ds_read_b128 v[68:71], v210 offset:2560\n" \
    "v_exp_f32 v49, v19\n" \
    "v_exp_f32 v20, v20\n" \
    "v_mov_b32_sdwa v48, v16 dst_sel:WORD_0 dst_unused:UNUSED_PRESERVE src0_sel:WORD_1\n" \
    "v_mfma_f32_32x32x8_bf16 v[32:47], v[76:77], a[210:211], 0\n" \
    "ds_read_b128 v[72:75], v210 offset:3200\n" \
    "ds_read_b64_tr_b16 v[160:161], v211 offset:12416\n" \
    "v_exp_f32 v50, v21\n" \
    "v_mov_b32_sdwa v49, v18 dst_sel:WORD_0 dst_unused:UNUSED_PRESERVE src0_sel:WORD_1\n" \
    "s_add_u32 m0, m0, 0x1000\n" \
    "v_exp_f32 v22, v22\n" \
    "v_mov_b32_sdwa v50, v20 dst_sel:WORD_0 dst_unused:UNUSED_PRESERVE src0_sel:WORD_1\n" \
    "v_mfma_f32_16x16x32_bf16 a[112:115], v[80:83], v[192:195], a[112:115]\n" \
    "v_exp_f32 v51, v23\n" \
    "v_mfma_f32_16x16x32_bf16 a[112:115], v[84:87], v[196:199], a[112:115]\n" \
    "v_exp_f32 v24, v24\n" \
    "v_mfma_f32_32x32x16_bf16 v[32:47], v[64:67], a[196:199], v[32:47]\n" \
    "buffer_load_dwordx4 v9, s[84:87], s92 offen sc1 lds\n" \
    "s_add_u32 s92, s92, 10240\n" \
    "v_exp_f32 v52, v25\n" \
    "v_mov_b32_sdwa v51, v22 dst_sel:WORD_0 dst_unused:UNUSED_PRESERVE src0_sel:WORD_1\n" \
    "v_exp_f32 v26, v26\n" \
    "v_mov_b32_sdwa v52, v24 dst_sel:WORD_0 dst_unused:UNUSED_PRESERVE src0_sel:WORD_1\n" \
    "v_mfma_f32_16x16x32_bf16 a[116:119], v[88:91], v[192:195], a[116:119]\n" \
    "v_exp_f32 v53, v27\n" \
    "v_mfma_f32_16x16x32_bf16 a[116:119], v[92:95], v[196:199], a[116:119]\n" \
    "ds_read_b64_tr_b16 v[162:163], v211 offset:15488\n" \
    "v_exp_f32 v28, v28\n" \
    "v_mfma_f32_16x16x32_bf16 a[120:123], v[96:99], v[192:195], a[120:123]\n" \
    "v_exp_f32 v54, v29\n" \
    "v_mov_b32_sdwa v53, v26 dst_sel:WORD_0 dst_unused:UNUSED_PRESERVE src0_sel:WORD_1\n" \
    "v_mfma_f32_16x16x32_bf16 a[120:123], v[100:103], v[196:199], a[120:123]\n" \
    "v_exp_f32 v30, v30\n" \
    "v_exp_f32 v55, v31\n" \
    "v_mov_b32_sdwa v54, v28 dst_sel:WORD_0 dst_unused:UNUSED_PRESERVE src0_sel:WORD_1\n" \
    "v_mov_b32_sdwa v55, v30 dst_sel:WORD_0 dst_unused:UNUSED_PRESERVE src0_sel:WORD_1\n" \
    "s_waitcnt vmcnt(10) lgkmcnt(2)\n" \
    "v_mfma_f32_32x32x16_bf16 a[0:15], v[68:71], v[48:51], a[0:15]\n" \
    "ds_read_b64_tr_b16 v[164:165], v211 offset:18560\n" \
    "v_exp_f32 v32, v32\n" \
    "v_exp_f32 v56, v33\n" \
    "s_add_u32 m0, s96, s97\n" \
    "v_exp_f32 v34, v34\n" \
    "v_mfma_f32_32x32x16_bf16 a[0:15], v[72:75], v[52:55], a[0:15]\n" \
    "buffer_load_dwordx4 v11, s[88:91], s93 offen lds\n" \
    "v_exp_f32 v57, v35\n" \
    "v_exp_f32 v36, v36\n" \
    "v_mov_b32_sdwa v56, v32 dst_sel:WORD_0 dst_unused:UNUSED_PRESERVE src0_sel:WORD_1\n" \
    "v_mfma_f32_32x32x8_bf16 v[16:31], v[76:77], a[212:213], 0\n" \
    "ds_read_b64_tr_b16 v[166:167], v211 offset:21632\n" \
    "v_exp_f32 v58, v37\n" \
    "v_mov_b32_sdwa v57, v34 dst_sel:WORD_0 dst_unused:UNUSED_PRESERVE src0_sel:WORD_1\n" \
    "v_exp_f32 v38, v38\n" \
    "v_mov_b32_sdwa v58, v36 dst_sel:WORD_0 dst_unused:UNUSED_PRESERVE src0_sel:WORD_1\n" \
    "v_mfma_f32_16x16x32_bf16 a[124:127], v[104:107], v[192:195], a[124:127]\n" \
    "v_exp_f32 v59, v39\n" \
    "v_mfma_f32_16x16x32_bf16 a[124:127], v[108:111], v[196:199], a[124:127]\n" \
    "v_exp_f32 v40, v40\n" \
    "v_mfma_f32_32x32x16_bf16 v[16:31], v[64:67], a[200:203], v[16:31]\n" \
    "v_exp_f32 v60, v41\n" \
    "v_mov_b32_sdwa v59, v38 dst_sel:WORD_0 dst_unused:UNUSED_PRESERVE src0_sel:WORD_1\n" \
    "v_exp_f32 v42, v42\n" \
    "v_mov_b32_sdwa v60, v40 dst_sel:WORD_0 dst_unused:UNUSED_PRESERVE src0_sel:WORD_1\n" \
    "v_mfma_f32_16x16x32_bf16 a[128:131], v[112:115], v[192:195], a[128:131]\n" \
    "v_exp_f32 v61, v43\n" \
    "v_mfma_f32_16x16x32_bf16 a[128:131], v[116:119], v[196:199], a[128:131]\n" \
    "ds_read_b64_tr_b16 v[168:169], v212 offset:12416\n" \
    "v_exp_f32 v44, v44\n" \
    "v_mfma_f32_16x16x32_bf16 a[132:135], v[120:123], v[192:195], a[132:135]\n" \
    "v_exp_f32 v62, v45\n" \
    "v_mov_b32_sdwa v61, v42 dst_sel:WORD_0 dst_unused:UNUSED_PRESERVE src0_sel:WORD_1\n" \
    "v_mfma_f32_16x16x32_bf16 a[132:135], v[124:127], v[196:199], a[132:135]\n" \
    "buffer_load_dwordx4 v[192:195], v8, s[52:55], s64 offen offset:0 sc1\n" \
    "buffer_load_dwordx4 v[196:199], v8, s[52:55], s64 offen offset:1024 sc1\n" \
    "v_exp_f32 v46, v46\n" \
    "v_exp_f32 v63, v47\n" \
    "v_mov_b32_sdwa v62, v44 dst_sel:WORD_0 dst_unused:UNUSED_PRESERVE src0_sel:WORD_1\n" \
    "v_mov_b32_sdwa v63, v46 dst_sel:WORD_0 dst_unused:UNUSED_PRESERVE src0_sel:WORD_1\n" \
    "v_mfma_f32_32x32x16_bf16 a[16:31], v[68:71], v[56:59], a[16:31]\n" \
    "ds_read_b64_tr_b16 v[170:171], v212 offset:15488\n" \
    "v_exp_f32 v16, v16\n" \
    "v_exp_f32 v48, v17\n" \
    "v_exp_f32 v18, v18\n" \
    "v_mfma_f32_32x32x16_bf16 a[16:31], v[72:75], v[60:63], a[16:31]\n" \
    "buffer_load_dwordx4 v12, s[88:91], s93 offen offset:1024 lds\n" \
    "v_exp_f32 v49, v19\n" \
    "v_exp_f32 v20, v20\n" \
    "v_mov_b32_sdwa v48, v16 dst_sel:WORD_0 dst_unused:UNUSED_PRESERVE src0_sel:WORD_1\n" \
    "v_mfma_f32_32x32x8_bf16 v[32:47], v[76:77], a[214:215], 0\n" \
    "ds_read_b64_tr_b16 v[172:173], v212 offset:18560\n" \
    "v_exp_f32 v50, v21\n" \
    "v_mov_b32_sdwa v49, v18 dst_sel:WORD_0 dst_unused:UNUSED_PRESERVE src0_sel:WORD_1\n" \
    "v_exp_f32 v22, v22\n" \
    "v_mov_b32_sdwa v50, v20 dst_sel:WORD_0 dst_unused:UNUSED_PRESERVE src0_sel:WORD_1\n" \
    "v_mfma_f32_16x16x32_bf16 a[136:139], v[80:83], v[200:203], a[136:139]\n" \
    "v_exp_f32 v51, v23\n" \
    "v_mfma_f32_16x16x32_bf16 a[136:139], v[84:87], v[204:207], a[136:139]\n" \
    "v_exp_f32 v24, v24\n" \
    "v_mfma_f32_32x32x16_bf16 v[32:47], v[64:67], a[204:207], v[32:47]\n" \
    "ds_read_b128 v[64:67], v208 offset:5120\n" \
    "ds_read_b32 v76, v209 offset:5120\n" \
    "v_exp_f32 v52, v25\n" \
    "v_mov_b32_sdwa v51, v22 dst_sel:WORD_0 dst_unused:UNUSED_PRESERVE src0_sel:WORD_1\n" \
    "v_exp_f32 v26, v26\n" \
    "v_mov_b32_sdwa v52, v24 dst_sel:WORD_0 dst_unused:UNUSED_PRESERVE src0_sel:WORD_1\n" \
    "v_mfma_f32_16x16x32_bf16 a[140:143], v[88:91], v[200:203], a[140:143]\n" \
    "v_exp_f32 v53, v27\n" \
    "v_mfma_f32_16x16x32_bf16 a[140:143], v[92:95], v[204:207], a[140:143]\n" \
    "ds_read_b64_tr_b16 v[174:175], v212 offset:21632\n" \
    "v_exp_f32 v28, v28\n" \
    "v_mfma_f32_16x16x32_bf16 a[144:147], v[96:99], v[200:203], a[144:147]\n" \
    "v_exp_f32 v54, v29\n" \
    "v_mov_b32_sdwa v53, v26 dst_sel:WORD_0 dst_unused:UNUSED_PRESERVE src0_sel:WORD_1\n" \
    "v_mfma_f32_16x16x32_bf16 a[144:147], v[100:103], v[204:207], a[144:147]\n" \
    "v_exp_f32 v30, v30\n" \
    "v_exp_f32 v55, v31\n" \
    "v_mov_b32_sdwa v54, v28 dst_sel:WORD_0 dst_unused:UNUSED_PRESERVE src0_sel:WORD_1\n" \
    "v_mov_b32_sdwa v55, v30 dst_sel:WORD_0 dst_unused:UNUSED_PRESERVE src0_sel:WORD_1\n" \
    "v_mfma_f32_32x32x16_bf16 a[32:47], v[68:71], v[48:51], a[32:47]\n" \
    "v_exp_f32 v32, v32\n" \
    "v_exp_f32 v56, v33\n" \
    "v_exp_f32 v34, v34\n" \
    "v_mfma_f32_32x32x16_bf16 a[32:47], v[72:75], v[52:55], a[32:47]\n" \
    "buffer_load_dwordx4 v13, s[88:91], s93 offen offset:2048 lds\n" \
    "s_add_u32 s93, s93, 737280\n" \
    "v_exp_f32 v57, v35\n" \
    "v_exp_f32 v36, v36\n" \
    "v_mov_b32_sdwa v56, v32 dst_sel:WORD_0 dst_unused:UNUSED_PRESERVE src0_sel:WORD_1\n" \
    "s_waitcnt vmcnt(11) lgkmcnt(0)\n" \
    "v_mfma_f32_32x32x8_bf16 v[16:31], v[76:77], a[208:209], 0\n" \
    "v_exp_f32 v58, v37\n" \
    "v_mov_b32_sdwa v57, v34 dst_sel:WORD_0 dst_unused:UNUSED_PRESERVE src0_sel:WORD_1\n" \
    "v_exp_f32 v38, v38\n" \
    "v_mov_b32_sdwa v58, v36 dst_sel:WORD_0 dst_unused:UNUSED_PRESERVE src0_sel:WORD_1\n" \
    "v_mfma_f32_16x16x32_bf16 a[148:151], v[104:107], v[200:203], a[148:151]\n" \
    "v_exp_f32 v59, v39\n" \
    "v_mfma_f32_16x16x32_bf16 a[148:151], v[108:111], v[204:207], a[148:151]\n" \
    "v_exp_f32 v40, v40\n" \
    "v_mfma_f32_32x32x16_bf16 v[16:31], v[64:67], a[192:195], v[16:31]\n" \
    "v_exp_f32 v60, v41\n" \
    "v_mov_b32_sdwa v59, v38 dst_sel:WORD_0 dst_unused:UNUSED_PRESERVE src0_sel:WORD_1\n" \
    "v_exp_f32 v42, v42\n" \
    "v_mov_b32_sdwa v60, v40 dst_sel:WORD_0 dst_unused:UNUSED_PRESERVE src0_sel:WORD_1\n" \
    "v_mfma_f32_16x16x32_bf16 a[152:155], v[112:115], v[200:203], a[152:155]\n" \
    "v_exp_f32 v61, v43\n" \
    "v_mfma_f32_16x16x32_bf16 a[152:155], v[116:119], v[204:207], a[152:155]\n" \
    "v_exp_f32 v44, v44\n" \
    "v_mfma_f32_16x16x32_bf16 a[156:159], v[120:123], v[200:203], a[156:159]\n" \
    "v_exp_f32 v62, v45\n" \
    "v_mov_b32_sdwa v61, v42 dst_sel:WORD_0 dst_unused:UNUSED_PRESERVE src0_sel:WORD_1\n" \
    "v_mfma_f32_16x16x32_bf16 a[156:159], v[124:127], v[204:207], a[156:159]\n" \
    "buffer_load_dwordx4 v[200:203], v8, s[56:59], s64 offen offset:0 sc1\n" \
    "buffer_load_dwordx4 v[204:207], v8, s[56:59], s64 offen offset:1024 sc1\n" \
    "s_add_u32 s64, s64, 10240\n" \
    "v_exp_f32 v46, v46\n" \
    "v_exp_f32 v63, v47\n" \
    "v_mov_b32_sdwa v62, v44 dst_sel:WORD_0 dst_unused:UNUSED_PRESERVE src0_sel:WORD_1\n" \
    "v_mov_b32_sdwa v63, v46 dst_sel:WORD_0 dst_unused:UNUSED_PRESERVE src0_sel:WORD_1\n" \
    "v_add_u32 v211, s66, v220\n" \
    "v_xor_b32 v212, 32, v211\n" \
    "v_mfma_f32_32x32x16_bf16 a[48:63], v[68:71], v[56:59], a[48:63]\n" \
    "ds_read_b64_tr_b16 v[80:81], v211 offset:0\n" \
    "v_exp_f32 v16, v16\n" \
    "v_exp_f32 v48, v17\n" \
    "v_exp_f32 v18, v18\n" \
    "v_mfma_f32_32x32x16_bf16 a[48:63], v[72:75], v[60:63], a[48:63]\n" \
    "ds_read_b128 v[68:71], v210 offset:5120\n" \
    "v_exp_f32 v49, v19\n" \
    "v_exp_f32 v20, v20\n" \
    "v_mov_b32_sdwa v48, v16 dst_sel:WORD_0 dst_unused:UNUSED_PRESERVE src0_sel:WORD_1\n" \
    "v_mfma_f32_32x32x8_bf16 v[32:47], v[76:77], a[210:211], 0\n" \
    "ds_read_b128 v[72:75], v210 offset:5760\n" \
    "ds_read_b64_tr_b16 v[82:83], v211 offset:3072\n" \
    "v_exp_f32 v50, v21\n" \
    "v_mov_b32_sdwa v49, v18 dst_sel:WORD_0 dst_unused:UNUSED_PRESERVE src0_sel:WORD_1\n" \
    "s_add_u32 m0, m0, 0xc00\n" \
    "v_exp_f32 v22, v22\n" \
    "v_mov_b32_sdwa v50, v20 dst_sel:WORD_0 dst_unused:UNUSED_PRESERVE src0_sel:WORD_1\n" \
    "v_mfma_f32_16x16x32_bf16 a[64:67], v[128:131], v[176:179], a[64:67]\n" \
    "v_exp_f32 v51, v23\n" \
    "v_mfma_f32_16x16x32_bf16 a[64:67], v[132:135], v[180:183], a[64:67]\n" \
    "v_exp_f32 v24, v24\n" \
    "v_mfma_f32_32x32x16_bf16 v[32:47], v[64:67], a[196:199], v[32:47]\n" \
    "buffer_load_dwordx4 v11, s[88:91], s94 offen lds\n" \
    "ds_read_b64_tr_b16 v[84:85], v211 offset:6144\n" \
    "v_exp_f32 v52, v25\n" \
    "v_mov_b32_sdwa v51, v22 dst_sel:WORD_0 dst_unused:UNUSED_PRESERVE src0_sel:WORD_1\n" \
    "v_exp_f32 v26, v26\n" \
    "v_mov_b32_sdwa v52, v24 dst_sel:WORD_0 dst_unused:UNUSED_PRESERVE src0_sel:WORD_1\n" \
    "v_mfma_f32_16x16x32_bf16 a[68:71], v[136:139], v[176:179], a[68:71]\n" \
    "v_exp_f32 v53, v27\n" \
    "v_mfma_f32_16x16x32_bf16 a[68:71], v[140:143], v[180:183], a[68:71]\n" \
    "ds_read_b64_tr_b16 v[86:87], v211 offset:9216\n" \
    "v_exp_f32 v28, v28\n" \
    "v_mfma_f32_16x16x32_bf16 a[72:75], v[144:147], v[176:179], a[72:75]\n" \
    "v_exp_f32 v54, v29\n" \
    "v_mov_b32_sdwa v53, v26 dst_sel:WORD_0 dst_unused:UNUSED_PRESERVE src0_sel:WORD_1\n" \
    "v_mfma_f32_16x16x32_bf16 a[72:75], v[148:151], v[180:183], a[72:75]\n" \
    "v_exp_f32 v30, v30\n" \
    "v_exp_f32 v55, v31\n" \
    "v_mov_b32_sdwa v54, v28 dst_sel:WORD_0 dst_unused:UNUSED_PRESERVE src0_sel:WORD_1\n" \
    "v_mov_b32_sdwa v55, v30 dst_sel:WORD_0 dst_unused:UNUSED_PRESERVE src0_sel:WORD_1\n" \
    "s_waitcnt vmcnt(9) lgkmcnt(3)\n" \
    "v_mfma_f32_32x32x16_bf16 a[0:15], v[68:71], v[48:51], a[0:15]\n" \
    "ds_read_b64_tr_b16 v[88:89], v212 offset:0\n" \
    "v_exp_f32 v32, v32\n" \
    "v_exp_f32 v56, v33\n" \
    "v_exp_f32 v34, v34\n" \
    "v_mfma_f32_32x32x16_bf16 a[0:15], v[72:75], v[52:55], a[0:15]\n" \
    "buffer_load_dwordx4 v12, s[88:91], s94 offen offset:1024 lds\n" \
    "v_exp_f32 v57, v35\n" \
    "v_exp_f32 v36, v36\n" \
    "v_mov_b32_sdwa v56, v32 dst_sel:WORD_0 dst_unused:UNUSED_PRESERVE src0_sel:WORD_1\n" \
    "v_mfma_f32_32x32x8_bf16 v[16:31], v[76:77], a[212:213], 0\n" \
    "ds_read_b64_tr_b16 v[90:91], v212 offset:3072\n" \
    "v_exp_f32 v58, v37\n" \
    "v_mov_b32_sdwa v57, v34 dst_sel:WORD_0 dst_unused:UNUSED_PRESERVE src0_sel:WORD_1\n" \
    "v_exp_f32 v38, v38\n" \
    "v_mov_b32_sdwa v58, v36 dst_sel:WORD_0 dst_unused:UNUSED_PRESERVE src0_sel:WORD_1\n" \
    "v_mfma_f32_16x16x32_bf16 a[76:79], v[152:155], v[176:179], a[76:79]\n" \
    "v_exp_f32 v59, v39\n" \
    "v_mfma_f32_16x16x32_bf16 a[76:79], v[156:159], v[180:183], a[76:79]\n" \
    "v_exp_f32 v40, v40\n" \
    "v_mfma_f32_32x32x16_bf16 v[16:31], v[64:67], a[200:203], v[16:31]\n" \
    "ds_read_b64_tr_b16 v[92:93], v212 offset:6144\n" \
    "v_exp_f32 v60, v41\n" \
    "v_mov_b32_sdwa v59, v38 dst_sel:WORD_0 dst_unused:UNUSED_PRESERVE src0_sel:WORD_1\n" \
    "v_exp_f32 v42, v42\n" \
    "v_mov_b32_sdwa v60, v40 dst_sel:WORD_0 dst_unused:UNUSED_PRESERVE src0_sel:WORD_1\n" \
    "v_mfma_f32_16x16x32_bf16 a[80:83], v[160:163], v[176:179], a[80:83]\n" \
    "v_exp_f32 v61, v43\n" \
    "v_mfma_f32_16x16x32_bf16 a[80:83], v[164:167], v[180:183], a[80:83]\n" \
    "ds_read_b64_tr_b16 v[94:95], v212 offset:9216\n" \
    "v_exp_f32 v44, v44\n" \
    "v_mfma_f32_16x16x32_bf16 a[84:87], v[168:171], v[176:179], a[84:87]\n" \
    "v_exp_f32 v62, v45\n" \
    "v_mov_b32_sdwa v61, v42 dst_sel:WORD_0 dst_unused:UNUSED_PRESERVE src0_sel:WORD_1\n" \
    "v_mfma_f32_16x16x32_bf16 a[84:87], v[172:175], v[180:183], a[84:87]\n" \
    "buffer_load_dwordx4 v[176:179], v8, s[44:47], s64 offen offset:0 sc1\n" \
    "buffer_load_dwordx4 v[180:183], v8, s[44:47], s64 offen offset:1024 sc1\n" \
    "v_exp_f32 v46, v46\n" \
    "v_exp_f32 v63, v47\n" \
    "v_mov_b32_sdwa v62, v44 dst_sel:WORD_0 dst_unused:UNUSED_PRESERVE src0_sel:WORD_1\n" \
    "v_mov_b32_sdwa v63, v46 dst_sel:WORD_0 dst_unused:UNUSED_PRESERVE src0_sel:WORD_1\n" \
    "v_mfma_f32_32x32x16_bf16 a[16:31], v[68:71], v[56:59], a[16:31]\n" \
    "ds_read_b64_tr_b16 v[96:97], v211 offset:64\n" \
    "v_exp_f32 v16, v16\n" \
    "v_exp_f32 v48, v17\n" \
    "v_exp_f32 v18, v18\n" \
    "v_mfma_f32_32x32x16_bf16 a[16:31], v[72:75], v[60:63], a[16:31]\n" \
    "buffer_load_dwordx4 v13, s[88:91], s94 offen offset:2048 lds\n" \
    "s_add_u32 s94, s94, 737280\n" \
    "v_exp_f32 v49, v19\n" \
    "v_exp_f32 v20, v20\n" \
    "v_mov_b32_sdwa v48, v16 dst_sel:WORD_0 dst_unused:UNUSED_PRESERVE src0_sel:WORD_1\n" \
    "v_mfma_f32_32x32x8_bf16 v[32:47], v[76:77], a[214:215], 0\n" \
    "ds_read_b64_tr_b16 v[98:99], v211 offset:3136\n" \
    "v_exp_f32 v50, v21\n" \
    "v_mov_b32_sdwa v49, v18 dst_sel:WORD_0 dst_unused:UNUSED_PRESERVE src0_sel:WORD_1\n" \
    "v_exp_f32 v22, v22\n" \
    "v_mov_b32_sdwa v50, v20 dst_sel:WORD_0 dst_unused:UNUSED_PRESERVE src0_sel:WORD_1\n" \
    "v_mfma_f32_16x16x32_bf16 a[88:91], v[128:131], v[184:187], a[88:91]\n" \
    "v_exp_f32 v51, v23\n" \
    "v_mfma_f32_16x16x32_bf16 a[88:91], v[132:135], v[188:191], a[88:91]\n" \
    "v_exp_f32 v24, v24\n" \
    "v_mfma_f32_32x32x16_bf16 v[32:47], v[64:67], a[204:207], v[32:47]\n" \
    "ds_read_b128 v[64:67], v208 offset:7680\n" \
    "ds_read_b32 v76, v209 offset:7680\n" \
    "ds_read_b64_tr_b16 v[100:101], v211 offset:6208\n" \
    "v_exp_f32 v52, v25\n" \
    "v_mov_b32_sdwa v51, v22 dst_sel:WORD_0 dst_unused:UNUSED_PRESERVE src0_sel:WORD_1\n" \
    "v_exp_f32 v26, v26\n" \
    "v_mov_b32_sdwa v52, v24 dst_sel:WORD_0 dst_unused:UNUSED_PRESERVE src0_sel:WORD_1\n" \
    "v_mfma_f32_16x16x32_bf16 a[92:95], v[136:139], v[184:187], a[92:95]\n" \
    "v_exp_f32 v53, v27\n" \
    "v_mfma_f32_16x16x32_bf16 a[92:95], v[140:143], v[188:191], a[92:95]\n" \
    "ds_read_b64_tr_b16 v[102:103], v211 offset:9280\n" \
    "v_exp_f32 v28, v28\n" \
    "v_mfma_f32_16x16x32_bf16 a[96:99], v[144:147], v[184:187], a[96:99]\n" \
    "v_exp_f32 v54, v29\n" \
    "v_mov_b32_sdwa v53, v26 dst_sel:WORD_0 dst_unused:UNUSED_PRESERVE src0_sel:WORD_1\n" \
    "v_mfma_f32_16x16x32_bf16 a[96:99], v[148:151], v[188:191], a[96:99]\n" \
    "v_exp_f32 v30, v30\n" \
    "v_exp_f32 v55, v31\n" \
    "v_mov_b32_sdwa v54, v28 dst_sel:WORD_0 dst_unused:UNUSED_PRESERVE src0_sel:WORD_1\n" \
    "v_mov_b32_sdwa v55, v30 dst_sel:WORD_0 dst_unused:UNUSED_PRESERVE src0_sel:WORD_1\n" \
    "v_mfma_f32_32x32x16_bf16 a[32:47], v[68:71], v[48:51], a[32:47]\n" \
    "ds_read_b64_tr_b16 v[104:105], v212 offset:64\n" \
    "v_exp_f32 v32, v32\n" \
    "v_exp_f32 v56, v33\n" \
    "v_exp_f32 v34, v34\n" \
    "v_mfma_f32_32x32x16_bf16 a[32:47], v[72:75], v[52:55], a[32:47]\n" \
    "v_exp_f32 v57, v35\n" \
    "v_exp_f32 v36, v36\n" \
    "v_mov_b32_sdwa v56, v32 dst_sel:WORD_0 dst_unused:UNUSED_PRESERVE src0_sel:WORD_1\n" \
    "s_waitcnt vmcnt(9) lgkmcnt(3)\n" \
    "v_mfma_f32_32x32x8_bf16 v[16:31], v[76:77], a[208:209], 0\n" \
    "ds_read_b64_tr_b16 v[106:107], v212 offset:3136\n" \
    "v_exp_f32 v58, v37\n" \
    "v_mov_b32_sdwa v57, v34 dst_sel:WORD_0 dst_unused:UNUSED_PRESERVE src0_sel:WORD_1\n" \
    "v_exp_f32 v38, v38\n" \
    "v_mov_b32_sdwa v58, v36 dst_sel:WORD_0 dst_unused:UNUSED_PRESERVE src0_sel:WORD_1\n" \
    "v_mfma_f32_16x16x32_bf16 a[100:103], v[152:155], v[184:187], a[100:103]\n" \
    "v_exp_f32 v59, v39\n" \
    "v_mfma_f32_16x16x32_bf16 a[100:103], v[156:159], v[188:191], a[100:103]\n" \
    "v_exp_f32 v40, v40\n" \
    "v_mfma_f32_32x32x16_bf16 v[16:31], v[64:67], a[192:195], v[16:31]\n" \
    "v_exp_f32 v60, v41\n" \
    "v_mov_b32_sdwa v59, v38 dst_sel:WORD_0 dst_unused:UNUSED_PRESERVE src0_sel:WORD_1\n" \
    "v_exp_f32 v42, v42\n" \
    "v_mov_b32_sdwa v60, v40 dst_sel:WORD_0 dst_unused:UNUSED_PRESERVE src0_sel:WORD_1\n" \
    "v_mfma_f32_16x16x32_bf16 a[104:107], v[160:163], v[184:187], a[104:107]\n" \
    "v_exp_f32 v61, v43\n" \
    "v_mfma_f32_16x16x32_bf16 a[104:107], v[164:167], v[188:191], a[104:107]\n" \
    "ds_read_b64_tr_b16 v[108:109], v212 offset:6208\n" \
    "v_exp_f32 v44, v44\n" \
    "v_mfma_f32_16x16x32_bf16 a[108:111], v[168:171], v[184:187], a[108:111]\n" \
    "v_exp_f32 v62, v45\n" \
    "v_mov_b32_sdwa v61, v42 dst_sel:WORD_0 dst_unused:UNUSED_PRESERVE src0_sel:WORD_1\n" \
    "v_mfma_f32_16x16x32_bf16 a[108:111], v[172:175], v[188:191], a[108:111]\n" \
    "buffer_load_dwordx4 v[184:187], v8, s[48:51], s64 offen offset:0 sc1\n" \
    "buffer_load_dwordx4 v[188:191], v8, s[48:51], s64 offen offset:1024 sc1\n" \
    "v_exp_f32 v46, v46\n" \
    "v_exp_f32 v63, v47\n" \
    "v_mov_b32_sdwa v62, v44 dst_sel:WORD_0 dst_unused:UNUSED_PRESERVE src0_sel:WORD_1\n" \
    "v_mov_b32_sdwa v63, v46 dst_sel:WORD_0 dst_unused:UNUSED_PRESERVE src0_sel:WORD_1\n" \
    "v_mfma_f32_32x32x16_bf16 a[48:63], v[68:71], v[56:59], a[48:63]\n" \
    "ds_read_b64_tr_b16 v[110:111], v212 offset:9280\n" \
    "v_exp_f32 v16, v16\n" \
    "v_exp_f32 v48, v17\n" \
    "v_exp_f32 v18, v18\n" \
    "v_mfma_f32_32x32x16_bf16 a[48:63], v[72:75], v[60:63], a[48:63]\n" \
    "ds_read_b128 v[68:71], v210 offset:7680\n" \
    "v_exp_f32 v49, v19\n" \
    "v_exp_f32 v20, v20\n" \
    "v_mov_b32_sdwa v48, v16 dst_sel:WORD_0 dst_unused:UNUSED_PRESERVE src0_sel:WORD_1\n" \
    "v_mfma_f32_32x32x8_bf16 v[32:47], v[76:77], a[210:211], 0\n" \
    "ds_read_b128 v[72:75], v210 offset:8320\n" \
    "ds_read_b64_tr_b16 v[112:113], v211 offset:128\n" \
    "v_exp_f32 v50, v21\n" \
    "v_mov_b32_sdwa v49, v18 dst_sel:WORD_0 dst_unused:UNUSED_PRESERVE src0_sel:WORD_1\n" \
    "v_exp_f32 v22, v22\n" \
    "v_mov_b32_sdwa v50, v20 dst_sel:WORD_0 dst_unused:UNUSED_PRESERVE src0_sel:WORD_1\n" \
    "v_mfma_f32_16x16x32_bf16 a[112:115], v[128:131], v[192:195], a[112:115]\n" \
    "v_exp_f32 v51, v23\n" \
    "v_mfma_f32_16x16x32_bf16 a[112:115], v[132:135], v[196:199], a[112:115]\n" \
    "v_exp_f32 v24, v24\n" \
    "v_mfma_f32_32x32x16_bf16 v[32:47], v[64:67], a[196:199], v[32:47]\n" \
    "v_exp_f32 v52, v25\n" \
    "v_mov_b32_sdwa v51, v22 dst_sel:WORD_0 dst_unused:UNUSED_PRESERVE src0_sel:WORD_1\n" \
    "v_exp_f32 v26, v26\n" \
    "v_mov_b32_sdwa v52, v24 dst_sel:WORD_0 dst_unused:UNUSED_PRESERVE src0_sel:WORD_1\n" \
    "v_mfma_f32_16x16x32_bf16 a[116:119], v[136:139], v[192:195], a[116:119]\n" \
    "v_exp_f32 v53, v27\n" \
    "v_mfma_f32_16x16x32_bf16 a[116:119], v[140:143], v[196:199], a[116:119]\n" \
    "ds_read_b64_tr_b16 v[114:115], v211 offset:3200\n" \
    "v_exp_f32 v28, v28\n" \
    "v_mfma_f32_16x16x32_bf16 a[120:123], v[144:147], v[192:195], a[120:123]\n" \
    "v_exp_f32 v54, v29\n" \
    "v_mov_b32_sdwa v53, v26 dst_sel:WORD_0 dst_unused:UNUSED_PRESERVE src0_sel:WORD_1\n" \
    "v_mfma_f32_16x16x32_bf16 a[120:123], v[148:151], v[196:199], a[120:123]\n" \
    "v_exp_f32 v30, v30\n" \
    "v_exp_f32 v55, v31\n" \
    "v_mov_b32_sdwa v54, v28 dst_sel:WORD_0 dst_unused:UNUSED_PRESERVE src0_sel:WORD_1\n" \
    "v_mov_b32_sdwa v55, v30 dst_sel:WORD_0 dst_unused:UNUSED_PRESERVE src0_sel:WORD_1\n" \
    "s_waitcnt vmcnt(7) lgkmcnt(2)\n" \
    "v_mfma_f32_32x32x16_bf16 a[0:15], v[68:71], v[48:51], a[0:15]\n" \
    "ds_read_b64_tr_b16 v[116:117], v211 offset:6272\n" \
    "v_exp_f32 v32, v32\n" \
    "v_exp_f32 v56, v33\n" \
    "v_exp_f32 v34, v34\n" \
    "v_mfma_f32_32x32x16_bf16 a[0:15], v[72:75], v[52:55], a[0:15]\n" \
    "v_exp_f32 v57, v35\n" \
    "v_exp_f32 v36, v36\n" \
    "v_mov_b32_sdwa v56, v32 dst_sel:WORD_0 dst_unused:UNUSED_PRESERVE src0_sel:WORD_1\n" \
    "v_mfma_f32_32x32x8_bf16 v[16:31], v[76:77], a[212:213], 0\n" \
    "ds_read_b64_tr_b16 v[118:119], v211 offset:9344\n" \
    "v_exp_f32 v58, v37\n" \
    "v_mov_b32_sdwa v57, v34 dst_sel:WORD_0 dst_unused:UNUSED_PRESERVE src0_sel:WORD_1\n" \
    "v_add_u32 v210, s66, v219\n" \
    "v_exp_f32 v38, v38\n" \
    "v_mov_b32_sdwa v58, v36 dst_sel:WORD_0 dst_unused:UNUSED_PRESERVE src0_sel:WORD_1\n" \
    "v_mfma_f32_16x16x32_bf16 a[124:127], v[152:155], v[192:195], a[124:127]\n" \
    "v_exp_f32 v59, v39\n" \
    "v_mfma_f32_16x16x32_bf16 a[124:127], v[156:159], v[196:199], a[124:127]\n" \
    "v_exp_f32 v40, v40\n" \
    "v_mfma_f32_32x32x16_bf16 v[16:31], v[64:67], a[200:203], v[16:31]\n" \
    "v_exp_f32 v60, v41\n" \
    "v_mov_b32_sdwa v59, v38 dst_sel:WORD_0 dst_unused:UNUSED_PRESERVE src0_sel:WORD_1\n" \
    "v_exp_f32 v42, v42\n" \
    "v_mov_b32_sdwa v60, v40 dst_sel:WORD_0 dst_unused:UNUSED_PRESERVE src0_sel:WORD_1\n" \
    "v_mfma_f32_16x16x32_bf16 a[128:131], v[160:163], v[192:195], a[128:131]\n" \
    "v_exp_f32 v61, v43\n" \
    "v_mfma_f32_16x16x32_bf16 a[128:131], v[164:167], v[196:199], a[128:131]\n" \
    "ds_read_b64_tr_b16 v[120:121], v212 offset:128\n" \
    "v_exp_f32 v44, v44\n" \
    "v_mfma_f32_16x16x32_bf16 a[132:135], v[168:171], v[192:195], a[132:135]\n" \
    "v_exp_f32 v62, v45\n" \
    "v_mov_b32_sdwa v61, v42 dst_sel:WORD_0 dst_unused:UNUSED_PRESERVE src0_sel:WORD_1\n" \
    "v_mfma_f32_16x16x32_bf16 a[132:135], v[172:175], v[196:199], a[132:135]\n" \
    "buffer_load_dwordx4 v[192:195], v8, s[52:55], s64 offen offset:0 sc1\n" \
    "buffer_load_dwordx4 v[196:199], v8, s[52:55], s64 offen offset:1024 sc1\n" \
    "v_exp_f32 v46, v46\n" \
    "v_exp_f32 v63, v47\n" \
    "v_mov_b32_sdwa v62, v44 dst_sel:WORD_0 dst_unused:UNUSED_PRESERVE src0_sel:WORD_1\n" \
    "v_mov_b32_sdwa v63, v46 dst_sel:WORD_0 dst_unused:UNUSED_PRESERVE src0_sel:WORD_1\n" \
    "v_mfma_f32_32x32x16_bf16 a[16:31], v[68:71], v[56:59], a[16:31]\n" \
    "ds_read_b64_tr_b16 v[122:123], v212 offset:3200\n" \
    "v_exp_f32 v16, v16\n" \
    "v_exp_f32 v48, v17\n" \
    "v_exp_f32 v18, v18\n" \
    "v_mfma_f32_32x32x16_bf16 a[16:31], v[72:75], v[60:63], a[16:31]\n" \
    "v_exp_f32 v49, v19\n" \
    "v_exp_f32 v20, v20\n" \
    "v_mov_b32_sdwa v48, v16 dst_sel:WORD_0 dst_unused:UNUSED_PRESERVE src0_sel:WORD_1\n" \
    "v_mfma_f32_32x32x8_bf16 v[32:47], v[76:77], a[214:215], 0\n" \
    "ds_read_b64_tr_b16 v[124:125], v212 offset:6272\n" \
    "v_exp_f32 v50, v21\n" \
    "v_mov_b32_sdwa v49, v18 dst_sel:WORD_0 dst_unused:UNUSED_PRESERVE src0_sel:WORD_1\n" \
    "v_add_u32 v208, s66, v217\n" \
    "v_add_u32 v209, s66, v218\n" \
    "v_exp_f32 v22, v22\n" \
    "v_mov_b32_sdwa v50, v20 dst_sel:WORD_0 dst_unused:UNUSED_PRESERVE src0_sel:WORD_1\n" \
    "v_mfma_f32_16x16x32_bf16 a[136:139], v[128:131], v[200:203], a[136:139]\n" \
    "v_exp_f32 v51, v23\n" \
    "v_mfma_f32_16x16x32_bf16 a[136:139], v[132:135], v[204:207], a[136:139]\n" \
    "v_exp_f32 v24, v24\n" \
    "v_mfma_f32_32x32x16_bf16 v[32:47], v[64:67], a[204:207], v[32:47]\n" \
    "ds_read_b128 v[64:67], v208 offset:0\n" \
    "ds_read_b32 v76, v209 offset:0\n" \
    "v_exp_f32 v52, v25\n" \
    "v_mov_b32_sdwa v51, v22 dst_sel:WORD_0 dst_unused:UNUSED_PRESERVE src0_sel:WORD_1\n" \
    "v_exp_f32 v26, v26\n" \
    "v_mov_b32_sdwa v52, v24 dst_sel:WORD_0 dst_unused:UNUSED_PRESERVE src0_sel:WORD_1\n" \
    "v_mfma_f32_16x16x32_bf16 a[140:143], v[136:139], v[200:203], a[140:143]\n" \
    "v_exp_f32 v53, v27\n" \
    "v_mfma_f32_16x16x32_bf16 a[140:143], v[140:143], v[204:207], a[140:143]\n" \
    "ds_read_b64_tr_b16 v[126:127], v212 offset:9344\n" \
    "v_exp_f32 v28, v28\n" \
    "v_mfma_f32_16x16x32_bf16 a[144:147], v[144:147], v[200:203], a[144:147]\n" \
    "v_exp_f32 v54, v29\n" \
    "v_mov_b32_sdwa v53, v26 dst_sel:WORD_0 dst_unused:UNUSED_PRESERVE src0_sel:WORD_1\n" \
    "v_mfma_f32_16x16x32_bf16 a[144:147], v[148:151], v[204:207], a[144:147]\n" \
    "v_exp_f32 v30, v30\n" \
    "v_exp_f32 v55, v31\n" \
    "v_mov_b32_sdwa v54, v28 dst_sel:WORD_0 dst_unused:UNUSED_PRESERVE src0_sel:WORD_1\n" \
    "v_mov_b32_sdwa v55, v30 dst_sel:WORD_0 dst_unused:UNUSED_PRESERVE src0_sel:WORD_1\n" \
    "v_mfma_f32_32x32x16_bf16 a[32:47], v[68:71], v[48:51], a[32:47]\n" \
    "v_exp_f32 v32, v32\n" \
    "v_exp_f32 v56, v33\n" \
    "v_exp_f32 v34, v34\n" \
    "v_mfma_f32_32x32x16_bf16 a[32:47], v[72:75], v[52:55], a[32:47]\n" \
    "v_exp_f32 v57, v35\n" \
    "v_exp_f32 v36, v36\n" \
    "v_mov_b32_sdwa v56, v32 dst_sel:WORD_0 dst_unused:UNUSED_PRESERVE src0_sel:WORD_1\n" \
    "s_waitcnt vmcnt(5) lgkmcnt(0)\n" \
    "v_mfma_f32_32x32x8_bf16 v[16:31], v[76:77], a[208:209], 0\n" \
    "v_exp_f32 v58, v37\n" \
    "v_mov_b32_sdwa v57, v34 dst_sel:WORD_0 dst_unused:UNUSED_PRESERVE src0_sel:WORD_1\n" \
    "s_mov_b32 s66, s97\n" \
    "s_add_u32 s97, s97, 45056\n" \
    "s_cmp_eq_u32 s97, 135168\n" \
    "s_cselect_b32 s97, 0, s97\n" \
    "v_exp_f32 v38, v38\n" \
    "v_mov_b32_sdwa v58, v36 dst_sel:WORD_0 dst_unused:UNUSED_PRESERVE src0_sel:WORD_1\n" \
    "v_mfma_f32_16x16x32_bf16 a[148:151], v[152:155], v[200:203], a[148:151]\n" \
    "v_exp_f32 v59, v39\n" \
    "v_mfma_f32_16x16x32_bf16 a[148:151], v[156:159], v[204:207], a[148:151]\n" \
    "v_exp_f32 v40, v40\n" \
    "v_mfma_f32_32x32x16_bf16 v[16:31], v[64:67], a[192:195], v[16:31]\n" \
    "v_exp_f32 v60, v41\n" \
    "v_mov_b32_sdwa v59, v38 dst_sel:WORD_0 dst_unused:UNUSED_PRESERVE src0_sel:WORD_1\n" \
    "v_exp_f32 v42, v42\n" \
    "v_mov_b32_sdwa v60, v40 dst_sel:WORD_0 dst_unused:UNUSED_PRESERVE src0_sel:WORD_1\n" \
    "v_mfma_f32_16x16x32_bf16 a[152:155], v[160:163], v[200:203], a[152:155]\n" \
    "v_exp_f32 v61, v43\n" \
    "v_mfma_f32_16x16x32_bf16 a[152:155], v[164:167], v[204:207], a[152:155]\n" \
    "v_exp_f32 v44, v44\n" \
    "v_mfma_f32_16x16x32_bf16 a[156:159], v[168:171], v[200:203], a[156:159]\n" \
    "v_exp_f32 v62, v45\n" \
    "v_mov_b32_sdwa v61, v42 dst_sel:WORD_0 dst_unused:UNUSED_PRESERVE src0_sel:WORD_1\n" \
    "v_mfma_f32_16x16x32_bf16 a[156:159], v[172:175], v[204:207], a[156:159]\n" \
    "buffer_load_dwordx4 v[200:203], v8, s[56:59], s64 offen offset:0 sc1\n" \
    "buffer_load_dwordx4 v[204:207], v8, s[56:59], s64 offen offset:1024 sc1\n" \
    "s_add_u32 s64, s64, 10240\n" \
    "v_exp_f32 v46, v46\n" \
    "v_exp_f32 v63, v47\n" \
    "v_mov_b32_sdwa v62, v44 dst_sel:WORD_0 dst_unused:UNUSED_PRESERVE src0_sel:WORD_1\n" \
    "v_mov_b32_sdwa v63, v46 dst_sel:WORD_0 dst_unused:UNUSED_PRESERVE src0_sel:WORD_1\n" \
    "s_cmp_eq_u32 s65, 4\n" \
    "s_cbranch_scc1 F2_CHK%=\n" \
    "F2_OK%=:\n" \
    "s_sub_u32 s65, s65, 1\n" \
    "s_cmp_lg_u32 s65, 0\n" \
    "s_waitcnt vmcnt(6)\n" \
    "s_barrier\n" \
    "s_cbranch_scc1 K2_LOOP%=\n" \
    "s_lshl_b32 s70, %6, 2\n" \
    "s_mul_i32 s67, s70, 18432\n" \
    "s_mul_i32 s69, s70, 3072\n" \
    "s_add_u32 s69, s69, %7\n" \
    "s_add_u32 s69, s69, 90112\n" \
    "v_mfma_f32_32x32x16_bf16 a[48:63], v[68:71], v[56:59], a[48:63]\n" \
    "ds_read_b64_tr_b16 v[128:129], v211 offset:12288\n" \
    "v_exp_f32 v16, v16\n" \
    "v_exp_f32 v48, v17\n" \
    "v_exp_f32 v18, v18\n" \
    "v_mfma_f32_32x32x16_bf16 a[48:63], v[72:75], v[60:63], a[48:63]\n" \
    "ds_read_b128 v[68:71], v210 offset:0\n" \
    "v_exp_f32 v49, v19\n" \
    "v_exp_f32 v20, v20\n" \
    "v_mov_b32_sdwa v48, v16 dst_sel:WORD_0 dst_unused:UNUSED_PRESERVE src0_sel:WORD_1\n" \
    "v_mfma_f32_32x32x8_bf16 v[32:47], v[76:77], a[210:211], 0\n" \
    "ds_read_b128 v[72:75], v210 offset:640\n" \
    "ds_read_b64_tr_b16 v[130:131], v211 offset:15360\n" \
    "v_exp_f32 v50, v21\n" \
    "v_mov_b32_sdwa v49, v18 dst_sel:WORD_0 dst_unused:UNUSED_PRESERVE src0_sel:WORD_1\n" \
    "s_mov_b32 m0, s69\n" \
    "v_exp_f32 v22, v22\n" \
    "v_mov_b32_sdwa v50, v20 dst_sel:WORD_0 dst_unused:UNUSED_PRESERVE src0_sel:WORD_1\n" \
    "v_mfma_f32_16x16x32_bf16 a[64:67], v[80:83], v[176:179], a[64:67]\n" \
    "v_exp_f32 v51, v23\n" \
    "v_mfma_f32_16x16x32_bf16 a[64:67], v[84:87], v[180:183], a[64:67]\n" \
    "v_exp_f32 v24, v24\n" \
    "v_mfma_f32_32x32x16_bf16 v[32:47], v[64:67], a[196:199], v[32:47]\n" \
    "buffer_load_dwordx4 v11, s[60:63], s67 offen lds\n" \
    "ds_read_b64_tr_b16 v[132:133], v211 offset:18432\n" \
    "v_exp_f32 v52, v25\n" \
    "v_mov_b32_sdwa v51, v22 dst_sel:WORD_0 dst_unused:UNUSED_PRESERVE src0_sel:WORD_1\n" \
    "v_exp_f32 v26, v26\n" \
    "v_mov_b32_sdwa v52, v24 dst_sel:WORD_0 dst_unused:UNUSED_PRESERVE src0_sel:WORD_1\n" \
    "v_mfma_f32_16x16x32_bf16 a[68:71], v[88:91], v[176:179], a[68:71]\n" \
    "v_exp_f32 v53, v27\n" \
    "v_mfma_f32_16x16x32_bf16 a[68:71], v[92:95], v[180:183], a[68:71]\n" \
    "ds_read_b64_tr_b16 v[134:135], v211 offset:21504\n" \
    "v_exp_f32 v28, v28\n" \
    "v_mfma_f32_16x16x32_bf16 a[72:75], v[96:99], v[176:179], a[72:75]\n" \
    "v_exp_f32 v54, v29\n" \
    "v_mov_b32_sdwa v53, v26 dst_sel:WORD_0 dst_unused:UNUSED_PRESERVE src0_sel:WORD_1\n" \
    "v_mfma_f32_16x16x32_bf16 a[72:75], v[100:103], v[180:183], a[72:75]\n" \
    "v_exp_f32 v30, v30\n" \
    "v_exp_f32 v55, v31\n" \
    "v_mov_b32_sdwa v54, v28 dst_sel:WORD_0 dst_unused:UNUSED_PRESERVE src0_sel:WORD_1\n" \
    "v_mov_b32_sdwa v55, v30 dst_sel:WORD_0 dst_unused:UNUSED_PRESERVE src0_sel:WORD_1\n" \
    "s_waitcnt vmcnt(5) lgkmcnt(3)\n" \
    "v_mfma_f32_32x32x16_bf16 a[0:15], v[68:71], v[48:51], a[0:15]\n" \
    "ds_read_b64_tr_b16 v[136:137], v212 offset:12288\n" \
    "v_exp_f32 v32, v32\n" \
    "v_exp_f32 v56, v33\n" \
    "v_exp_f32 v34, v34\n" \
    "v_mfma_f32_32x32x16_bf16 a[0:15], v[72:75], v[52:55], a[0:15]\n" \
    "buffer_load_dwordx4 v12, s[60:63], s67 offen offset:1024 lds\n" \
    "s_add_u32 s69, s69, 0xc00\n" \
    "v_exp_f32 v57, v35\n" \
    "v_exp_f32 v36, v36\n" \
    "v_mov_b32_sdwa v56, v32 dst_sel:WORD_0 dst_unused:UNUSED_PRESERVE src0_sel:WORD_1\n" \
    "v_mfma_f32_32x32x8_bf16 v[16:31], v[76:77], a[212:213], 0\n" \
    "ds_read_b64_tr_b16 v[138:139], v212 offset:15360\n" \
    "v_exp_f32 v58, v37\n" \
    "v_mov_b32_sdwa v57, v34 dst_sel:WORD_0 dst_unused:UNUSED_PRESERVE src0_sel:WORD_1\n" \
    "v_exp_f32 v38, v38\n" \
    "v_mov_b32_sdwa v58, v36 dst_sel:WORD_0 dst_unused:UNUSED_PRESERVE src0_sel:WORD_1\n" \
    "v_mfma_f32_16x16x32_bf16 a[76:79], v[104:107], v[176:179], a[76:79]\n" \
    "v_exp_f32 v59, v39\n" \
    "v_mfma_f32_16x16x32_bf16 a[76:79], v[108:111], v[180:183], a[76:79]\n" \
    "v_exp_f32 v40, v40\n" \
    "v_mfma_f32_32x32x16_bf16 v[16:31], v[64:67], a[200:203], v[16:31]\n" \
    "ds_read_b64_tr_b16 v[140:141], v212 offset:18432\n" \
    "v_exp_f32 v60, v41\n" \
    "v_mov_b32_sdwa v59, v38 dst_sel:WORD_0 dst_unused:UNUSED_PRESERVE src0_sel:WORD_1\n" \
    "v_exp_f32 v42, v42\n" \
    "v_mov_b32_sdwa v60, v40 dst_sel:WORD_0 dst_unused:UNUSED_PRESERVE src0_sel:WORD_1\n" \
    "v_mfma_f32_16x16x32_bf16 a[80:83], v[112:115], v[176:179], a[80:83]\n" \
    "v_exp_f32 v61, v43\n" \
    "v_mfma_f32_16x16x32_bf16 a[80:83], v[116:119], v[180:183], a[80:83]\n" \
    "ds_read_b64_tr_b16 v[142:143], v212 offset:21504\n" \
    "v_exp_f32 v44, v44\n" \
    "v_mfma_f32_16x16x32_bf16 a[84:87], v[120:123], v[176:179], a[84:87]\n" \
    "v_exp_f32 v62, v45\n" \
    "v_mov_b32_sdwa v61, v42 dst_sel:WORD_0 dst_unused:UNUSED_PRESERVE src0_sel:WORD_1\n" \
    "v_mfma_f32_16x16x32_bf16 a[84:87], v[124:127], v[180:183], a[84:87]\n" \
    "buffer_load_dwordx4 v[176:179], v8, s[44:47], s64 offen offset:0 sc1\n" \
    "buffer_load_dwordx4 v[180:183], v8, s[44:47], s64 offen offset:1024 sc1\n" \
    "v_exp_f32 v46, v46\n" \
    "v_exp_f32 v63, v47\n" \
    "v_mov_b32_sdwa v62, v44 dst_sel:WORD_0 dst_unused:UNUSED_PRESERVE src0_sel:WORD_1\n" \
    "v_mov_b32_sdwa v63, v46 dst_sel:WORD_0 dst_unused:UNUSED_PRESERVE src0_sel:WORD_1\n" \
    "v_mfma_f32_32x32x16_bf16 a[16:31], v[68:71], v[56:59], a[16:31]\n" \
    "ds_read_b64_tr_b16 v[144:145], v211 offset:12352\n" \
    "v_exp_f32 v16, v16\n" \
    "v_exp_f32 v48, v17\n" \
    "v_exp_f32 v18, v18\n" \
    "v_mfma_f32_32x32x16_bf16 a[16:31], v[72:75], v[60:63], a[16:31]\n" \
    "buffer_load_dwordx4 v13, s[60:63], s67 offen offset:2048 lds\n" \
    "s_add_u32 s67, s67, 18432\n" \
    "v_exp_f32 v49, v19\n" \
    "v_exp_f32 v20, v20\n" \
    "v_mov_b32_sdwa v48, v16 dst_sel:WORD_0 dst_unused:UNUSED_PRESERVE src0_sel:WORD_1\n" \
    "v_mfma_f32_32x32x8_bf16 v[32:47], v[76:77], a[214:215], 0\n" \
    "ds_read_b64_tr_b16 v[146:147], v211 offset:15424\n" \
    "v_exp_f32 v50, v21\n" \
    "v_mov_b32_sdwa v49, v18 dst_sel:WORD_0 dst_unused:UNUSED_PRESERVE src0_sel:WORD_1\n" \
    "v_exp_f32 v22, v22\n" \
    "v_mov_b32_sdwa v50, v20 dst_sel:WORD_0 dst_unused:UNUSED_PRESERVE src0_sel:WORD_1\n" \
    "v_mfma_f32_16x16x32_bf16 a[88:91], v[80:83], v[184:187], a[88:91]\n" \
    "v_exp_f32 v51, v23\n" \
    "v_mfma_f32_16x16x32_bf16 a[88:91], v[84:87], v[188:191], a[88:91]\n" \
    "v_exp_f32 v24, v24\n" \
    "v_mfma_f32_32x32x16_bf16 v[32:47], v[64:67], a[204:207], v[32:47]\n" \
    "ds_read_b128 v[64:67], v208 offset:2560\n" \
    "ds_read_b32 v76, v209 offset:2560\n" \
    "ds_read_b64_tr_b16 v[148:149], v211 offset:18496\n" \
    "v_exp_f32 v52, v25\n" \
    "v_mov_b32_sdwa v51, v22 dst_sel:WORD_0 dst_unused:UNUSED_PRESERVE src0_sel:WORD_1\n" \
    "v_exp_f32 v26, v26\n" \
    "v_mov_b32_sdwa v52, v24 dst_sel:WORD_0 dst_unused:UNUSED_PRESERVE src0_sel:WORD_1\n" \
    "v_mfma_f32_16x16x32_bf16 a[92:95], v[88:91], v[184:187], a[92:95]\n" \
    "v_exp_f32 v53, v27\n" \
    "v_mfma_f32_16x16x32_bf16 a[92:95], v[92:95], v[188:191], a[92:95]\n" \
    "ds_read_b64_tr_b16 v[150:151], v211 offset:21568\n" \
    "v_exp_f32 v28, v28\n" \
    "v_mfma_f32_16x16x32_bf16 a[96:99], v[96:99], v[184:187], a[96:99]\n" \
    "v_exp_f32 v54, v29\n" \
    "v_mov_b32_sdwa v53, v26 dst_sel:WORD_0 dst_unused:UNUSED_PRESERVE src0_sel:WORD_1\n" \
    "v_mfma_f32_16x16x32_bf16 a[96:99], v[100:103], v[188:191], a[96:99]\n" \
    "v_exp_f32 v30, v30\n" \
    "v_exp_f32 v55, v31\n" \
    "v_mov_b32_sdwa v54, v28 dst_sel:WORD_0 dst_unused:UNUSED_PRESERVE src0_sel:WORD_1\n" \
    "v_mov_b32_sdwa v55, v30 dst_sel:WORD_0 dst_unused:UNUSED_PRESERVE src0_sel:WORD_1\n" \
    "v_mfma_f32_32x32x16_bf16 a[32:47], v[68:71], v[48:51], a[32:47]\n" \
    "ds_read_b64_tr_b16 v[152:153], v212 offset:12352\n" \
    "v_exp_f32 v32, v32\n" \
    "v_exp_f32 v56, v33\n" \
    "s_mov_b32 m0, s69\n" \
    "v_exp_f32 v34, v34\n" \
    "v_mfma_f32_32x32x16_bf16 a[32:47], v[72:75], v[52:55], a[32:47]\n" \
    "buffer_load_dwordx4 v11, s[60:63], s67 offen lds\n" \
    "v_exp_f32 v57, v35\n" \
    "v_exp_f32 v36, v36\n" \
    "v_mov_b32_sdwa v56, v32 dst_sel:WORD_0 dst_unused:UNUSED_PRESERVE src0_sel:WORD_1\n" \
    "s_waitcnt vmcnt(8) lgkmcnt(3)\n" \
    "v_mfma_f32_32x32x8_bf16 v[16:31], v[76:77], a[208:209], 0\n" \
    "ds_read_b64_tr_b16 v[154:155], v212 offset:15424\n" \
    "v_exp_f32 v58, v37\n" \
    "v_mov_b32_sdwa v57, v34 dst_sel:WORD_0 dst_unused:UNUSED_PRESERVE src0_sel:WORD_1\n" \
    "v_exp_f32 v38, v38\n" \
    "v_mov_b32_sdwa v58, v36 dst_sel:WORD_0 dst_unused:UNUSED_PRESERVE src0_sel:WORD_1\n" \
    "v_mfma_f32_16x16x32_bf16 a[100:103], v[104:107], v[184:187], a[100:103]\n" \
    "v_exp_f32 v59, v39\n" \
    "v_mfma_f32_16x16x32_bf16 a[100:103], v[108:111], v[188:191], a[100:103]\n" \
    "v_exp_f32 v40, v40\n" \
    "v_mfma_f32_32x32x16_bf16 v[16:31], v[64:67], a[192:195], v[16:31]\n" \
    "v_exp_f32 v60, v41\n" \
    "v_mov_b32_sdwa v59, v38 dst_sel:WORD_0 dst_unused:UNUSED_PRESERVE src0_sel:WORD_1\n" \
    "v_exp_f32 v42, v42\n" \
    "v_mov_b32_sdwa v60, v40 dst_sel:WORD_0 dst_unused:UNUSED_PRESERVE src0_sel:WORD_1\n" \
    "v_mfma_f32_16x16x32_bf16 a[104:107], v[112:115], v[184:187], a[104:107]\n" \
    "v_exp_f32 v61, v43\n" \
    "v_mfma_f32_16x16x32_bf16 a[104:107], v[116:119], v[188:191], a[104:107]\n" \
    "ds_read_b64_tr_b16 v[156:157], v212 offset:18496\n" \
    "v_exp_f32 v44, v44\n" \
    "v_mfma_f32_16x16x32_bf16 a[108:111], v[120:123], v[184:187], a[108:111]\n" \
    "v_exp_f32 v62, v45\n" \
    "v_mov_b32_sdwa v61, v42 dst_sel:WORD_0 dst_unused:UNUSED_PRESERVE src0_sel:WORD_1\n" \
    "v_mfma_f32_16x16x32_bf16 a[108:111], v[124:127], v[188:191], a[108:111]\n" \
    "buffer_load_dwordx4 v[184:187], v8, s[48:51], s64 offen offset:0 sc1\n" \
    "buffer_load_dwordx4 v[188:191], v8, s[48:51], s64 offen offset:1024 sc1\n" \
    "s_mov_b64 s[72:73], %15\n" \
    "s_sub_u32 s72, s72, 41951232\n" \
    "s_or_b32 s72, s72, %6\n" \
    "s_cmp_lg_u32 s72, 0\n" \
    "s_cbranch_scc1 T_BUMP%=\n" \
    "s_mov_b32 s72, 41943040\n" \
    "v_mov_b64 v[226:227], %0\n" \
    "s_mov_b64 exec, 1\n" \
    "buffer_store_dword v226, off, s[40:43], s72 sc1\n" \
    "s_mov_b64 exec, -1\n" \
    "T_BUMP%=:\n" \
    "v_exp_f32 v46, v46\n" \
    "v_exp_f32 v63, v47\n" \
    "v_mov_b32_sdwa v62, v44 dst_sel:WORD_0 dst_unused:UNUSED_PRESERVE src0_sel:WORD_1\n" \
    "v_mov_b32_sdwa v63, v46 dst_sel:WORD_0 dst_unused:UNUSED_PRESERVE src0_sel:WORD_1\n" \
    "v_mfma_f32_32x32x16_bf16 a[48:63], v[68:71], v[56:59], a[48:63]\n" \
    "ds_read_b64_tr_b16 v[158:159], v212 offset:21568\n" \
    "v_exp_f32 v16, v16\n" \
    "v_exp_f32 v48, v17\n" \
    "v_exp_f32 v18, v18\n" \
    "v_mfma_f32_32x32x16_bf16 a[48:63], v[72:75], v[60:63], a[48:63]\n" \
    "ds_read_b128 v[68:71], v210 offset:2560\n" \
    "v_exp_f32 v49, v19\n" \
    "v_exp_f32 v20, v20\n" \
    "v_mov_b32_sdwa v48, v16 dst_sel:WORD_0 dst_unused:UNUSED_PRESERVE src0_sel:WORD_1\n" \
    "v_mfma_f32_32x32x8_bf16 v[32:47], v[76:77], a[210:211], 0\n" \
    "ds_read_b128 v[72:75], v210 offset:3200\n" \
    "ds_read_b64_tr_b16 v[160:161], v211 offset:12416\n" \
    "v_exp_f32 v50, v21\n" \
    "v_mov_b32_sdwa v49, v18 dst_sel:WORD_0 dst_unused:UNUSED_PRESERVE src0_sel:WORD_1\n" \
    "v_exp_f32 v22, v22\n" \
    "v_mov_b32_sdwa v50, v20 dst_sel:WORD_0 dst_unused:UNUSED_PRESERVE src0_sel:WORD_1\n" \
    "v_mfma_f32_16x16x32_bf16 a[112:115], v[80:83], v[192:195], a[112:115]\n" \
    "v_exp_f32 v51, v23\n" \
    "v_mfma_f32_16x16x32_bf16 a[112:115], v[84:87], v[196:199], a[112:115]\n" \
    "v_exp_f32 v24, v24\n" \
    "v_mfma_f32_32x32x16_bf16 v[32:47], v[64:67], a[196:199], v[32:47]\n" \
    "buffer_load_dwordx4 v12, s[60:63], s67 offen offset:1024 lds\n" \
    "s_add_u32 s69, s69, 0xc00\n" \
    "v_exp_f32 v52, v25\n" \
    "v_mov_b32_sdwa v51, v22 dst_sel:WORD_0 dst_unused:UNUSED_PRESERVE src0_sel:WORD_1\n" \
    "v_exp_f32 v26, v26\n" \
    "v_mov_b32_sdwa v52, v24 dst_sel:WORD_0 dst_unused:UNUSED_PRESERVE src0_sel:WORD_1\n" \
    "v_mfma_f32_16x16x32_bf16 a[116:119], v[88:91], v[192:195], a[116:119]\n" \
    "v_exp_f32 v53, v27\n" \
    "v_mfma_f32_16x16x32_bf16 a[116:119], v[92:95], v[196:199], a[116:119]\n" \
    "ds_read_b64_tr_b16 v[162:163], v211 offset:15488\n" \
    "v_exp_f32 v28, v28\n" \
    "v_mfma_f32_16x16x32_bf16 a[120:123], v[96:99], v[192:195], a[120:123]\n" \
    "v_exp_f32 v54, v29\n" \
    "v_mov_b32_sdwa v53, v26 dst_sel:WORD_0 dst_unused:UNUSED_PRESERVE src0_sel:WORD_1\n" \
    "v_mfma_f32_16x16x32_bf16 a[120:123], v[100:103], v[196:199], a[120:123]\n" \
    "v_exp_f32 v30, v30\n" \
    "v_exp_f32 v55, v31\n" \
    "v_mov_b32_sdwa v54, v28 dst_sel:WORD_0 dst_unused:UNUSED_PRESERVE src0_sel:WORD_1\n" \
    "v_mov_b32_sdwa v55, v30 dst_sel:WORD_0 dst_unused:UNUSED_PRESERVE src0_sel:WORD_1\n" \
    "s_waitcnt vmcnt(9) lgkmcnt(2)\n" \
    "v_mfma_f32_32x32x16_bf16 a[0:15], v[68:71], v[48:51], a[0:15]\n" \
    "ds_read_b64_tr_b16 v[164:165], v211 offset:18560\n" \
    "v_exp_f32 v32, v32\n" \
    "v_exp_f32 v56, v33\n" \
    "v_exp_f32 v34, v34\n" \
    "v_mfma_f32_32x32x16_bf16 a[0:15], v[72:75], v[52:55], a[0:15]\n" \
    "buffer_load_dwordx4 v13, s[60:63], s67 offen offset:2048 lds\n" \
    "s_add_u32 s67, s67, 18432\n" \
    "v_exp_f32 v57, v35\n" \
    "v_exp_f32 v36, v36\n" \
    "v_mov_b32_sdwa v56, v32 dst_sel:WORD_0 dst_unused:UNUSED_PRESERVE src0_sel:WORD_1\n" \
    "v_mfma_f32_32x32x8_bf16 v[16:31], v[76:77], a[212:213], 0\n" \
    "ds_read_b64_tr_b16 v[166:167], v211 offset:21632\n" \
    "v_exp_f32 v58, v37\n" \
    "v_mov_b32_sdwa v57, v34 dst_sel:WORD_0 dst_unused:UNUSED_PRESERVE src0_sel:WORD_1\n" \
    "v_exp_f32 v38, v38\n" \
    "v_mov_b32_sdwa v58, v36 dst_sel:WORD_0 dst_unused:UNUSED_PRESERVE src0_sel:WORD_1\n" \
    "v_mfma_f32_16x16x32_bf16 a[124:127], v[104:107], v[192:195], a[124:127]\n" \
    "v_exp_f32 v59, v39\n" \
    "v_mfma_f32_16x16x32_bf16 a[124:127], v[108:111], v[196:199], a[124:127]\n" \
    "v_exp_f32 v40, v40\n" \
    "v_mfma_f32_32x32x16_bf16 v[16:31], v[64:67], a[200:203], v[16:31]\n" \
    "v_exp_f32 v60, v41\n" \
    "v_mov_b32_sdwa v59, v38 dst_sel:WORD_0 dst_unused:UNUSED_PRESERVE src0_sel:WORD_1\n" \
    "v_exp_f32 v42, v42\n" \
    "v_mov_b32_sdwa v60, v40 dst_sel:WORD_0 dst_unused:UNUSED_PRESERVE src0_sel:WORD_1\n" \
    "v_mfma_f32_16x16x32_bf16 a[128:131], v[112:115], v[192:195], a[128:131]\n" \
    "v_exp_f32 v61, v43\n" \
    "v_mfma_f32_16x16x32_bf16 a[128:131], v[116:119], v[196:199], a[128:131]\n" \
    "ds_read_b64_tr_b16 v[168:169], v212 offset:12416\n" \
    "v_exp_f32 v44, v44\n" \
    "v_mfma_f32_16x16x32_bf16 a[132:135], v[120:123], v[192:195], a[132:135]\n" \
    "v_exp_f32 v62, v45\n" \
    "v_mov_b32_sdwa v61, v42 dst_sel:WORD_0 dst_unused:UNUSED_PRESERVE src0_sel:WORD_1\n" \
    "v_mfma_f32_16x16x32_bf16 a[132:135], v[124:127], v[196:199], a[132:135]\n" \
    "buffer_load_dwordx4 v[192:195], v8, s[52:55], s64 offen offset:0 sc1\n" \
    "buffer_load_dwordx4 v[196:199], v8, s[52:55], s64 offen offset:1024 sc1\n" \
    "v_exp_f32 v46, v46\n" \
    "v_exp_f32 v63, v47\n" \
    "v_mov_b32_sdwa v62, v44 dst_sel:WORD_0 dst_unused:UNUSED_PRESERVE src0_sel:WORD_1\n" \
    "v_mov_b32_sdwa v63, v46 dst_sel:WORD_0 dst_unused:UNUSED_PRESERVE src0_sel:WORD_1\n" \
    "v_mfma_f32_32x32x16_bf16 a[16:31], v[68:71], v[56:59], a[16:31]\n" \
    "ds_read_b64_tr_b16 v[170:171], v212 offset:15488\n" \
    "v_exp_f32 v16, v16\n" \
    "v_exp_f32 v48, v17\n" \
    "s_mov_b32 m0, s69\n" \
    "v_exp_f32 v18, v18\n" \
    "v_mfma_f32_32x32x16_bf16 a[16:31], v[72:75], v[60:63], a[16:31]\n" \
    "buffer_load_dwordx4 v11, s[60:63], s67 offen lds\n" \
    "v_exp_f32 v49, v19\n" \
    "v_exp_f32 v20, v20\n" \
    "v_mov_b32_sdwa v48, v16 dst_sel:WORD_0 dst_unused:UNUSED_PRESERVE src0_sel:WORD_1\n" \
    "v_mfma_f32_32x32x8_bf16 v[32:47], v[76:77], a[214:215], 0\n" \
    "ds_read_b64_tr_b16 v[172:173], v212 offset:18560\n" \
    "v_exp_f32 v50, v21\n" \
    "v_mov_b32_sdwa v49, v18 dst_sel:WORD_0 dst_unused:UNUSED_PRESERVE src0_sel:WORD_1\n" \
    "v_exp_f32 v22, v22\n" \
    "v_mov_b32_sdwa v50, v20 dst_sel:WORD_0 dst_unused:UNUSED_PRESERVE src0_sel:WORD_1\n" \
    "v_mfma_f32_16x16x32_bf16 a[136:139], v[80:83], v[200:203], a[136:139]\n" \
    "v_exp_f32 v51, v23\n" \
    "v_mfma_f32_16x16x32_bf16 a[136:139], v[84:87], v[204:207], a[136:139]\n" \
    "v_exp_f32 v24, v24\n" \
    "v_mfma_f32_32x32x16_bf16 v[32:47], v[64:67], a[204:207], v[32:47]\n" \
    "ds_read_b128 v[64:67], v208 offset:5120\n" \
    "ds_read_b32 v76, v209 offset:5120\n" \
    "v_exp_f32 v52, v25\n" \
    "v_mov_b32_sdwa v51, v22 dst_sel:WORD_0 dst_unused:UNUSED_PRESERVE src0_sel:WORD_1\n" \
    "v_exp_f32 v26, v26\n" \
    "v_mov_b32_sdwa v52, v24 dst_sel:WORD_0 dst_unused:UNUSED_PRESERVE src0_sel:WORD_1\n" \
    "v_mfma_f32_16x16x32_bf16 a[140:143], v[88:91], v[200:203], a[140:143]\n" \
    "v_exp_f32 v53, v27\n" \
    "v_mfma_f32_16x16x32_bf16 a[140:143], v[92:95], v[204:207], a[140:143]\n" \
    "ds_read_b64_tr_b16 v[174:175], v212 offset:21632\n" \
    "v_exp_f32 v28, v28\n" \
    "v_mfma_f32_16x16x32_bf16 a[144:147], v[96:99], v[200:203], a[144:147]\n" \
    "v_exp_f32 v54, v29\n" \
    "v_mov_b32_sdwa v53, v26 dst_sel:WORD_0 dst_unused:UNUSED_PRESERVE src0_sel:WORD_1\n" \
    "v_mfma_f32_16x16x32_bf16 a[144:147], v[100:103], v[204:207], a[144:147]\n" \
    "v_exp_f32 v30, v30\n" \
    "v_exp_f32 v55, v31\n" \
    "v_mov_b32_sdwa v54, v28 dst_sel:WORD_0 dst_unused:UNUSED_PRESERVE src0_sel:WORD_1\n" \
    "v_mov_b32_sdwa v55, v30 dst_sel:WORD_0 dst_unused:UNUSED_PRESERVE src0_sel:WORD_1\n" \
    "v_mfma_f32_32x32x16_bf16 a[32:47], v[68:71], v[48:51], a[32:47]\n" \
    "v_exp_f32 v32, v32\n" \
    "v_exp_f32 v56, v33\n" \
    "v_exp_f32 v34, v34\n" \
    "v_mfma_f32_32x32x16_bf16 a[32:47], v[72:75], v[52:55], a[32:47]\n" \
    "buffer_load_dwordx4 v12, s[60:63], s67 offen offset:1024 lds\n" \
    "s_add_u32 s69, s69, 0xc00\n" \
    "v_exp_f32 v57, v35\n" \
    "v_exp_f32 v36, v36\n" \
    "v_mov_b32_sdwa v56, v32 dst_sel:WORD_0 dst_unused:UNUSED_PRESERVE src0_sel:WORD_1\n" \
    "s_waitcnt vmcnt(10) lgkmcnt(0)\n" \
    "v_mfma_f32_32x32x8_bf16 v[16:31], v[76:77], a[208:209], 0\n" \
    "v_exp_f32 v58, v37\n" \
    "v_mov_b32_sdwa v57, v34 dst_sel:WORD_0 dst_unused:UNUSED_PRESERVE src0_sel:WORD_1\n" \
    "v_exp_f32 v38, v38\n" \
    "v_mov_b32_sdwa v58, v36 dst_sel:WORD_0 dst_unused:UNUSED_PRESERVE src0_sel:WORD_1\n" \
    "v_mfma_f32_16x16x32_bf16 a[148:151], v[104:107], v[200:203], a[148:151]\n" \
    "v_exp_f32 v59, v39\n" \
    "v_mfma_f32_16x16x32_bf16 a[148:151], v[108:111], v[204:207], a[148:151]\n" \
    "v_exp_f32 v40, v40\n" \
    "v_mfma_f32_32x32x16_bf16 v[16:31], v[64:67], a[192:195], v[16:31]\n" \
    "v_exp_f32 v60, v41\n" \
    "v_mov_b32_sdwa v59, v38 dst_sel:WORD_0 dst_unused:UNUSED_PRESERVE src0_sel:WORD_1\n" \
    "v_exp_f32 v42, v42\n" \
    "v_mov_b32_sdwa v60, v40 dst_sel:WORD_0 dst_unused:UNUSED_PRESERVE src0_sel:WORD_1\n" \
    "v_mfma_f32_16x16x32_bf16 a[152:155], v[112:115], v[200:203], a[152:155]\n" \
    "v_exp_f32 v61, v43\n" \
    "v_mfma_f32_16x16x32_bf16 a[152:155], v[116:119], v[204:207], a[152:155]\n" \
    "v_exp_f32 v44, v44\n" \
    "v_mfma_f32_16x16x32_bf16 a[156:159], v[120:123], v[200:203], a[156:159]\n" \
    "v_exp_f32 v62, v45\n" \
    "v_mov_b32_sdwa v61, v42 dst_sel:WORD_0 dst_unused:UNUSED_PRESERVE src0_sel:WORD_1\n" \
    "v_mfma_f32_16x16x32_bf16 a[156:159], v[124:127], v[204:207], a[156:159]\n" \
    "buffer_load_dwordx4 v[200:203], v8, s[56:59], s64 offen offset:0 sc1\n" \
    "buffer_load_dwordx4 v[204:207], v8, s[56:59], s64 offen offset:1024 sc1\n" \
    "s_add_u32 s64, s64, 10240\n" \
    "v_exp_f32 v46, v46\n" \
    "v_exp_f32 v63, v47\n" \
    "v_mov_b32_sdwa v62, v44 dst_sel:WORD_0 dst_unused:UNUSED_PRESERVE src0_sel:WORD_1\n" \
    "v_mov_b32_sdwa v63, v46 dst_sel:WORD_0 dst_unused:UNUSED_PRESERVE src0_sel:WORD_1\n" \
    "v_add_u32 v211, s66, v220\n" \
    "v_xor_b32 v212, 32, v211\n" \
    "v_mfma_f32_32x32x16_bf16 a[48:63], v[68:71], v[56:59], a[48:63]\n" \
    "ds_read_b64_tr_b16 v[80:81], v211 offset:0\n" \
    "v_exp_f32 v16, v16\n" \
    "v_exp_f32 v48, v17\n" \
    "v_exp_f32 v18, v18\n" \
    "v_mfma_f32_32x32x16_bf16 a[48:63], v[72:75], v[60:63], a[48:63]\n" \
    "ds_read_b128 v[68:71], v210 offset:5120\n" \
    "v_exp_f32 v49, v19\n" \
    "v_exp_f32 v20, v20\n" \
    "v_mov_b32_sdwa v48, v16 dst_sel:WORD_0 dst_unused:UNUSED_PRESERVE src0_sel:WORD_1\n" \
    "v_mfma_f32_32x32x8_bf16 v[32:47], v[76:77], a[210:211], 0\n" \
    "ds_read_b128 v[72:75], v210 offset:5760\n" \
    "ds_read_b64_tr_b16 v[82:83], v211 offset:3072\n" \
    "v_exp_f32 v50, v21\n" \
    "v_mov_b32_sdwa v49, v18 dst_sel:WORD_0 dst_unused:UNUSED_PRESERVE src0_sel:WORD_1\n" \
    "v_exp_f32 v22, v22\n" \
    "v_mov_b32_sdwa v50, v20 dst_sel:WORD_0 dst_unused:UNUSED_PRESERVE src0_sel:WORD_1\n" \
    "v_mfma_f32_16x16x32_bf16 a[64:67], v[128:131], v[176:179], a[64:67]\n" \
    "v_exp_f32 v51, v23\n" \
    "v_mfma_f32_16x16x32_bf16 a[64:67], v[132:135], v[180:183], a[64:67]\n" \
    "v_exp_f32 v24, v24\n" \
    "v_mfma_f32_32x32x16_bf16 v[32:47], v[64:67], a[196:199], v[32:47]\n" \
    "buffer_load_dwordx4 v13, s[60:63], s67 offen offset:2048 lds\n" \
    "s_add_u32 s67, s67, 18432\n" \
    "ds_read_b64_tr_b16 v[84:85], v211 offset:6144\n" \
    "v_exp_f32 v52, v25\n" \
    "v_mov_b32_sdwa v51, v22 dst_sel:WORD_0 dst_unused:UNUSED_PRESERVE src0_sel:WORD_1\n" \
    "v_exp_f32 v26, v26\n" \
    "v_mov_b32_sdwa v52, v24 dst_sel:WORD_0 dst_unused:UNUSED_PRESERVE src0_sel:WORD_1\n" \
    "v_mfma_f32_16x16x32_bf16 a[68:71], v[136:139], v[176:179], a[68:71]\n" \
    "v_exp_f32 v53, v27\n" \
    "v_mfma_f32_16x16x32_bf16 a[68:71], v[140:143], v[180:183], a[68:71]\n" \
    "ds_read_b64_tr_b16 v[86:87], v211 offset:9216\n" \
    "v_exp_f32 v28, v28\n" \
    "v_mfma_f32_16x16x32_bf16 a[72:75], v[144:147], v[176:179], a[72:75]\n" \
    "v_exp_f32 v54, v29\n" \
    "v_mov_b32_sdwa v53, v26 dst_sel:WORD_0 dst_unused:UNUSED_PRESERVE src0_sel:WORD_1\n" \
    "v_mfma_f32_16x16x32_bf16 a[72:75], v[148:151], v[180:183], a[72:75]\n" \
    "v_exp_f32 v30, v30\n" \
    "v_exp_f32 v55, v31\n" \
    "v_mov_b32_sdwa v54, v28 dst_sel:WORD_0 dst_unused:UNUSED_PRESERVE src0_sel:WORD_1\n" \
    "v_mov_b32_sdwa v55, v30 dst_sel:WORD_0 dst_unused:UNUSED_PRESERVE src0_sel:WORD_1\n" \
    "s_waitcnt vmcnt(9) lgkmcnt(3)\n" \
    "v_mfma_f32_32x32x16_bf16 a[0:15], v[68:71], v[48:51], a[0:15]\n" \
    "ds_read_b64_tr_b16 v[88:89], v212 offset:0\n" \
    "v_exp_f32 v32, v32\n" \
    "v_exp_f32 v56, v33\n" \
    "s_mov_b32 m0, s69\n" \
    "v_exp_f32 v34, v34\n" \
    "v_mfma_f32_32x32x16_bf16 a[0:15], v[72:75], v[52:55], a[0:15]\n" \
    "buffer_load_dwordx4 v11, s[60:63], s67 offen lds\n" \
    "v_exp_f32 v57, v35\n" \
    "v_exp_f32 v36, v36\n" \
    "v_mov_b32_sdwa v56, v32 dst_sel:WORD_0 dst_unused:UNUSED_PRESERVE src0_sel:WORD_1\n" \
    "v_mfma_f32_32x32x8_bf16 v[16:31], v[76:77], a[212:213], 0\n" \
    "ds_read_b64_tr_b16 v[90:91], v212 offset:3072\n" \
    "v_exp_f32 v58, v37\n" \
    "v_mov_b32_sdwa v57, v34 dst_sel:WORD_0 dst_unused:UNUSED_PRESERVE src0_sel:WORD_1\n" \
    "v_exp_f32 v38, v38\n" \
    "v_mov_b32_sdwa v58, v36 dst_sel:WORD_0 dst_unused:UNUSED_PRESERVE src0_sel:WORD_1\n" \
    "v_mfma_f32_16x16x32_bf16 a[76:79], v[152:155], v[176:179], a[76:79]\n" \
    "v_exp_f32 v59, v39\n" \
    "v_mfma_f32_16x16x32_bf16 a[76:79], v[156:159], v[180:183], a[76:79]\n" \
    "v_exp_f32 v40, v40\n" \
    "v_mfma_f32_32x32x16_bf16 v[16:31], v[64:67], a[200:203], v[16:31]\n" \
    "ds_read_b64_tr_b16 v[92:93], v212 offset:6144\n" \
    "v_exp_f32 v60, v41\n" \
    "v_mov_b32_sdwa v59, v38 dst_sel:WORD_0 dst_unused:UNUSED_PRESERVE src0_sel:WORD_1\n" \
    "v_exp_f32 v42, v42\n" \
    "v_mov_b32_sdwa v60, v40 dst_sel:WORD_0 dst_unused:UNUSED_PRESERVE src0_sel:WORD_1\n" \
    "v_mfma_f32_16x16x32_bf16 a[80:83], v[160:163], v[176:179], a[80:83]\n" \
    "v_exp_f32 v61, v43\n" \
    "v_mfma_f32_16x16x32_bf16 a[80:83], v[164:167], v[180:183], a[80:83]\n" \
    "ds_read_b64_tr_b16 v[94:95], v212 offset:9216\n" \
    "v_exp_f32 v44, v44\n" \
    "v_mfma_f32_16x16x32_bf16 a[84:87], v[168:171], v[176:179], a[84:87]\n" \
    "v_exp_f32 v62, v45\n" \
    "v_mov_b32_sdwa v61, v42 dst_sel:WORD_0 dst_unused:UNUSED_PRESERVE src0_sel:WORD_1\n" \
    "v_mfma_f32_16x16x32_bf16 a[84:87], v[172:175], v[180:183], a[84:87]\n" \
    "buffer_load_dwordx4 v[176:179], v8, s[44:47], s64 offen offset:0 sc1\n" \
    "buffer_load_dwordx4 v[180:183], v8, s[44:47], s64 offen offset:1024 sc1\n" \
    "v_exp_f32 v46, v46\n" \
    "v_exp_f32 v63, v47\n" \
    "v_mov_b32_sdwa v62, v44 dst_sel:WORD_0 dst_unused:UNUSED_PRESERVE src0_sel:WORD_1\n" \
    "v_mov_b32_sdwa v63, v46 dst_sel:WORD_0 dst_unused:UNUSED_PRESERVE src0_sel:WORD_1\n" \
    "v_mfma_f32_32x32x16_bf16 a[16:31], v[68:71], v[56:59], a[16:31]\n" \
    "ds_read_b64_tr_b16 v[96:97], v211 offset:64\n" \
    "v_exp_f32 v16, v16\n" \
    "v_exp_f32 v48, v17\n" \
    "v_exp_f32 v18, v18\n" \
    "v_mfma_f32_32x32x16_bf16 a[16:31], v[72:75], v[60:63], a[16:31]\n" \
    "buffer_load_dwordx4 v12, s[60:63], s67 offen offset:1024 lds\n" \
    "s_add_u32 s69, s69, 0xc00\n" \
    "v_exp_f32 v49, v19\n" \
    "v_exp_f32 v20, v20\n" \
    "v_mov_b32_sdwa v48, v16 dst_sel:WORD_0 dst_unused:UNUSED_PRESERVE src0_sel:WORD_1\n" \
    "v_mfma_f32_32x32x8_bf16 v[32:47], v[76:77], a[214:215], 0\n" \
    "ds_read_b64_tr_b16 v[98:99], v211 offset:3136\n" \
    "v_exp_f32 v50, v21\n" \
    "v_mov_b32_sdwa v49, v18 dst_sel:WORD_0 dst_unused:UNUSED_PRESERVE src0_sel:WORD_1\n" \
    "v_exp_f32 v22, v22\n" \
    "v_mov_b32_sdwa v50, v20 dst_sel:WORD_0 dst_unused:UNUSED_PRESERVE src0_sel:WORD_1\n" \
    "v_mfma_f32_16x16x32_bf16 a[88:91], v[128:131], v[184:187], a[88:91]\n" \
    "v_exp_f32 v51, v23\n" \
    "v_mfma_f32_16x16x32_bf16 a[88:91], v[132:135], v[188:191], a[88:91]\n" \
    "v_exp_f32 v24, v24\n" \
    "v_mfma_f32_32x32x16_bf16 v[32:47], v[64:67], a[204:207], v[32:47]\n" \
    "ds_read_b128 v[64:67], v208 offset:7680\n" \
    "ds_read_b32 v76, v209 offset:7680\n" \
    "ds_read_b64_tr_b16 v[100:101], v211 offset:6208\n" \
    "v_exp_f32 v52, v25\n" \
    "v_mov_b32_sdwa v51, v22 dst_sel:WORD_0 dst_unused:UNUSED_PRESERVE src0_sel:WORD_1\n" \
    "v_exp_f32 v26, v26\n" \
    "v_mov_b32_sdwa v52, v24 dst_sel:WORD_0 dst_unused:UNUSED_PRESERVE src0_sel:WORD_1\n" \
    "v_mfma_f32_16x16x32_bf16 a[92:95], v[136:139], v[184:187], a[92:95]\n" \
    "v_exp_f32 v53, v27\n" \
    "v_mfma_f32_16x16x32_bf16 a[92:95], v[140:143], v[188:191], a[92:95]\n" \
    "ds_read_b64_tr_b16 v[102:103], v211 offset:9280\n" \
    "v_exp_f32 v28, v28\n" \
    "v_mfma_f32_16x16x32_bf16 a[96:99], v[144:147], v[184:187], a[96:99]\n" \
    "v_exp_f32 v54, v29\n" \
    "v_mov_b32_sdwa v53, v26 dst_sel:WORD_0 dst_unused:UNUSED_PRESERVE src0_sel:WORD_1\n" \
    "v_mfma_f32_16x16x32_bf16 a[96:99], v[148:151], v[188:191], a[96:99]\n" \
    "v_exp_f32 v30, v30\n" \
    "v_exp_f32 v55, v31\n" \
    "v_mov_b32_sdwa v54, v28 dst_sel:WORD_0 dst_unused:UNUSED_PRESERVE src0_sel:WORD_1\n" \
    "v_mov_b32_sdwa v55, v30 dst_sel:WORD_0 dst_unused:UNUSED_PRESERVE src0_sel:WORD_1\n" \
    "v_mfma_f32_32x32x16_bf16 a[32:47], v[68:71], v[48:51], a[32:47]\n" \
    "ds_read_b64_tr_b16 v[104:105], v212 offset:64\n" \
    "v_exp_f32 v32, v32\n" \
    "v_exp_f32 v56, v33\n" \
    "v_exp_f32 v34, v34\n" \
    "v_mfma_f32_32x32x16_bf16 a[32:47], v[72:75], v[52:55], a[32:47]\n" \
    "buffer_load_dwordx4 v13, s[60:63], s67 offen offset:2048 lds\n" \
    "s_add_u32 s67, s67, 18432\n" \
    "v_exp_f32 v57, v35\n" \
    "v_exp_f32 v36, v36\n" \
    "v_mov_b32_sdwa v56, v32 dst_sel:WORD_0 dst_unused:UNUSED_PRESERVE src0_sel:WORD_1\n" \
    "s_waitcnt vmcnt(10) lgkmcnt(3)\n" \
    "v_mfma_f32_32x32x8_bf16 v[16:31], v[76:77], a[208:209], 0\n" \
    "ds_read_b64_tr_b16 v[106:107], v212 offset:3136\n" \
    "v_exp_f32 v58, v37\n" \
    "v_mov_b32_sdwa v57, v34 dst_sel:WORD_0 dst_unused:UNUSED_PRESERVE src0_sel:WORD_1\n" \
    "v_exp_f32 v38, v38\n" \
    "v_mov_b32_sdwa v58, v36 dst_sel:WORD_0 dst_unused:UNUSED_PRESERVE src0_sel:WORD_1\n" \
    "v_mfma_f32_16x16x32_bf16 a[100:103], v[152:155], v[184:187], a[100:103]\n" \
    "v_exp_f32 v59, v39\n" \
    "v_mfma_f32_16x16x32_bf16 a[100:103], v[156:159], v[188:191], a[100:103]\n" \
    "v_exp_f32 v40, v40\n" \
    "v_mfma_f32_32x32x16_bf16 v[16:31], v[64:67], a[192:195], v[16:31]\n" \
    "v_exp_f32 v60, v41\n" \
    "v_mov_b32_sdwa v59, v38 dst_sel:WORD_0 dst_unused:UNUSED_PRESERVE src0_sel:WORD_1\n" \
    "v_exp_f32 v42, v42\n" \
    "v_mov_b32_sdwa v60, v40 dst_sel:WORD_0 dst_unused:UNUSED_PRESERVE src0_sel:WORD_1\n" \
    "v_mfma_f32_16x16x32_bf16 a[104:107], v[160:163], v[184:187], a[104:107]\n" \
    "v_exp_f32 v61, v43\n" \
    "v_mfma_f32_16x16x32_bf16 a[104:107], v[164:167], v[188:191], a[104:107]\n" \
    "ds_read_b64_tr_b16 v[108:109], v212 offset:6208\n" \
    "v_exp_f32 v44, v44\n" \
    "v_mfma_f32_16x16x32_bf16 a[108:111], v[168:171], v[184:187], a[108:111]\n" \
    "v_exp_f32 v62, v45\n" \
    "v_mov_b32_sdwa v61, v42 dst_sel:WORD_0 dst_unused:UNUSED_PRESERVE src0_sel:WORD_1\n" \
    "v_mfma_f32_16x16x32_bf16 a[108:111], v[172:175], v[188:191], a[108:111]\n" \
    "buffer_load_dwordx4 v[184:187], v8, s[48:51], s64 offen offset:0 sc1\n" \
    "buffer_load_dwordx4 v[188:191], v8, s[48:51], s64 offen offset:1024 sc1\n" \
    "v_exp_f32 v46, v46\n" \
    "v_exp_f32 v63, v47\n" \
    "v_mov_b32_sdwa v62, v44 dst_sel:WORD_0 dst_unused:UNUSED_PRESERVE src0_sel:WORD_1\n" \
    "v_mov_b32_sdwa v63, v46 dst_sel:WORD_0 dst_unused:UNUSED_PRESERVE src0_sel:WORD_1\n" \
    "v_mfma_f32_32x32x16_bf16 a[48:63], v[68:71], v[56:59], a[48:63]\n" \
    "ds_read_b64_tr_b16 v[110:111], v212 offset:9280\n" \
    "v_exp_f32 v16, v16\n" \
    "v_exp_f32 v48, v17\n" \
    "v_exp_f32 v18, v18\n" \
    "v_mfma_f32_32x32x16_bf16 a[48:63], v[72:75], v[60:63], a[48:63]\n" \
    "ds_read_b128 v[68:71], v210 offset:7680\n" \
    "v_exp_f32 v49, v19\n" \
    "v_exp_f32 v20, v20\n" \
    "v_mov_b32_sdwa v48, v16 dst_sel:WORD_0 dst_unused:UNUSED_PRESERVE src0_sel:WORD_1\n" \
    "v_mfma_f32_32x32x8_bf16 v[32:47], v[76:77], a[210:211], 0\n" \
    "ds_read_b128 v[72:75], v210 offset:8320\n" \
    "ds_read_b64_tr_b16 v[112:113], v211 offset:128\n" \
    "v_exp_f32 v50, v21\n" \
    "v_mov_b32_sdwa v49, v18 dst_sel:WORD_0 dst_unused:UNUSED_PRESERVE src0_sel:WORD_1\n" \
    "v_exp_f32 v22, v22\n" \
    "v_mov_b32_sdwa v50, v20 dst_sel:WORD_0 dst_unused:UNUSED_PRESERVE src0_sel:WORD_1\n" \
    "v_mfma_f32_16x16x32_bf16 a[112:115], v[128:131], v[192:195], a[112:115]\n" \
    "v_exp_f32 v51, v23\n" \
    "v_mfma_f32_16x16x32_bf16 a[112:115], v[132:135], v[196:199], a[112:115]\n" \
    "v_exp_f32 v24, v24\n" \
    "v_mfma_f32_32x32x16_bf16 v[32:47], v[64:67], a[196:199], v[32:47]\n" \
    "v_exp_f32 v52, v25\n" \
    "v_mov_b32_sdwa v51, v22 dst_sel:WORD_0 dst_unused:UNUSED_PRESERVE src0_sel:WORD_1\n" \
    "v_exp_f32 v26, v26\n" \
    "v_mov_b32_sdwa v52, v24 dst_sel:WORD_0 dst_unused:UNUSED_PRESERVE src0_sel:WORD_1\n" \
    "v_mfma_f32_16x16x32_bf16 a[116:119], v[136:139], v[192:195], a[116:119]\n" \
    "v_exp_f32 v53, v27\n" \
    "v_mfma_f32_16x16x32_bf16 a[116:119], v[140:143], v[196:199], a[116:119]\n" \
    "ds_read_b64_tr_b16 v[114:115], v211 offset:3200\n" \
    "v_exp_f32 v28, v28\n" \
    "v_mfma_f32_16x16x32_bf16 a[120:123], v[144:147], v[192:195], a[120:123]\n" \
    "v_exp_f32 v54, v29\n" \
    "v_mov_b32_sdwa v53, v26 dst_sel:WORD_0 dst_unused:UNUSED_PRESERVE src0_sel:WORD_1\n" \
    "v_mfma_f32_16x16x32_bf16 a[120:123], v[148:151], v[196:199], a[120:123]\n" \
    "v_exp_f32 v30, v30\n" \
    "v_exp_f32 v55, v31\n" \
    "v_mov_b32_sdwa v54, v28 dst_sel:WORD_0 dst_unused:UNUSED_PRESERVE src0_sel:WORD_1\n" \
    "v_mov_b32_sdwa v55, v30 dst_sel:WORD_0 dst_unused:UNUSED_PRESERVE src0_sel:WORD_1\n" \
    "s_waitcnt vmcnt(8) lgkmcnt(2)\n" \
    "v_mfma_f32_32x32x16_bf16 a[0:15], v[68:71], v[48:51], a[0:15]\n" \
    "ds_read_b64_tr_b16 v[116:117], v211 offset:6272\n" \
    "v_exp_f32 v32, v32\n" \
    "v_exp_f32 v56, v33\n" \
    "v_exp_f32 v34, v34\n" \
    "v_mfma_f32_32x32x16_bf16 a[0:15], v[72:75], v[52:55], a[0:15]\n" \
    "v_exp_f32 v57, v35\n" \
    "v_exp_f32 v36, v36\n" \
    "v_mov_b32_sdwa v56, v32 dst_sel:WORD_0 dst_unused:UNUSED_PRESERVE src0_sel:WORD_1\n" \
    "v_mfma_f32_32x32x8_bf16 v[16:31], v[76:77], a[212:213], 0\n" \
    "ds_read_b64_tr_b16 v[118:119], v211 offset:9344\n" \
    "v_exp_f32 v58, v37\n" \
    "v_mov_b32_sdwa v57, v34 dst_sel:WORD_0 dst_unused:UNUSED_PRESERVE src0_sel:WORD_1\n" \
    "v_add_u32 v210, s66, v219\n" \
    "v_exp_f32 v38, v38\n" \
    "v_mov_b32_sdwa v58, v36 dst_sel:WORD_0 dst_unused:UNUSED_PRESERVE src0_sel:WORD_1\n" \
    "v_mfma_f32_16x16x32_bf16 a[124:127], v[152:155], v[192:195], a[124:127]\n" \
    "v_exp_f32 v59, v39\n" \
    "v_mfma_f32_16x16x32_bf16 a[124:127], v[156:159], v[196:199], a[124:127]\n" \
    "v_exp_f32 v40, v40\n" \
    "v_mfma_f32_32x32x16_bf16 v[16:31], v[64:67], a[200:203], v[16:31]\n" \
    "v_exp_f32 v60, v41\n" \
    "v_mov_b32_sdwa v59, v38 dst_sel:WORD_0 dst_unused:UNUSED_PRESERVE src0_sel:WORD_1\n" \
    "v_exp_f32 v42, v42\n" \
    "v_mov_b32_sdwa v60, v40 dst_sel:WORD_0 dst_unused:UNUSED_PRESERVE src0_sel:WORD_1\n" \
    "v_mfma_f32_16x16x32_bf16 a[128:131], v[160:163], v[192:195], a[128:131]\n" \
    "v_exp_f32 v61, v43\n" \
    "v_mfma_f32_16x16x32_bf16 a[128:131], v[164:167], v[196:199], a[128:131]\n" \
    "ds_read_b64_tr_b16 v[120:121], v212 offset:128\n" \
    "v_exp_f32 v44, v44\n" \
    "v_mfma_f32_16x16x32_bf16 a[132:135], v[168:171], v[192:195], a[132:135]\n" \
    "v_exp_f32 v62, v45\n" \
    "v_mov_b32_sdwa v61, v42 dst_sel:WORD_0 dst_unused:UNUSED_PRESERVE src0_sel:WORD_1\n" \
    "v_mfma_f32_16x16x32_bf16 a[132:135], v[172:175], v[196:199], a[132:135]\n" \
    "buffer_load_dwordx4 v[192:195], v8, s[52:55], s64 offen offset:0 sc1\n" \
    "buffer_load_dwordx4 v[196:199], v8, s[52:55], s64 offen offset:1024 sc1\n" \
    "v_exp_f32 v46, v46\n" \
    "v_exp_f32 v63, v47\n" \
    "v_mov_b32_sdwa v62, v44 dst_sel:WORD_0 dst_unused:UNUSED_PRESERVE src0_sel:WORD_1\n" \
    "v_mov_b32_sdwa v63, v46 dst_sel:WORD_0 dst_unused:UNUSED_PRESERVE src0_sel:WORD_1\n" \
    "v_mfma_f32_32x32x16_bf16 a[16:31], v[68:71], v[56:59], a[16:31]\n" \
    "ds_read_b64_tr_b16 v[122:123], v212 offset:3200\n" \
    "v_exp_f32 v16, v16\n" \
    "v_exp_f32 v48, v17\n" \
    "v_exp_f32 v18, v18\n" \
    "v_mfma_f32_32x32x16_bf16 a[16:31], v[72:75], v[60:63], a[16:31]\n" \
    "v_exp_f32 v49, v19\n" \
    "v_exp_f32 v20, v20\n" \
    "v_mov_b32_sdwa v48, v16 dst_sel:WORD_0 dst_unused:UNUSED_PRESERVE src0_sel:WORD_1\n" \
    "v_mfma_f32_32x32x8_bf16 v[32:47], v[76:77], a[214:215], 0\n" \
    "ds_read_b64_tr_b16 v[124:125], v212 offset:6272\n" \
    "v_exp_f32 v50, v21\n" \
    "v_mov_b32_sdwa v49, v18 dst_sel:WORD_0 dst_unused:UNUSED_PRESERVE src0_sel:WORD_1\n" \
    "v_add_u32 v208, s66, v217\n" \
    "v_add_u32 v209, s66, v218\n" \
    "v_exp_f32 v22, v22\n" \
    "v_mov_b32_sdwa v50, v20 dst_sel:WORD_0 dst_unused:UNUSED_PRESERVE src0_sel:WORD_1\n" \
    "v_mfma_f32_16x16x32_bf16 a[136:139], v[128:131], v[200:203], a[136:139]\n" \
    "v_exp_f32 v51, v23\n" \
    "v_mfma_f32_16x16x32_bf16 a[136:139], v[132:135], v[204:207], a[136:139]\n" \
    "v_exp_f32 v24, v24\n" \
    "v_mfma_f32_32x32x16_bf16 v[32:47], v[64:67], a[204:207], v[32:47]\n" \
    "ds_read_b128 v[64:67], v208 offset:0\n" \
    "ds_read_b32 v76, v209 offset:0\n" \
    "v_exp_f32 v52, v25\n" \
    "v_mov_b32_sdwa v51, v22 dst_sel:WORD_0 dst_unused:UNUSED_PRESERVE src0_sel:WORD_1\n" \
    "v_exp_f32 v26, v26\n" \
    "v_mov_b32_sdwa v52, v24 dst_sel:WORD_0 dst_unused:UNUSED_PRESERVE src0_sel:WORD_1\n" \
    "v_mfma_f32_16x16x32_bf16 a[140:143], v[136:139], v[200:203], a[140:143]\n" \
    "v_exp_f32 v53, v27\n" \
    "v_mfma_f32_16x16x32_bf16 a[140:143], v[140:143], v[204:207], a[140:143]\n" \
    "ds_read_b64_tr_b16 v[126:127], v212 offset:9344\n" \
    "v_exp_f32 v28, v28\n" \
    "v_mfma_f32_16x16x32_bf16 a[144:147], v[144:147], v[200:203], a[144:147]\n" \
    "v_exp_f32 v54, v29\n" \
    "v_mov_b32_sdwa v53, v26 dst_sel:WORD_0 dst_unused:UNUSED_PRESERVE src0_sel:WORD_1\n" \
    "v_mfma_f32_16x16x32_bf16 a[144:147], v[148:151], v[204:207], a[144:147]\n" \
    "v_exp_f32 v30, v30\n" \
    "v_exp_f32 v55, v31\n" \
    "v_mov_b32_sdwa v54, v28 dst_sel:WORD_0 dst_unused:UNUSED_PRESERVE src0_sel:WORD_1\n" \
    "v_mov_b32_sdwa v55, v30 dst_sel:WORD_0 dst_unused:UNUSED_PRESERVE src0_sel:WORD_1\n" \
    "v_mfma_f32_32x32x16_bf16 a[32:47], v[68:71], v[48:51], a[32:47]\n" \
    "v_exp_f32 v32, v32\n" \
    "v_exp_f32 v56, v33\n" \
    "v_exp_f32 v34, v34\n" \
    "v_mfma_f32_32x32x16_bf16 a[32:47], v[72:75], v[52:55], a[32:47]\n" \
    "v_exp_f32 v57, v35\n" \
    "v_exp_f32 v36, v36\n" \
    "v_mov_b32_sdwa v56, v32 dst_sel:WORD_0 dst_unused:UNUSED_PRESERVE src0_sel:WORD_1\n" \
    "s_waitcnt vmcnt(6) lgkmcnt(0)\n" \
    "v_mfma_f32_32x32x8_bf16 v[16:31], v[76:77], a[208:209], 0\n" \
    "v_exp_f32 v58, v37\n" \
    "v_mov_b32_sdwa v57, v34 dst_sel:WORD_0 dst_unused:UNUSED_PRESERVE src0_sel:WORD_1\n" \
    "s_mov_b32 s66, s97\n" \
    "s_add_u32 s97, s97, 45056\n" \
    "s_cmp_eq_u32 s97, 135168\n" \
    "s_cselect_b32 s97, 0, s97\n" \
    "v_exp_f32 v38, v38\n" \
    "v_mov_b32_sdwa v58, v36 dst_sel:WORD_0 dst_unused:UNUSED_PRESERVE src0_sel:WORD_1\n" \
    "v_mfma_f32_16x16x32_bf16 a[148:151], v[152:155], v[200:203], a[148:151]\n" \
    "v_exp_f32 v59, v39\n" \
    "v_mfma_f32_16x16x32_bf16 a[148:151], v[156:159], v[204:207], a[148:151]\n" \
    "v_exp_f32 v40, v40\n" \
    "v_mfma_f32_32x32x16_bf16 v[16:31], v[64:67], a[192:195], v[16:31]\n" \
    "v_exp_f32 v60, v41\n" \
    "v_mov_b32_sdwa v59, v38 dst_sel:WORD_0 dst_unused:UNUSED_PRESERVE src0_sel:WORD_1\n" \
    "v_exp_f32 v42, v42\n" \
    "v_mov_b32_sdwa v60, v40 dst_sel:WORD_0 dst_unused:UNUSED_PRESERVE src0_sel:WORD_1\n" \
    "v_mfma_f32_16x16x32_bf16 a[152:155], v[160:163], v[200:203], a[152:155]\n" \
    "v_exp_f32 v61, v43\n" \
    "v_mfma_f32_16x16x32_bf16 a[152:155], v[164:167], v[204:207], a[152:155]\n" \
    "v_exp_f32 v44, v44\n" \
    "v_mfma_f32_16x16x32_bf16 a[156:159], v[168:171], v[200:203], a[156:159]\n" \
    "v_exp_f32 v62, v45\n" \
    "v_mov_b32_sdwa v61, v42 dst_sel:WORD_0 dst_unused:UNUSED_PRESERVE src0_sel:WORD_1\n" \
    "v_mfma_f32_16x16x32_bf16 a[156:159], v[172:175], v[204:207], a[156:159]\n" \
    "buffer_load_dwordx4 v[200:203], v8, s[56:59], s64 offen offset:0 sc1\n" \
    "buffer_load_dwordx4 v[204:207], v8, s[56:59], s64 offen offset:1024 sc1\n" \
    "s_add_u32 s64, s64, 10240\n" \
    "v_exp_f32 v46, v46\n" \
    "v_exp_f32 v63, v47\n" \
    "v_mov_b32_sdwa v62, v44 dst_sel:WORD_0 dst_unused:UNUSED_PRESERVE src0_sel:WORD_1\n" \
    "v_mov_b32_sdwa v63, v46 dst_sel:WORD_0 dst_unused:UNUSED_PRESERVE src0_sel:WORD_1\n" \
    "s_barrier\n" \
    "s_mul_i32 s70, %6, 3\n" \
    "s_mul_i32 s69, s70, 3072\n" \
    "s_add_u32 s70, s70, 24\n" \
    "s_mul_i32 s67, s70, 18432\n" \
    "s_add_u32 s69, s69, %7\n" \
    "v_mfma_f32_32x32x16_bf16 a[48:63], v[68:71], v[56:59], a[48:63]\n" \
    "v_exp_f32 v16, v16\n" \
    "v_exp_f32 v48, v17\n" \
    "v_exp_f32 v18, v18\n" \
    "v_mfma_f32_32x32x16_bf16 a[48:63], v[72:75], v[60:63], a[48:63]\n" \
    "ds_read_b128 v[68:71], v210 offset:0\n" \
    "v_exp_f32 v49, v19\n" \
    "v_exp_f32 v20, v20\n" \
    "v_mov_b32_sdwa v48, v16 dst_sel:WORD_0 dst_unused:UNUSED_PRESERVE src0_sel:WORD_1\n" \
    "v_mfma_f32_32x32x8_bf16 v[32:47], v[76:77], a[210:211], 0\n" \
    "ds_read_b128 v[72:75], v210 offset:640\n" \
    "v_exp_f32 v50, v21\n" \
    "v_mov_b32_sdwa v49, v18 dst_sel:WORD_0 dst_unused:UNUSED_PRESERVE src0_sel:WORD_1\n" \
    "s_mov_b32 m0, s69\n" \
    "v_exp_f32 v22, v22\n" \
    "v_mov_b32_sdwa v50, v20 dst_sel:WORD_0 dst_unused:UNUSED_PRESERVE src0_sel:WORD_1\n" \
    "v_mfma_f32_16x16x32_bf16 a[64:67], v[80:83], v[176:179], a[64:67]\n" \
    "v_exp_f32 v51, v23\n" \
    "v_mfma_f32_16x16x32_bf16 a[64:67], v[84:87], v[180:183], a[64:67]\n" \
    "v_exp_f32 v24, v24\n" \
    "v_mfma_f32_32x32x16_bf16 v[32:47], v[64:67], a[196:199], v[32:47]\n" \
    "buffer_load_dwordx4 v11, s[60:63], s67 offen lds\n" \
    "v_exp_f32 v52, v25\n" \
    "v_mov_b32_sdwa v51, v22 dst_sel:WORD_0 dst_unused:UNUSED_PRESERVE src0_sel:WORD_1\n" \
    "v_exp_f32 v26, v26\n" \
    "v_mov_b32_sdwa v52, v24 dst_sel:WORD_0 dst_unused:UNUSED_PRESERVE src0_sel:WORD_1\n" \
    "v_mfma_f32_16x16x32_bf16 a[68:71], v[88:91], v[176:179], a[68:71]\n" \
    "v_exp_f32 v53, v27\n" \
    "v_mfma_f32_16x16x32_bf16 a[68:71], v[92:95], v[180:183], a[68:71]\n" \
    "v_exp_f32 v28, v28\n" \
    "v_mfma_f32_16x16x32_bf16 a[72:75], v[96:99], v[176:179], a[72:75]\n" \
    "v_exp_f32 v54, v29\n" \
    "v_mov_b32_sdwa v53, v26 dst_sel:WORD_0 dst_unused:UNUSED_PRESERVE src0_sel:WORD_1\n" \
    "v_mfma_f32_16x16x32_bf16 a[72:75], v[100:103], v[180:183], a[72:75]\n" \
    "v_mov_b64 v[128:129], %16\n" \
    "s_mov_b64 s[84:85], %2\n" \
    "v_readlane_b32 s44, v128, 40\n" \
    "v_readlane_b32 s45, v129, 40\n" \
    "v_exp_f32 v30, v30\n" \
    "v_exp_f32 v55, v31\n" \
    "v_mov_b32_sdwa v54, v28 dst_sel:WORD_0 dst_unused:UNUSED_PRESERVE src0_sel:WORD_1\n" \
    "v_mov_b32_sdwa v55, v30 dst_sel:WORD_0 dst_unused:UNUSED_PRESERVE src0_sel:WORD_1\n" \
    "s_waitcnt vmcnt(5) lgkmcnt(0)\n" \
    "v_mfma_f32_32x32x16_bf16 a[0:15], v[68:71], v[48:51], a[0:15]\n" \
    "v_exp_f32 v32, v32\n" \
    "v_exp_f32 v56, v33\n" \
    "v_exp_f32 v34, v34\n" \
    "v_mfma_f32_32x32x16_bf16 a[0:15], v[72:75], v[52:55], a[0:15]\n" \
    "buffer_load_dwordx4 v12, s[60:63], s67 offen offset:1024 lds\n" \
    "s_add_u32 s69, s69, 0xc00\n" \
    "v_exp_f32 v57, v35\n" \
    "v_exp_f32 v36, v36\n" \
    "v_mov_b32_sdwa v56, v32 dst_sel:WORD_0 dst_unused:UNUSED_PRESERVE src0_sel:WORD_1\n" \
    "v_mfma_f32_32x32x8_bf16 v[16:31], v[76:77], a[212:213], 0\n" \
    "v_exp_f32 v58, v37\n" \
    "v_mov_b32_sdwa v57, v34 dst_sel:WORD_0 dst_unused:UNUSED_PRESERVE src0_sel:WORD_1\n" \
    "v_exp_f32 v38, v38\n" \
    "v_mov_b32_sdwa v58, v36 dst_sel:WORD_0 dst_unused:UNUSED_PRESERVE src0_sel:WORD_1\n" \
    "v_mfma_f32_16x16x32_bf16 a[76:79], v[104:107], v[176:179], a[76:79]\n" \
    "v_exp_f32 v59, v39\n" \
    "v_mfma_f32_16x16x32_bf16 a[76:79], v[108:111], v[180:183], a[76:79]\n" \
    "v_exp_f32 v40, v40\n" \
    "v_mfma_f32_32x32x16_bf16 v[16:31], v[64:67], a[200:203], v[16:31]\n" \
    "v_exp_f32 v60, v41\n" \
    "v_mov_b32_sdwa v59, v38 dst_sel:WORD_0 dst_unused:UNUSED_PRESERVE src0_sel:WORD_1\n" \
    "v_exp_f32 v42, v42\n" \
    "v_mov_b32_sdwa v60, v40 dst_sel:WORD_0 dst_unused:UNUSED_PRESERVE src0_sel:WORD_1\n" \
    "v_mfma_f32_16x16x32_bf16 a[80:83], v[112:115], v[176:179], a[80:83]\n" \
    "v_exp_f32 v61, v43\n" \
    "v_mfma_f32_16x16x32_bf16 a[80:83], v[116:119], v[180:183], a[80:83]\n" \
    "v_exp_f32 v44, v44\n" \
    "v_mfma_f32_16x16x32_bf16 a[84:87], v[120:123], v[176:179], a[84:87]\n" \
    "v_exp_f32 v62, v45\n" \
    "v_mov_b32_sdwa v61, v42 dst_sel:WORD_0 dst_unused:UNUSED_PRESERVE src0_sel:WORD_1\n" \
    "v_mfma_f32_16x16x32_bf16 a[84:87], v[124:127], v[180:183], a[84:87]\n" \
    "v_readlane_b32 s46, v128, 41\n" \
    "v_readlane_b32 s47, v129, 41\n" \
    "v_readlane_b32 s48, v128, 42\n" \
    "v_readlane_b32 s49, v129, 42\n" \
    "v_exp_f32 v46, v46\n" \
    "v_exp_f32 v63, v47\n" \
    "v_mov_b32_sdwa v62, v44 dst_sel:WORD_0 dst_unused:UNUSED_PRESERVE src0_sel:WORD_1\n" \
    "v_mov_b32_sdwa v63, v46 dst_sel:WORD_0 dst_unused:UNUSED_PRESERVE src0_sel:WORD_1\n" \
    "v_mfma_f32_32x32x16_bf16 a[16:31], v[68:71], v[56:59], a[16:31]\n" \
    "v_exp_f32 v16, v16\n" \
    "v_exp_f32 v48, v17\n" \
    "v_exp_f32 v18, v18\n" \
    "v_mfma_f32_32x32x16_bf16 a[16:31], v[72:75], v[60:63], a[16:31]\n" \
    "buffer_load_dwordx4 v13, s[60:63], s67 offen offset:2048 lds\n" \
    "s_add_u32 s67, s67, 18432\n" \
    "v_exp_f32 v49, v19\n" \
    "v_exp_f32 v20, v20\n" \
    "v_mov_b32_sdwa v48, v16 dst_sel:WORD_0 dst_unused:UNUSED_PRESERVE src0_sel:WORD_1\n" \
    "v_mfma_f32_32x32x8_bf16 v[32:47], v[76:77], a[214:215], 0\n" \
    "v_exp_f32 v50, v21\n" \
    "v_mov_b32_sdwa v49, v18 dst_sel:WORD_0 dst_unused:UNUSED_PRESERVE src0_sel:WORD_1\n" \
    "v_exp_f32 v22, v22\n" \
    "v_mov_b32_sdwa v50, v20 dst_sel:WORD_0 dst_unused:UNUSED_PRESERVE src0_sel:WORD_1\n" \
    "v_mfma_f32_16x16x32_bf16 a[88:91], v[80:83], v[184:187], a[88:91]\n" \
    "v_exp_f32 v51, v23\n" \
    "v_mfma_f32_16x16x32_bf16 a[88:91], v[84:87], v[188:191], a[88:91]\n" \
    "v_exp_f32 v24, v24\n" \
    "v_mfma_f32_32x32x16_bf16 v[32:47], v[64:67], a[204:207], v[32:47]\n" \
    "ds_read_b128 v[64:67], v208 offset:2560\n" \
    "ds_read_b32 v76, v209 offset:2560\n" \
    "v_exp_f32 v52, v25\n" \
    "v_mov_b32_sdwa v51, v22 dst_sel:WORD_0 dst_unused:UNUSED_PRESERVE src0_sel:WORD_1\n" \
    "v_exp_f32 v26, v26\n" \
    "v_mov_b32_sdwa v52, v24 dst_sel:WORD_0 dst_unused:UNUSED_PRESERVE src0_sel:WORD_1\n" \
    "v_mfma_f32_16x16x32_bf16 a[92:95], v[88:91], v[184:187], a[92:95]\n" \
    "v_exp_f32 v53, v27\n" \
    "v_mfma_f32_16x16x32_bf16 a[92:95], v[92:95], v[188:191], a[92:95]\n" \
    "v_exp_f32 v28, v28\n" \
    "v_mfma_f32_16x16x32_bf16 a[96:99], v[96:99], v[184:187], a[96:99]\n" \
    "v_exp_f32 v54, v29\n" \
    "v_mov_b32_sdwa v53, v26 dst_sel:WORD_0 dst_unused:UNUSED_PRESERVE src0_sel:WORD_1\n" \
    "v_mfma_f32_16x16x32_bf16 a[96:99], v[100:103], v[188:191], a[96:99]\n" \
    "v_readlane_b32 s50, v128, 43\n" \
    "v_readlane_b32 s51, v129, 43\n" \
    "v_readlane_b32 s52, v128, 44\n" \
    "v_readlane_b32 s53, v129, 44\n" \
    "v_exp_f32 v30, v30\n" \
    "v_exp_f32 v55, v31\n" \
    "v_mov_b32_sdwa v54, v28 dst_sel:WORD_0 dst_unused:UNUSED_PRESERVE src0_sel:WORD_1\n" \
    "v_mov_b32_sdwa v55, v30 dst_sel:WORD_0 dst_unused:UNUSED_PRESERVE src0_sel:WORD_1\n" \
    "v_mfma_f32_32x32x16_bf16 a[32:47], v[68:71], v[48:51], a[32:47]\n" \
    "v_exp_f32 v32, v32\n" \
    "v_exp_f32 v56, v33\n" \
    "s_mov_b32 m0, s69\n" \
    "v_exp_f32 v34, v34\n" \
    "v_mfma_f32_32x32x16_bf16 a[32:47], v[72:75], v[52:55], a[32:47]\n" \
    "buffer_load_dwordx4 v11, s[60:63], s67 offen lds\n" \
    "v_exp_f32 v57, v35\n" \
    "v_exp_f32 v36, v36\n" \
    "v_mov_b32_sdwa v56, v32 dst_sel:WORD_0 dst_unused:UNUSED_PRESERVE src0_sel:WORD_1\n" \
    "s_waitcnt vmcnt(6) lgkmcnt(0)\n" \
    "v_mfma_f32_32x32x8_bf16 v[16:31], v[76:77], a[208:209], 0\n" \
    "v_exp_f32 v58, v37\n" \
    "v_mov_b32_sdwa v57, v34 dst_sel:WORD_0 dst_unused:UNUSED_PRESERVE src0_sel:WORD_1\n" \
    "v_exp_f32 v38, v38\n" \
    "v_mov_b32_sdwa v58, v36 dst_sel:WORD_0 dst_unused:UNUSED_PRESERVE src0_sel:WORD_1\n" \
    "v_mfma_f32_16x16x32_bf16 a[100:103], v[104:107], v[184:187], a[100:103]\n" \
    "v_exp_f32 v59, v39\n" \
    "v_mfma_f32_16x16x32_bf16 a[100:103], v[108:111], v[188:191], a[100:103]\n" \
    "v_exp_f32 v40, v40\n" \
    "v_mfma_f32_32x32x16_bf16 v[16:31], v[64:67], a[192:195], v[16:31]\n" \
    "v_exp_f32 v60, v41\n" \
    "v_mov_b32_sdwa v59, v38 dst_sel:WORD_0 dst_unused:UNUSED_PRESERVE src0_sel:WORD_1\n" \
    "v_exp_f32 v42, v42\n" \
    "v_mov_b32_sdwa v60, v40 dst_sel:WORD_0 dst_unused:UNUSED_PRESERVE src0_sel:WORD_1\n" \
    "v_mfma_f32_16x16x32_bf16 a[104:107], v[112:115], v[184:187], a[104:107]\n" \
    "v_exp_f32 v61, v43\n" \
    "v_mfma_f32_16x16x32_bf16 a[104:107], v[116:119], v[188:191], a[104:107]\n" \
    "v_exp_f32 v44, v44\n" \
    "v_mfma_f32_16x16x32_bf16 a[108:111], v[120:123], v[184:187], a[108:111]\n" \
    "v_exp_f32 v62, v45\n" \
    "v_mov_b32_sdwa v61, v42 dst_sel:WORD_0 dst_unused:UNUSED_PRESERVE src0_sel:WORD_1\n" \
    "v_mfma_f32_16x16x32_bf16 a[108:111], v[124:127], v[188:191], a[108:111]\n" \
    "v_readlane_b32 s54, v128, 45\n" \
    "v_readlane_b32 s55, v129, 45\n" \
    "v_readlane_b32 s88, v128, 46\n" \
    "v_readlane_b32 s89, v129, 46\n" \
    "v_exp_f32 v46, v46\n" \
    "v_exp_f32 v63, v47\n" \
    "v_mov_b32_sdwa v62, v44 dst_sel:WORD_0 dst_unused:UNUSED_PRESERVE src0_sel:WORD_1\n" \
    "v_mov_b32_sdwa v63, v46 dst_sel:WORD_0 dst_unused:UNUSED_PRESERVE src0_sel:WORD_1\n" \
    "v_mfma_f32_32x32x16_bf16 a[48:63], v[68:71], v[56:59], a[48:63]\n" \
    "v_exp_f32 v16, v16\n" \
    "v_exp_f32 v48, v17\n" \
    "v_exp_f32 v18, v18\n" \
    "v_mfma_f32_32x32x16_bf16 a[48:63], v[72:75], v[60:63], a[48:63]\n" \
    "ds_read_b128 v[68:71], v210 offset:2560\n" \
    "v_exp_f32 v49, v19\n" \
    "v_exp_f32 v20, v20\n" \
    "v_mov_b32_sdwa v48, v16 dst_sel:WORD_0 dst_unused:UNUSED_PRESERVE src0_sel:WORD_1\n" \
    "v_mfma_f32_32x32x8_bf16 v[32:47], v[76:77], a[210:211], 0\n" \
    "ds_read_b128 v[72:75], v210 offset:3200\n" \
    "v_exp_f32 v50, v21\n" \
    "v_mov_b32_sdwa v49, v18 dst_sel:WORD_0 dst_unused:UNUSED_PRESERVE src0_sel:WORD_1\n" \
    "v_exp_f32 v22, v22\n" \
    "v_mov_b32_sdwa v50, v20 dst_sel:WORD_0 dst_unused:UNUSED_PRESERVE src0_sel:WORD_1\n" \
    "v_mfma_f32_16x16x32_bf16 a[112:115], v[80:83], v[192:195], a[112:115]\n" \
    "v_exp_f32 v51, v23\n" \
    "v_mfma_f32_16x16x32_bf16 a[112:115], v[84:87], v[196:199], a[112:115]\n" \
    "v_exp_f32 v24, v24\n" \
    "v_mfma_f32_32x32x16_bf16 v[32:47], v[64:67], a[196:199], v[32:47]\n" \
    "buffer_load_dwordx4 v12, s[60:63], s67 offen offset:1024 lds\n" \
    "s_add_u32 s69, s69, 0xc00\n" \
    "v_exp_f32 v52, v25\n" \
    "v_mov_b32_sdwa v51, v22 dst_sel:WORD_0 dst_unused:UNUSED_PRESERVE src0_sel:WORD_1\n" \
    "v_exp_f32 v26, v26\n" \
    "v_mov_b32_sdwa v52, v24 dst_sel:WORD_0 dst_unused:UNUSED_PRESERVE src0_sel:WORD_1\n" \
    "v_mfma_f32_16x16x32_bf16 a[116:119], v[88:91], v[192:195], a[116:119]\n" \
    "v_exp_f32 v53, v27\n" \
    "v_mfma_f32_16x16x32_bf16 a[116:119], v[92:95], v[196:199], a[116:119]\n" \
    "v_exp_f32 v28, v28\n" \
    "v_mfma_f32_16x16x32_bf16 a[120:123], v[96:99], v[192:195], a[120:123]\n" \
    "v_exp_f32 v54, v29\n" \
    "v_mov_b32_sdwa v53, v26 dst_sel:WORD_0 dst_unused:UNUSED_PRESERVE src0_sel:WORD_1\n" \
    "v_mfma_f32_16x16x32_bf16 a[120:123], v[100:103], v[196:199], a[120:123]\n" \
    "s_mov_b32 s90, 2359296\n" \
    "s_mov_b32 s91, 0x20000\n" \
    "s_mov_b32 s86, 2359296\n" \
    "s_mov_b32 s87, 0x20000\n" \
    "v_exp_f32 v30, v30\n" \
    "v_exp_f32 v55, v31\n" \
    "v_mov_b32_sdwa v54, v28 dst_sel:WORD_0 dst_unused:UNUSED_PRESERVE src0_sel:WORD_1\n" \
    "v_mov_b32_sdwa v55, v30 dst_sel:WORD_0 dst_unused:UNUSED_PRESERVE src0_sel:WORD_1\n" \
    "s_waitcnt vmcnt(5) lgkmcnt(0)\n" \
    "v_mfma_f32_32x32x16_bf16 a[0:15], v[68:71], v[48:51], a[0:15]\n" \
    "v_exp_f32 v32, v32\n" \
    "v_exp_f32 v56, v33\n" \
    "v_exp_f32 v34, v34\n" \
    "v_mfma_f32_32x32x16_bf16 a[0:15], v[72:75], v[52:55], a[0:15]\n" \
    "buffer_load_dwordx4 v13, s[60:63], s67 offen offset:2048 lds\n" \
    "s_add_u32 s67, s67, 18432\n" \
    "v_exp_f32 v57, v35\n" \
    "v_exp_f32 v36, v36\n" \
    "v_mov_b32_sdwa v56, v32 dst_sel:WORD_0 dst_unused:UNUSED_PRESERVE src0_sel:WORD_1\n" \
    "v_mfma_f32_32x32x8_bf16 v[16:31], v[76:77], a[212:213], 0\n" \
    "v_exp_f32 v58, v37\n" \
    "v_mov_b32_sdwa v57, v34 dst_sel:WORD_0 dst_unused:UNUSED_PRESERVE src0_sel:WORD_1\n" \
    "v_exp_f32 v38, v38\n" \
    "v_mov_b32_sdwa v58, v36 dst_sel:WORD_0 dst_unused:UNUSED_PRESERVE src0_sel:WORD_1\n" \
    "v_mfma_f32_16x16x32_bf16 a[124:127], v[104:107], v[192:195], a[124:127]\n" \
    "v_exp_f32 v59, v39\n" \
    "v_mfma_f32_16x16x32_bf16 a[124:127], v[108:111], v[196:199], a[124:127]\n" \
    "v_exp_f32 v40, v40\n" \
    "v_mfma_f32_32x32x16_bf16 v[16:31], v[64:67], a[200:203], v[16:31]\n" \
    "v_exp_f32 v60, v41\n" \
    "v_mov_b32_sdwa v59, v38 dst_sel:WORD_0 dst_unused:UNUSED_PRESERVE src0_sel:WORD_1\n" \
    "v_exp_f32 v42, v42\n" \
    "v_mov_b32_sdwa v60, v40 dst_sel:WORD_0 dst_unused:UNUSED_PRESERVE src0_sel:WORD_1\n" \
    "v_mfma_f32_16x16x32_bf16 a[128:131], v[112:115], v[192:195], a[128:131]\n" \
    "v_exp_f32 v61, v43\n" \
    "v_mfma_f32_16x16x32_bf16 a[128:131], v[116:119], v[196:199], a[128:131]\n" \
    "v_exp_f32 v44, v44\n" \
    "v_mfma_f32_16x16x32_bf16 a[132:135], v[120:123], v[192:195], a[132:135]\n" \
    "v_exp_f32 v62, v45\n" \
    "v_mov_b32_sdwa v61, v42 dst_sel:WORD_0 dst_unused:UNUSED_PRESERVE src0_sel:WORD_1\n" \
    "v_mfma_f32_16x16x32_bf16 a[132:135], v[124:127], v[196:199], a[132:135]\n" \
    "v_accvgpr_read_b32 v130, a239\n" \
    "v_accvgpr_read_b32 v131, a240\n" \
    "v_accvgpr_read_b32 v133, a241\n" \
    "v_accvgpr_read_b32 v135, a242\n" \
    "v_exp_f32 v46, v46\n" \
    "v_exp_f32 v63, v47\n" \
    "v_mov_b32_sdwa v62, v44 dst_sel:WORD_0 dst_unused:UNUSED_PRESERVE src0_sel:WORD_1\n" \
    "v_mov_b32_sdwa v63, v46 dst_sel:WORD_0 dst_unused:UNUSED_PRESERVE src0_sel:WORD_1\n" \
    "v_mfma_f32_32x32x16_bf16 a[16:31], v[68:71], v[56:59], a[16:31]\n" \
    "v_exp_f32 v16, v16\n" \
    "v_exp_f32 v48, v17\n" \
    "s_mov_b32 m0, s69\n" \
    "v_exp_f32 v18, v18\n" \
    "v_mfma_f32_32x32x16_bf16 a[16:31], v[72:75], v[60:63], a[16:31]\n" \
    "buffer_load_dwordx4 v11, s[60:63], s67 offen lds\n" \
    "v_exp_f32 v49, v19\n" \
    "v_exp_f32 v20, v20\n" \
    "v_mov_b32_sdwa v48, v16 dst_sel:WORD_0 dst_unused:UNUSED_PRESERVE src0_sel:WORD_1\n" \
    "v_mfma_f32_32x32x8_bf16 v[32:47], v[76:77], a[214:215], 0\n" \
    "v_exp_f32 v50, v21\n" \
    "v_mov_b32_sdwa v49, v18 dst_sel:WORD_0 dst_unused:UNUSED_PRESERVE src0_sel:WORD_1\n" \
    "v_exp_f32 v22, v22\n" \
    "v_mov_b32_sdwa v50, v20 dst_sel:WORD_0 dst_unused:UNUSED_PRESERVE src0_sel:WORD_1\n" \
    "v_mfma_f32_16x16x32_bf16 a[136:139], v[80:83], v[200:203], a[136:139]\n" \
    "v_exp_f32 v51, v23\n" \
    "v_mfma_f32_16x16x32_bf16 a[136:139], v[84:87], v[204:207], a[136:139]\n" \
    "v_exp_f32 v24, v24\n" \
    "v_mfma_f32_32x32x16_bf16 v[32:47], v[64:67], a[204:207], v[32:47]\n" \
    "ds_read_b128 v[64:67], v208 offset:5120\n" \
    "ds_read_b32 v76, v209 offset:5120\n" \
    "v_exp_f32 v52, v25\n" \
    "v_mov_b32_sdwa v51, v22 dst_sel:WORD_0 dst_unused:UNUSED_PRESERVE src0_sel:WORD_1\n" \
    "v_exp_f32 v26, v26\n" \
    "v_mov_b32_sdwa v52, v24 dst_sel:WORD_0 dst_unused:UNUSED_PRESERVE src0_sel:WORD_1\n" \
    "v_mfma_f32_16x16x32_bf16 a[140:143], v[88:91], v[200:203], a[140:143]\n" \
    "v_exp_f32 v53, v27\n" \
    "v_mfma_f32_16x16x32_bf16 a[140:143], v[92:95], v[204:207], a[140:143]\n" \
    "v_exp_f32 v28, v28\n" \
    "v_mfma_f32_16x16x32_bf16 a[144:147], v[96:99], v[200:203], a[144:147]\n" \
    "v_exp_f32 v54, v29\n" \
    "v_mov_b32_sdwa v53, v26 dst_sel:WORD_0 dst_unused:UNUSED_PRESERVE src0_sel:WORD_1\n" \
    "v_mfma_f32_16x16x32_bf16 a[144:147], v[100:103], v[204:207], a[144:147]\n" \
    "v_accvgpr_read_b32 v136, a243\n" \
    "v_exp_f32 v30, v30\n" \
    "v_exp_f32 v55, v31\n" \
    "v_mov_b32_sdwa v54, v28 dst_sel:WORD_0 dst_unused:UNUSED_PRESERVE src0_sel:WORD_1\n" \
    "v_mov_b32_sdwa v55, v30 dst_sel:WORD_0 dst_unused:UNUSED_PRESERVE src0_sel:WORD_1\n" \
    "v_mfma_f32_32x32x16_bf16 a[32:47], v[68:71], v[48:51], a[32:47]\n" \
    "v_exp_f32 v32, v32\n" \
    "v_exp_f32 v56, v33\n" \
    "v_exp_f32 v34, v34\n" \
    "v_mfma_f32_32x32x16_bf16 a[32:47], v[72:75], v[52:55], a[32:47]\n" \
    "buffer_load_dwordx4 v12, s[60:63], s67 offen offset:1024 lds\n" \
    "s_add_u32 s69, s69, 0xc00\n" \
    "v_exp_f32 v57, v35\n" \
    "v_exp_f32 v36, v36\n" \
    "v_mov_b32_sdwa v56, v32 dst_sel:WORD_0 dst_unused:UNUSED_PRESERVE src0_sel:WORD_1\n" \
    "s_waitcnt lgkmcnt(0)\n" \
    "v_mfma_f32_32x32x8_bf16 v[16:31], v[76:77], a[208:209], 0\n" \
    "v_exp_f32 v58, v37\n" \
    "v_mov_b32_sdwa v57, v34 dst_sel:WORD_0 dst_unused:UNUSED_PRESERVE src0_sel:WORD_1\n" \
    "v_exp_f32 v38, v38\n" \
    "v_mov_b32_sdwa v58, v36 dst_sel:WORD_0 dst_unused:UNUSED_PRESERVE src0_sel:WORD_1\n" \
    "v_mfma_f32_16x16x32_bf16 a[148:151], v[104:107], v[200:203], a[148:151]\n" \
    "v_exp_f32 v59, v39\n" \
    "v_mfma_f32_16x16x32_bf16 a[148:151], v[108:111], v[204:207], a[148:151]\n" \
    "v_exp_f32 v40, v40\n" \
    "v_mfma_f32_32x32x16_bf16 v[16:31], v[64:67], a[192:195], v[16:31]\n" \
    "v_exp_f32 v60, v41\n" \
    "v_mov_b32_sdwa v59, v38 dst_sel:WORD_0 dst_unused:UNUSED_PRESERVE src0_sel:WORD_1\n" \
    "v_exp_f32 v42, v42\n" \
    "v_mov_b32_sdwa v60, v40 dst_sel:WORD_0 dst_unused:UNUSED_PRESERVE src0_sel:WORD_1\n" \
    "v_mfma_f32_16x16x32_bf16 a[152:155], v[112:115], v[200:203], a[152:155]\n" \
    "v_exp_f32 v61, v43\n" \
    "v_mfma_f32_16x16x32_bf16 a[152:155], v[116:119], v[204:207], a[152:155]\n" \
    "v_exp_f32 v44, v44\n" \
    "v_mfma_f32_16x16x32_bf16 a[156:159], v[120:123], v[200:203], a[156:159]\n" \
    "v_exp_f32 v62, v45\n" \
    "v_mov_b32_sdwa v61, v42 dst_sel:WORD_0 dst_unused:UNUSED_PRESERVE src0_sel:WORD_1\n" \
    "v_mfma_f32_16x16x32_bf16 a[156:159], v[124:127], v[204:207], a[156:159]\n" \
    "v_exp_f32 v46, v46\n" \
    "v_exp_f32 v63, v47\n" \
    "v_mov_b32_sdwa v62, v44 dst_sel:WORD_0 dst_unused:UNUSED_PRESERVE src0_sel:WORD_1\n" \
    "v_mov_b32_sdwa v63, v46 dst_sel:WORD_0 dst_unused:UNUSED_PRESERVE src0_sel:WORD_1\n" \
    "v_mfma_f32_32x32x16_bf16 a[48:63], v[68:71], v[56:59], a[48:63]\n" \
    "v_exp_f32 v16, v16\n" \
    "v_exp_f32 v48, v17\n" \
    "v_exp_f32 v18, v18\n" \
    "v_mfma_f32_32x32x16_bf16 a[48:63], v[72:75], v[60:63], a[48:63]\n" \
    "ds_read_b128 v[68:71], v210 offset:5120\n" \
    "v_exp_f32 v49, v19\n" \
    "v_exp_f32 v20, v20\n" \
    "v_mov_b32_sdwa v48, v16 dst_sel:WORD_0 dst_unused:UNUSED_PRESERVE src0_sel:WORD_1\n" \
    "v_mfma_f32_32x32x8_bf16 v[32:47], v[76:77], a[210:211], 0\n" \
    "ds_read_b128 v[72:75], v210 offset:5760\n" \
    "v_exp_f32 v50, v21\n" \
    "v_mov_b32_sdwa v49, v18 dst_sel:WORD_0 dst_unused:UNUSED_PRESERVE src0_sel:WORD_1\n" \
    "v_exp_f32 v22, v22\n" \
    "v_mov_b32_sdwa v50, v20 dst_sel:WORD_0 dst_unused:UNUSED_PRESERVE src0_sel:WORD_1\n" \
    "v_accvgpr_read_b32 v80, a64\n" \
    "v_accvgpr_read_b32 v81, a65\n" \
    "v_accvgpr_read_b32 v82, a66\n" \
    "v_exp_f32 v51, v23\n" \
    "v_accvgpr_read_b32 v83, a67\n" \
    "v_accvgpr_read_b32 v84, a68\n" \
    "v_accvgpr_read_b32 v85, a69\n" \
    "v_accvgpr_read_b32 v86, a70\n" \
    "v_exp_f32 v24, v24\n" \
    "v_mfma_f32_32x32x16_bf16 v[32:47], v[64:67], a[196:199], v[32:47]\n" \
    "buffer_load_dwordx4 v13, s[60:63], s67 offen offset:2048 lds\n" \
    "s_add_u32 s67, s67, 18432\n" \
    "v_exp_f32 v52, v25\n" \
    "v_mov_b32_sdwa v51, v22 dst_sel:WORD_0 dst_unused:UNUSED_PRESERVE src0_sel:WORD_1\n" \
    "v_exp_f32 v26, v26\n" \
    "v_mov_b32_sdwa v52, v24 dst_sel:WORD_0 dst_unused:UNUSED_PRESERVE src0_sel:WORD_1\n" \
    "v_accvgpr_read_b32 v87, a71\n" \
    "v_cvt_pk_bf16_f32 v88, v80, v81\n" \
    "v_cvt_pk_bf16_f32 v89, v82, v83\n" \
    "v_cvt_pk_bf16_f32 v90, v84, v85\n" \
    "v_exp_f32 v53, v27\n" \
    "v_cvt_pk_bf16_f32 v91, v86, v87\n" \
    "buffer_store_dwordx4 v[88:91], v8, s[40:43], s68 offen offset:0 sc1\n" \
    "v_accvgpr_read_b32 v92, a72\n" \
    "v_exp_f32 v28, v28\n" \
    "v_accvgpr_read_b32 v93, a73\n" \
    "v_accvgpr_read_b32 v94, a74\n" \
    "v_accvgpr_read_b32 v95, a75\n" \
    "v_exp_f32 v54, v29\n" \
    "v_mov_b32_sdwa v53, v26 dst_sel:WORD_0 dst_unused:UNUSED_PRESERVE src0_sel:WORD_1\n" \
    "v_accvgpr_read_b32 v96, a76\n" \
    "v_accvgpr_read_b32 v97, a77\n" \
    "v_accvgpr_read_b32 v98, a78\n" \
    "v_exp_f32 v30, v30\n" \
    "v_exp_f32 v55, v31\n" \
    "v_mov_b32_sdwa v54, v28 dst_sel:WORD_0 dst_unused:UNUSED_PRESERVE src0_sel:WORD_1\n" \
    "v_mov_b32_sdwa v55, v30 dst_sel:WORD_0 dst_unused:UNUSED_PRESERVE src0_sel:WORD_1\n" \
    "s_waitcnt lgkmcnt(0)\n" \
    "v_mfma_f32_32x32x16_bf16 a[0:15], v[68:71], v[48:51], a[0:15]\n" \
    "v_exp_f32 v32, v32\n" \
    "v_exp_f32 v56, v33\n" \
    "v_exp_f32 v34, v34\n" \
    "v_mfma_f32_32x32x16_bf16 a[0:15], v[72:75], v[52:55], a[0:15]\n" \
    "v_exp_f32 v57, v35\n" \
    "v_exp_f32 v36, v36\n" \
    "v_mov_b32_sdwa v56, v32 dst_sel:WORD_0 dst_unused:UNUSED_PRESERVE src0_sel:WORD_1\n" \
    "v_mfma_f32_32x32x8_bf16 v[16:31], v[76:77], a[212:213], 0\n" \
    "v_exp_f32 v58, v37\n" \
    "v_mov_b32_sdwa v57, v34 dst_sel:WORD_0 dst_unused:UNUSED_PRESERVE src0_sel:WORD_1\n" \
    "v_exp_f32 v38, v38\n" \
    "v_mov_b32_sdwa v58, v36 dst_sel:WORD_0 dst_unused:UNUSED_PRESERVE src0_sel:WORD_1\n" \
    "v_accvgpr_read_b32 v99, a79\n" \
    "v_cvt_pk_bf16_f32 v100, v92, v93\n" \
    "v_cvt_pk_bf16_f32 v101, v94, v95\n" \
    "v_exp_f32 v59, v39\n" \
    "v_cvt_pk_bf16_f32 v102, v96, v97\n" \
    "v_cvt_pk_bf16_f32 v103, v98, v99\n" \
    "buffer_store_dwordx4 v[100:103], v8, s[40:43], s68 offen offset:1024 sc1\n" \
    "v_accvgpr_read_b32 v104, a80\n" \
    "v_exp_f32 v40, v40\n" \
    "v_mfma_f32_32x32x16_bf16 v[16:31], v[64:67], a[200:203], v[16:31]\n" \
    "v_exp_f32 v60, v41\n" \
    "v_mov_b32_sdwa v59, v38 dst_sel:WORD_0 dst_unused:UNUSED_PRESERVE src0_sel:WORD_1\n" \
    "v_exp_f32 v42, v42\n" \
    "v_mov_b32_sdwa v60, v40 dst_sel:WORD_0 dst_unused:UNUSED_PRESERVE src0_sel:WORD_1\n" \
    "v_accvgpr_read_b32 v105, a81\n" \
    "v_accvgpr_read_b32 v106, a82\n" \
    "v_accvgpr_read_b32 v107, a83\n" \
    "v_accvgpr_read_b32 v108, a84\n" \
    "v_exp_f32 v61, v43\n" \
    "v_accvgpr_read_b32 v109, a85\n" \
    "v_accvgpr_read_b32 v110, a86\n" \
    "v_accvgpr_read_b32 v111, a87\n" \
    "v_exp_f32 v44, v44\n" \
    "v_cvt_pk_bf16_f32 v112, v104, v105\n" \
    "v_cvt_pk_bf16_f32 v113, v106, v107\n" \
    "v_cvt_pk_bf16_f32 v114, v108, v109\n" \
    "v_exp_f32 v62, v45\n" \
    "v_mov_b32_sdwa v61, v42 dst_sel:WORD_0 dst_unused:UNUSED_PRESERVE src0_sel:WORD_1\n" \
    "v_cvt_pk_bf16_f32 v115, v110, v111\n" \
    "buffer_store_dwordx4 v[112:115], v8, s[40:43], s68 offen offset:2048 sc1\n" \
    "s_add_u32 s68, s68, 18432\n" \
    "v_accvgpr_read_b32 v116, a88\n" \
    "buffer_load_dwordx4 v[164:167], v136, s[84:87], s47 offen offset:0\n" \
    "buffer_load_dwordx4 v[168:171], v136, s[84:87], s47 offen offset:64\n" \
    "buffer_load_dwordx4 v[172:175], v136, s[84:87], s47 offen offset:128\n" \
    "v_exp_f32 v46, v46\n" \
    "v_exp_f32 v63, v47\n" \
    "v_mov_b32_sdwa v62, v44 dst_sel:WORD_0 dst_unused:UNUSED_PRESERVE src0_sel:WORD_1\n" \
    "v_mov_b32_sdwa v63, v46 dst_sel:WORD_0 dst_unused:UNUSED_PRESERVE src0_sel:WORD_1\n" \
    "v_mfma_f32_32x32x16_bf16 a[16:31], v[68:71], v[56:59], a[16:31]\n" \
    "v_exp_f32 v16, v16\n" \
    "v_exp_f32 v48, v17\n" \
    "v_exp_f32 v18, v18\n" \
    "v_mfma_f32_32x32x16_bf16 a[16:31], v[72:75], v[60:63], a[16:31]\n" \
    "v_exp_f32 v49, v19\n" \
    "v_exp_f32 v20, v20\n" \
    "v_mov_b32_sdwa v48, v16 dst_sel:WORD_0 dst_unused:UNUSED_PRESERVE src0_sel:WORD_1\n" \
    "v_mfma_f32_32x32x8_bf16 v[32:47], v[76:77], a[214:215], 0\n" \
    "v_exp_f32 v50, v21\n" \
    "v_mov_b32_sdwa v49, v18 dst_sel:WORD_0 dst_unused:UNUSED_PRESERVE src0_sel:WORD_1\n" \
    "v_exp_f32 v22, v22\n" \
    "v_mov_b32_sdwa v50, v20 dst_sel:WORD_0 dst_unused:UNUSED_PRESERVE src0_sel:WORD_1\n" \
    "v_accvgpr_read_b32 v117, a89\n" \
    "v_accvgpr_read_b32 v118, a90\n" \
    "v_accvgpr_read_b32 v119, a91\n" \
    "v_exp_f32 v51, v23\n" \
    "v_accvgpr_read_b32 v120, a92\n" \
    "v_accvgpr_read_b32 v121, a93\n" \
    "v_accvgpr_read_b32 v122, a94\n" \
    "v_accvgpr_read_b32 v123, a95\n" \
    "v_exp_f32 v24, v24\n" \
    "v_mfma_f32_32x32x16_bf16 v[32:47], v[64:67], a[204:207], v[32:47]\n" \
    "ds_read_b128 v[64:67], v208 offset:7680\n" \
    "ds_read_b32 v76, v209 offset:7680\n" \
    "v_exp_f32 v52, v25\n" \
    "v_mov_b32_sdwa v51, v22 dst_sel:WORD_0 dst_unused:UNUSED_PRESERVE src0_sel:WORD_1\n" \
    "v_exp_f32 v26, v26\n" \
    "v_mov_b32_sdwa v52, v24 dst_sel:WORD_0 dst_unused:UNUSED_PRESERVE src0_sel:WORD_1\n" \
    "v_cvt_pk_bf16_f32 v124, v116, v117\n" \
    "v_cvt_pk_bf16_f32 v125, v118, v119\n" \
    "v_cvt_pk_bf16_f32 v126, v120, v121\n" \
    "v_cvt_pk_bf16_f32 v127, v122, v123\n" \
    "v_exp_f32 v53, v27\n" \
    "buffer_store_dwordx4 v[124:127], v8, s[40:43], s68 offen offset:0 sc1\n" \
    "v_accvgpr_read_b32 v80, a96\n" \
    "v_accvgpr_read_b32 v81, a97\n" \
    "v_exp_f32 v28, v28\n" \
    "v_accvgpr_read_b32 v82, a98\n" \
    "v_accvgpr_read_b32 v83, a99\n" \
    "v_accvgpr_read_b32 v84, a100\n" \
    "v_exp_f32 v54, v29\n" \
    "v_mov_b32_sdwa v53, v26 dst_sel:WORD_0 dst_unused:UNUSED_PRESERVE src0_sel:WORD_1\n" \
    "v_accvgpr_read_b32 v85, a101\n" \
    "v_accvgpr_read_b32 v86, a102\n" \
    "v_accvgpr_read_b32 v87, a103\n" \
    "v_exp_f32 v30, v30\n" \
    "v_exp_f32 v55, v31\n" \
    "v_mov_b32_sdwa v54, v28 dst_sel:WORD_0 dst_unused:UNUSED_PRESERVE src0_sel:WORD_1\n" \
    "v_mov_b32_sdwa v55, v30 dst_sel:WORD_0 dst_unused:UNUSED_PRESERVE src0_sel:WORD_1\n" \
    "v_mfma_f32_32x32x16_bf16 a[32:47], v[68:71], v[48:51], a[32:47]\n" \
    "v_exp_f32 v32, v32\n" \
    "v_exp_f32 v56, v33\n" \
    "v_exp_f32 v34, v34\n" \
    "v_mfma_f32_32x32x16_bf16 a[32:47], v[72:75], v[52:55], a[32:47]\n" \
    "v_exp_f32 v57, v35\n" \
    "v_exp_f32 v36, v36\n" \
    "v_mov_b32_sdwa v56, v32 dst_sel:WORD_0 dst_unused:UNUSED_PRESERVE src0_sel:WORD_1\n" \
    "s_waitcnt lgkmcnt(0)\n" \
    "v_mfma_f32_32x32x8_bf16 v[16:31], v[76:77], a[208:209], 0\n" \
    "v_exp_f32 v58, v37\n" \
    "v_mov_b32_sdwa v57, v34 dst_sel:WORD_0 dst_unused:UNUSED_PRESERVE src0_sel:WORD_1\n" \
    "v_exp_f32 v38, v38\n" \
    "v_mov_b32_sdwa v58, v36 dst_sel:WORD_0 dst_unused:UNUSED_PRESERVE src0_sel:WORD_1\n" \
    "v_cvt_pk_bf16_f32 v88, v80, v81\n" \
    "v_cvt_pk_bf16_f32 v89, v82, v83\n" \
    "v_cvt_pk_bf16_f32 v90, v84, v85\n" \
    "v_exp_f32 v59, v39\n" \
    "v_cvt_pk_bf16_f32 v91, v86, v87\n" \
    "buffer_store_dwordx4 v[88:91], v8, s[40:43], s68 offen offset:1024 sc1\n" \
    "v_accvgpr_read_b32 v92, a104\n" \
    "v_accvgpr_read_b32 v93, a105\n" \
    "v_exp_f32 v40, v40\n" \
    "v_mfma_f32_32x32x16_bf16 v[16:31], v[64:67], a[192:195], v[16:31]\n" \
    "v_exp_f32 v60, v41\n" \
    "v_mov_b32_sdwa v59, v38 dst_sel:WORD_0 dst_unused:UNUSED_PRESERVE src0_sel:WORD_1\n" \
    "v_exp_f32 v42, v42\n" \
    "v_mov_b32_sdwa v60, v40 dst_sel:WORD_0 dst_unused:UNUSED_PRESERVE src0_sel:WORD_1\n" \
    "v_accvgpr_read_b32 v94, a106\n" \
    "v_accvgpr_read_b32 v95, a107\n" \
    "v_accvgpr_read_b32 v96, a108\n" \
    "v_accvgpr_read_b32 v97, a109\n" \
    "v_exp_f32 v61, v43\n" \
    "v_accvgpr_read_b32 v98, a110\n" \
    "v_accvgpr_read_b32 v99, a111\n" \
    "v_cvt_pk_bf16_f32 v100, v92, v93\n" \
    "v_exp_f32 v44, v44\n" \
    "v_cvt_pk_bf16_f32 v101, v94, v95\n" \
    "v_cvt_pk_bf16_f32 v102, v96, v97\n" \
    "v_cvt_pk_bf16_f32 v103, v98, v99\n" \
    "v_exp_f32 v62, v45\n" \
    "v_mov_b32_sdwa v61, v42 dst_sel:WORD_0 dst_unused:UNUSED_PRESERVE src0_sel:WORD_1\n" \
    "buffer_store_dwordx4 v[100:103], v8, s[40:43], s68 offen offset:2048 sc1\n" \
    "s_add_u32 s68, s68, 18432\n" \
    "v_accvgpr_read_b32 v104, a112\n" \
    "v_accvgpr_read_b32 v105, a113\n" \
    "v_exp_f32 v46, v46\n" \
    "v_exp_f32 v63, v47\n" \
    "v_mov_b32_sdwa v62, v44 dst_sel:WORD_0 dst_unused:UNUSED_PRESERVE src0_sel:WORD_1\n" \
    "v_mov_b32_sdwa v63, v46 dst_sel:WORD_0 dst_unused:UNUSED_PRESERVE src0_sel:WORD_1\n" \
    "v_mfma_f32_32x32x16_bf16 a[48:63], v[68:71], v[56:59], a[48:63]\n" \
    "v_exp_f32 v16, v16\n" \
    "v_exp_f32 v48, v17\n" \
    "v_exp_f32 v18, v18\n" \
    "v_mfma_f32_32x32x16_bf16 a[48:63], v[72:75], v[60:63], a[48:63]\n" \
    "ds_read_b128 v[68:71], v210 offset:7680\n" \
    "v_exp_f32 v49, v19\n" \
    "v_exp_f32 v20, v20\n" \
    "v_mov_b32_sdwa v48, v16 dst_sel:WORD_0 dst_unused:UNUSED_PRESERVE src0_sel:WORD_1\n" \
    "v_mfma_f32_32x32x8_bf16 v[32:47], v[76:77], a[210:211], 0\n" \
    "ds_read_b128 v[72:75], v210 offset:8320\n" \
    "v_exp_f32 v50, v21\n" \
    "v_mov_b32_sdwa v49, v18 dst_sel:WORD_0 dst_unused:UNUSED_PRESERVE src0_sel:WORD_1\n" \
    "v_exp_f32 v22, v22\n" \
    "v_mov_b32_sdwa v50, v20 dst_sel:WORD_0 dst_unused:UNUSED_PRESERVE src0_sel:WORD_1\n" \
    "v_accvgpr_read_b32 v106, a114\n" \
    "v_accvgpr_read_b32 v107, a115\n" \
    "v_accvgpr_read_b32 v108, a116\n" \
    "v_exp_f32 v51, v23\n" \
    "v_accvgpr_read_b32 v109, a117\n" \
    "v_accvgpr_read_b32 v110, a118\n" \
    "v_accvgpr_read_b32 v111, a119\n" \
    "v_cvt_pk_bf16_f32 v112, v104, v105\n" \
    "v_exp_f32 v24, v24\n" \
    "v_mfma_f32_32x32x16_bf16 v[32:47], v[64:67], a[196:199], v[32:47]\n" \
    "v_exp_f32 v52, v25\n" \
    "v_mov_b32_sdwa v51, v22 dst_sel:WORD_0 dst_unused:UNUSED_PRESERVE src0_sel:WORD_1\n" \
    "v_exp_f32 v26, v26\n" \
    "v_mov_b32_sdwa v52, v24 dst_sel:WORD_0 dst_unused:UNUSED_PRESERVE src0_sel:WORD_1\n" \
    "v_cvt_pk_bf16_f32 v113, v106, v107\n" \
    "v_cvt_pk_bf16_f32 v114, v108, v109\n" \
    "v_cvt_pk_bf16_f32 v115, v110, v111\n" \
    "buffer_store_dwordx4 v[112:115], v8, s[40:43], s68 offen offset:0 sc1\n" \
    "v_exp_f32 v53, v27\n" \
    "v_accvgpr_read_b32 v116, a120\n" \
    "v_accvgpr_read_b32 v117, a121\n" \
    "v_accvgpr_read_b32 v118, a122\n" \
    "v_exp_f32 v28, v28\n" \
    "v_accvgpr_read_b32 v119, a123\n" \
    "v_accvgpr_read_b32 v120, a124\n" \
    "v_accvgpr_read_b32 v121, a125\n" \
    "v_exp_f32 v54, v29\n" \
    "v_mov_b32_sdwa v53, v26 dst_sel:WORD_0 dst_unused:UNUSED_PRESERVE src0_sel:WORD_1\n" \
    "v_accvgpr_read_b32 v122, a126\n" \
    "v_accvgpr_read_b32 v123, a127\n" \
    "v_cvt_pk_bf16_f32 v124, v116, v117\n" \
    "v_exp_f32 v30, v30\n" \
    "v_exp_f32 v55, v31\n" \
    "v_mov_b32_sdwa v54, v28 dst_sel:WORD_0 dst_unused:UNUSED_PRESERVE src0_sel:WORD_1\n" \
    "v_mov_b32_sdwa v55, v30 dst_sel:WORD_0 dst_unused:UNUSED_PRESERVE src0_sel:WORD_1\n" \
    "s_waitcnt lgkmcnt(0)\n" \
    "v_mfma_f32_32x32x16_bf16 a[0:15], v[68:71], v[48:51], a[0:15]\n" \
    "v_exp_f32 v32, v32\n" \
    "v_exp_f32 v56, v33\n" \
    "v_exp_f32 v34, v34\n" \
    "v_mfma_f32_32x32x16_bf16 a[0:15], v[72:75], v[52:55], a[0:15]\n" \
    "v_exp_f32 v57, v35\n" \
    "v_exp_f32 v36, v36\n" \
    "v_mov_b32_sdwa v56, v32 dst_sel:WORD_0 dst_unused:UNUSED_PRESERVE src0_sel:WORD_1\n" \
    "v_mfma_f32_32x32x8_bf16 v[16:31], v[76:77], a[212:213], 0\n" \
    "v_exp_f32 v58, v37\n" \
    "v_mov_b32_sdwa v57, v34 dst_sel:WORD_0 dst_unused:UNUSED_PRESERVE src0_sel:WORD_1\n" \
    "v_exp_f32 v38, v38\n" \
    "v_mov_b32_sdwa v58, v36 dst_sel:WORD_0 dst_unused:UNUSED_PRESERVE src0_sel:WORD_1\n" \
    "v_cvt_pk_bf16_f32 v125, v118, v119\n" \
    "v_cvt_pk_bf16_f32 v126, v120, v121\n" \
    "v_cvt_pk_bf16_f32 v127, v122, v123\n" \
    "v_exp_f32 v59, v39\n" \
    "buffer_store_dwordx4 v[124:127], v8, s[40:43], s68 offen offset:1024 sc1\n" \
    "v_accvgpr_read_b32 v80, a128\n" \
    "v_accvgpr_read_b32 v81, a129\n" \
    "v_accvgpr_read_b32 v82, a130\n" \
    "v_exp_f32 v40, v40\n" \
    "v_mfma_f32_32x32x16_bf16 v[16:31], v[64:67], a[200:203], v[16:31]\n" \
    "v_exp_f32 v60, v41\n" \
    "v_mov_b32_sdwa v59, v38 dst_sel:WORD_0 dst_unused:UNUSED_PRESERVE src0_sel:WORD_1\n" \
    "v_exp_f32 v42, v42\n" \
    "v_mov_b32_sdwa v60, v40 dst_sel:WORD_0 dst_unused:UNUSED_PRESERVE src0_sel:WORD_1\n" \
    "v_accvgpr_read_b32 v83, a131\n" \
    "v_accvgpr_read_b32 v84, a132\n" \
    "v_accvgpr_read_b32 v85, a133\n" \
    "v_accvgpr_read_b32 v86, a134\n" \
    "v_exp_f32 v61, v43\n" \
    "v_accvgpr_read_b32 v87, a135\n" \
    "v_cvt_pk_bf16_f32 v88, v80, v81\n" \
    "v_cvt_pk_bf16_f32 v89, v82, v83\n" \
    "v_exp_f32 v44, v44\n" \
    "v_cvt_pk_bf16_f32 v90, v84, v85\n" \
    "v_cvt_pk_bf16_f32 v91, v86, v87\n" \
    "buffer_store_dwordx4 v[88:91], v8, s[40:43], s68 offen offset:2048 sc1\n" \
    "s_add_u32 s68, s68, 18432\n" \
    "v_exp_f32 v62, v45\n" \
    "v_mov_b32_sdwa v61, v42 dst_sel:WORD_0 dst_unused:UNUSED_PRESERVE src0_sel:WORD_1\n" \
    "v_accvgpr_read_b32 v92, a136\n" \
    "v_accvgpr_read_b32 v93, a137\n" \
    "v_accvgpr_read_b32 v94, a138\n" \
    "v_exp_f32 v46, v46\n" \
    "v_exp_f32 v63, v47\n" \
    "v_mov_b32_sdwa v62, v44 dst_sel:WORD_0 dst_unused:UNUSED_PRESERVE src0_sel:WORD_1\n" \
    "v_mov_b32_sdwa v63, v46 dst_sel:WORD_0 dst_unused:UNUSED_PRESERVE src0_sel:WORD_1\n" \
    "v_mfma_f32_32x32x16_bf16 a[16:31], v[68:71], v[56:59], a[16:31]\n" \
    "v_exp_f32 v16, v16\n" \
    "v_exp_f32 v48, v17\n" \
    "v_exp_f32 v18, v18\n" \
    "v_mfma_f32_32x32x16_bf16 a[16:31], v[72:75], v[60:63], a[16:31]\n" \
    "v_exp_f32 v49, v19\n" \
    "v_exp_f32 v20, v20\n" \
    "v_mov_b32_sdwa v48, v16 dst_sel:WORD_0 dst_unused:UNUSED_PRESERVE src0_sel:WORD_1\n" \
    "v_mfma_f32_32x32x8_bf16 v[32:47], v[76:77], a[214:215], 0\n" \
    "v_exp_f32 v50, v21\n" \
    "v_mov_b32_sdwa v49, v18 dst_sel:WORD_0 dst_unused:UNUSED_PRESERVE src0_sel:WORD_1\n" \
    "v_exp_f32 v22, v22\n" \
    "v_mov_b32_sdwa v50, v20 dst_sel:WORD_0 dst_unused:UNUSED_PRESERVE src0_sel:WORD_1\n" \
    "v_accvgpr_read_b32 v95, a139\n" \
    "v_accvgpr_read_b32 v96, a140\n" \
    "v_accvgpr_read_b32 v97, a141\n" \
    "v_exp_f32 v51, v23\n" \
    "v_accvgpr_read_b32 v98, a142\n" \
    "v_accvgpr_read_b32 v99, a143\n" \
    "v_cvt_pk_bf16_f32 v100, v92, v93\n" \
    "v_cvt_pk_bf16_f32 v101, v94, v95\n" \
    "v_exp_f32 v24, v24\n" \
    "v_mfma_f32_32x32x16_bf16 v[32:47], v[64:67], a[204:207], v[32:47]\n" \
    "v_exp_f32 v52, v25\n" \
    "v_mov_b32_sdwa v51, v22 dst_sel:WORD_0 dst_unused:UNUSED_PRESERVE src0_sel:WORD_1\n" \
    "v_exp_f32 v26, v26\n" \
    "v_mov_b32_sdwa v52, v24 dst_sel:WORD_0 dst_unused:UNUSED_PRESERVE src0_sel:WORD_1\n" \
    "v_cvt_pk_bf16_f32 v102, v96, v97\n" \
    "v_cvt_pk_bf16_f32 v103, v98, v99\n" \
    "buffer_store_dwordx4 v[100:103], v8, s[40:43], s68 offen offset:0 sc1\n" \
    "v_accvgpr_read_b32 v104, a144\n" \
    "v_exp_f32 v53, v27\n" \
    "v_accvgpr_read_b32 v105, a145\n" \
    "v_accvgpr_read_b32 v106, a146\n" \
    "v_accvgpr_read_b32 v107, a147\n" \
    "v_exp_f32 v28, v28\n" \
    "v_accvgpr_read_b32 v108, a148\n" \
    "v_accvgpr_read_b32 v109, a149\n" \
    "v_accvgpr_read_b32 v110, a150\n" \
    "v_exp_f32 v54, v29\n" \
    "v_mov_b32_sdwa v53, v26 dst_sel:WORD_0 dst_unused:UNUSED_PRESERVE src0_sel:WORD_1\n" \
    "v_accvgpr_read_b32 v111, a151\n" \
    "v_cvt_pk_bf16_f32 v112, v104, v105\n" \
    "v_cvt_pk_bf16_f32 v113, v106, v107\n" \
    "v_exp_f32 v30, v30\n" \
    "v_exp_f32 v55, v31\n" \
    "v_mov_b32_sdwa v54, v28 dst_sel:WORD_0 dst_unused:UNUSED_PRESERVE src0_sel:WORD_1\n" \
    "v_mov_b32_sdwa v55, v30 dst_sel:WORD_0 dst_unused:UNUSED_PRESERVE src0_sel:WORD_1\n" \
    "v_mfma_f32_32x32x16_bf16 a[32:47], v[68:71], v[48:51], a[32:47]\n" \
    "v_exp_f32 v32, v32\n" \
    "v_exp_f32 v56, v33\n" \
    "v_exp_f32 v34, v34\n" \
    "v_mfma_f32_32x32x16_bf16 a[32:47], v[72:75], v[52:55], a[32:47]\n" \
    "v_exp_f32 v57, v35\n" \
    "v_exp_f32 v36, v36\n" \
    "v_mov_b32_sdwa v56, v32 dst_sel:WORD_0 dst_unused:UNUSED_PRESERVE src0_sel:WORD_1\n" \
    "v_exp_f32 v58, v37\n" \
    "v_mov_b32_sdwa v57, v34 dst_sel:WORD_0 dst_unused:UNUSED_PRESERVE src0_sel:WORD_1\n" \
    "v_exp_f32 v38, v38\n" \
    "v_mov_b32_sdwa v58, v36 dst_sel:WORD_0 dst_unused:UNUSED_PRESERVE src0_sel:WORD_1\n" \
    "v_cvt_pk_bf16_f32 v114, v108, v109\n" \
    "v_cvt_pk_bf16_f32 v115, v110, v111\n" \
    "buffer_store_dwordx4 v[112:115], v8, s[40:43], s68 offen offset:1024 sc1\n" \
    "v_exp_f32 v59, v39\n" \
    "v_accvgpr_read_b32 v116, a152\n" \
    "v_accvgpr_read_b32 v117, a153\n" \
    "v_accvgpr_read_b32 v118, a154\n" \
    "v_accvgpr_read_b32 v119, a155\n" \
    "v_exp_f32 v40, v40\n" \
    "v_exp_f32 v60, v41\n" \
    "v_mov_b32_sdwa v59, v38 dst_sel:WORD_0 dst_unused:UNUSED_PRESERVE src0_sel:WORD_1\n" \
    "v_exp_f32 v42, v42\n" \
    "v_mov_b32_sdwa v60, v40 dst_sel:WORD_0 dst_unused:UNUSED_PRESERVE src0_sel:WORD_1\n" \
    "v_accvgpr_read_b32 v120, a156\n" \
    "v_accvgpr_read_b32 v121, a157\n" \
    "v_accvgpr_read_b32 v122, a158\n" \
    "v_accvgpr_read_b32 v123, a159\n" \
    "v_exp_f32 v61, v43\n" \
    "v_cvt_pk_bf16_f32 v124, v116, v117\n" \
    "v_cvt_pk_bf16_f32 v125, v118, v119\n" \
    "v_cvt_pk_bf16_f32 v126, v120, v121\n" \
    "v_exp_f32 v44, v44\n" \
    "v_cvt_pk_bf16_f32 v127, v122, v123\n" \
    "buffer_store_dwordx4 v[124:127], v8, s[40:43], s68 offen offset:2048 sc1\n" \
    "s_add_u32 s68, s68, 18432\n" \
    "v_exp_f32 v62, v45\n" \
    "v_mov_b32_sdwa v61, v42 dst_sel:WORD_0 dst_unused:UNUSED_PRESERVE src0_sel:WORD_1\n" \
    "v_exp_f32 v46, v46\n" \
    "v_exp_f32 v63, v47\n" \
    "v_mov_b32_sdwa v62, v44 dst_sel:WORD_0 dst_unused:UNUSED_PRESERVE src0_sel:WORD_1\n" \
    "v_mov_b32_sdwa v63, v46 dst_sel:WORD_0 dst_unused:UNUSED_PRESERVE src0_sel:WORD_1\n" \
    "s_nop 1\n" \
    "v_mfma_f32_32x32x16_bf16 a[48:63], v[68:71], v[56:59], a[48:63]\n" \
    "v_mfma_f32_32x32x16_bf16 a[48:63], v[72:75], v[60:63], a[48:63]\n" \
    "s_cmp_lt_u32 %6, 2\n" \
    "s_nop 7\n" \
    "s_cbranch_scc0 T_KH1%=\n" \
    "ds_write_b128 v130, a[32:35] offset:0\n" \
    "ds_write_b128 v130, a[36:39] offset:1024\n" \
    "ds_write_b128 v130, a[40:43] offset:2048\n" \
    "ds_write_b128 v130, a[48:51] offset:3072\n" \
    "ds_write_b128 v130, a[52:55] offset:4096\n" \
    "ds_write_b128 v130, a[56:59] offset:5120\n" \
    "v_accvgpr_read_b32 v16, a0\n" \
    "v_accvgpr_read_b32 v17, a1\n" \
    "v_accvgpr_read_b32 v18, a2\n" \
    "v_accvgpr_read_b32 v19, a3\n" \
    "v_accvgpr_read_b32 v20, a4\n" \
    "v_accvgpr_read_b32 v21, a5\n" \
    "v_accvgpr_read_b32 v22, a6\n" \
    "v_accvgpr_read_b32 v23, a7\n" \
    "v_accvgpr_read_b32 v24, a8\n" \
    "v_accvgpr_read_b32 v25, a9\n" \
    "v_accvgpr_read_b32 v26, a10\n" \
    "v_accvgpr_read_b32 v28, a16\n" \
    "v_accvgpr_read_b32 v29, a17\n" \
    "v_accvgpr_read_b32 v30, a18\n" \
    "v_accvgpr_read_b32 v31, a19\n" \
    "v_accvgpr_read_b32 v32, a20\n" \
    "v_accvgpr_read_b32 v33, a21\n" \
    "v_accvgpr_read_b32 v34, a22\n" \
    "v_accvgpr_read_b32 v35, a23\n" \
    "v_accvgpr_read_b32 v36, a24\n" \
    "v_accvgpr_read_b32 v37, a25\n" \
    "v_accvgpr_read_b32 v38, a26\n" \
    "s_branch T_KH2%=\n" \
    "T_KH1%=:\n" \
    "ds_write_b128 v130, a[0:3] offset:0\n" \
    "ds_write_b128 v130, a[4:7] offset:1024\n" \
    "ds_write_b128 v130, a[8:11] offset:2048\n" \
    "ds_write_b128 v130, a[16:19] offset:3072\n" \
    "ds_write_b128 v130, a[20:23] offset:4096\n" \
    "ds_write_b128 v130, a[24:27] offset:5120\n" \
    "v_accvgpr_read_b32 v16, a32\n" \
    "v_accvgpr_read_b32 v17, a33\n" \
    "v_accvgpr_read_b32 v18, a34\n" \
    "v_accvgpr_read_b32 v19, a35\n" \
    "v_accvgpr_read_b32 v20, a36\n" \
    "v_accvgpr_read_b32 v21, a37\n" \
    "v_accvgpr_read_b32 v22, a38\n" \
    "v_accvgpr_read_b32 v23, a39\n" \
    "v_accvgpr_read_b32 v24, a40\n" \
    "v_accvgpr_read_b32 v25, a41\n" \
    "v_accvgpr_read_b32 v26, a42\n" \
    "v_accvgpr_read_b32 v28, a48\n" \
    "v_accvgpr_read_b32 v29, a49\n" \
    "v_accvgpr_read_b32 v30, a50\n" \
    "v_accvgpr_read_b32 v31, a51\n" \
    "v_accvgpr_read_b32 v32, a52\n" \
    "v_accvgpr_read_b32 v33, a53\n" \
    "v_accvgpr_read_b32 v34, a54\n" \
    "v_accvgpr_read_b32 v35, a55\n" \
    "v_accvgpr_read_b32 v36, a56\n" \
    "v_accvgpr_read_b32 v37, a57\n" \
    "v_accvgpr_read_b32 v38, a58\n" \
    "T_KH2%=:\n" \
    "s_mov_b32 s60, 0x76453719\n" \
    "s_waitcnt vmcnt(15)\n" \
    "s_waitcnt lgkmcnt(0)\n" \
    "s_barrier\n" \
    "ds_read_b128 v[44:47], v131 offset:0\n" \
    "ds_read_b128 v[48:51], v131 offset:1024\n" \
    "ds_read_b128 v[52:55], v131 offset:2048\n" \
    "ds_read_b128 v[56:59], v131 offset:3072\n" \
    "ds_read_b128 v[60:63], v131 offset:4096\n" \
    "ds_read_b128 v[64:67], v131 offset:5120\n" \
    "s_waitcnt lgkmcnt(3)\n" \
    "v_add_f32 v16, v16, v44\n" \
    "v_add_f32 v17, v17, v45\n" \
    "v_add_f32 v18, v18, v46\n" \
    "v_add_f32 v19, v19, v47\n" \
    "v_add_f32 v20, v20, v48\n" \
    "v_add_f32 v21, v21, v49\n" \
    "v_add_f32 v22, v22, v50\n" \
    "v_add_f32 v23, v23, v51\n" \
    "v_add_f32 v24, v24, v52\n" \
    "v_add_f32 v25, v25, v53\n" \
    "v_add_f32 v26, v26, v54\n" \
    "v_fma_f32 v27, v16, 0, v26\n" \
    "v_fma_f32 v27, v17, 0, v27\n" \
    "v_fma_f32 v27, v18, 0, v27\n" \
    "v_fma_f32 v27, v19, 0, v27\n" \
    "v_fma_f32 v27, v20, 0, v27\n" \
    "v_fma_f32 v27, v21, 0, v27\n" \
    "v_fma_f32 v27, v22, 0, v27\n" \
    "v_fma_f32 v27, v23, 0, v27\n" \
    "v_fma_f32 v27, v24, 0, v27\n" \
    "v_fma_f32 v27, v25, 0, v27\n" \
    "s_waitcnt lgkmcnt(0)\n" \
    "v_add_f32 v28, v28, v56\n" \
    "v_add_f32 v29, v29, v57\n" \
    "v_add_f32 v30, v30, v58\n" \
    "v_add_f32 v31, v31, v59\n" \
    "v_add_f32 v32, v32, v60\n" \
    "v_add_f32 v33, v33, v61\n" \
    "v_add_f32 v34, v34, v62\n" \
    "v_add_f32 v35, v35, v63\n" \
    "v_add_f32 v36, v36, v64\n" \
    "v_add_f32 v37, v37, v65\n" \
    "v_add_f32 v38, v38, v66\n" \
    "v_fma_f32 v39, v28, 0, v38\n" \
    "v_fma_f32 v39, v29, 0, v39\n" \
    "v_fma_f32 v39, v30, 0, v39\n" \
    "v_fma_f32 v39, v31, 0, v39\n" \
    "v_fma_f32 v39, v32, 0, v39\n" \
    "v_fma_f32 v39, v33, 0, v39\n" \
    "v_fma_f32 v39, v34, 0, v39\n" \
    "v_fma_f32 v39, v35, 0, v39\n" \
    "v_fma_f32 v39, v36, 0, v39\n" \
    "v_fma_f32 v39, v37, 0, v39\n" \
    "v_cmp_ngt_f32 s[62:63], s60, v27\n" \
    "v_cmp_ngt_f32 s[64:65], s60, v39\n" \
    "v_cmp_nlt_f32 s[60:61], 0, v27\n" \
    "v_cmp_nlt_f32 vcc, 0, v39\n" \
    "s_or_b64 s[62:63], s[64:65], s[62:63]\n" \
    "s_or_b64 s[60:61], s[60:61], vcc\n" \
    "s_or_b64 s[60:61], s[60:61], s[62:63]\n" \
    "s_cbranch_scc1 T_EXACT%=\n" \
    "T_NORM%=:\n" \
    "v_rcp_f32 v27, v26\n" \
    "v_rcp_f32 v39, v38\n" \
    "v_mul_f32 v16, v16, v27\n" \
    "v_mul_f32 v17, v17, v27\n" \
    "v_mul_f32 v18, v18, v27\n" \
    "v_mul_f32 v19, v19, v27\n" \
    "v_mul_f32 v20, v20, v27\n" \
    "v_mul_f32 v21, v21, v27\n" \
    "v_mul_f32 v22, v22, v27\n" \
    "v_mul_f32 v23, v23, v27\n" \
    "v_mul_f32 v24, v24, v27\n" \
    "v_mul_f32 v25, v25, v27\n" \
    "v_cvt_pk_bf16_f32 v44, v16, v17\n" \
    "v_cvt_pk_bf16_f32 v45, v18, v19\n" \
    "v_cvt_pk_bf16_f32 v46, v20, v21\n" \
    "v_cvt_pk_bf16_f32 v47, v22, v23\n" \
    "v_cvt_pk_bf16_f32 v48, v24, v25\n" \
    "buffer_store_dwordx4 v[44:47], v135, s[40:43], s44 offen sc1\n" \
    "buffer_store_dword v48, v133, s[40:43], s44 offen sc1\n" \
    "s_add_u32 s44, s44, 36864\n" \
    "v_mul_f32 v28, v28, v39\n" \
    "v_mul_f32 v29, v29, v39\n" \
    "v_mul_f32 v30, v30, v39\n" \
    "v_mul_f32 v31, v31, v39\n" \
    "v_mul_f32 v32, v32, v39\n" \
    "v_mul_f32 v33, v33, v39\n" \
    "v_mul_f32 v34, v34, v39\n" \
    "v_mul_f32 v35, v35, v39\n" \
    "v_mul_f32 v36, v36, v39\n" \
    "v_mul_f32 v37, v37, v39\n" \
    "v_cvt_pk_bf16_f32 v52, v28, v29\n" \
    "v_cvt_pk_bf16_f32 v53, v30, v31\n" \
    "v_cvt_pk_bf16_f32 v54, v32, v33\n" \
    "v_cvt_pk_bf16_f32 v55, v34, v35\n" \
    "v_cvt_pk_bf16_f32 v56, v36, v37\n" \
    "buffer_store_dwordx4 v[52:55], v135, s[40:43], s44 offen sc1\n" \
    "buffer_store_dword v56, v133, s[40:43], s44 offen sc1\n" \
    "s_waitcnt vmcnt(4)\n" \
    "s_mov_b64 exec, 1\n" \
    "buffer_store_dwordx2 %0, off, s[40:43], s46 sc1\n" \
    "s_mov_b64 exec, -1\n" \
    "s_cmp_eq_u32 s47, 0xf0000000\n" \
    "s_cbranch_scc1 T_IDLE%=\n" \
    "v_accvgpr_read_b32 v84, a244\n" \
    "v_xor_b32 v85, 32, v84\n" \
    "v_accvgpr_read_b32 v86, a245\n" \
    "v_xor_b32 v87, 32, v86\n" \
    "v_accvgpr_read_b32 v88, a246\n" \
    "v_xor_b32 v89, 32, v88\n" \
    "v_accvgpr_read_b32 v90, a247\n" \
    "v_xor_b32 v91, 32, v90\n" \
    "v_accvgpr_read_b32 v92, a248\n" \
    "v_xor_b32 v93, 32, v92\n" \
    "v_accvgpr_read_b32 v110, a249\n" \
    "v_xor_b32 v111, 32, v110\n" \
    "v_accvgpr_read_b32 v112, a252\n" \
    "v_xor_b32 v113, 32, v112\n" \
    "v_accvgpr_read_b32 v114, a253\n" \
    "v_xor_b32 v115, 32, v114\n" \
    "v_mov_b32 v106, v136\n" \
    "v_lshlrev_b32 v104, 1, v10\n" \
    "v_mov_b32 v105, v8\n" \
    "v_permlane16_swap_b32 v164, v166\n" \
    "v_permlane16_swap_b32 v165, v167\n" \
    "v_permlane16_swap_b32 v168, v170\n" \
    "v_permlane16_swap_b32 v169, v171\n" \
    "v_permlane16_swap_b32 v172, v174\n" \
    "v_permlane16_swap_b32 v173, v175\n" \
    "s_nop 0\n" \
    "v_lshlrev_b32 v8, 16, v164\n" \
    "v_and_b32 v9, 0xffff0000, v164\n" \
    "v_lshlrev_b32 v10, 16, v165\n" \
    "v_and_b32 v11, 0xffff0000, v165\n" \
    "v_lshlrev_b32 v12, 16, v166\n" \
    "v_and_b32 v13, 0xffff0000, v166\n" \
    "v_lshlrev_b32 v14, 16, v167\n" \
    "v_and_b32 v15, 0xffff0000, v167\n" \
    "v_lshlrev_b32 v16, 16, v168\n" \
    "v_and_b32 v17, 0xffff0000, v168\n" \
    "v_lshlrev_b32 v18, 16, v169\n" \
    "v_and_b32 v19, 0xffff0000, v169\n" \
    "v_lshlrev_b32 v20, 16, v170\n" \
    "v_and_b32 v21, 0xffff0000, v170\n" \
    "v_lshlrev_b32 v22, 16, v171\n" \
    "v_and_b32 v23, 0xffff0000, v171\n" \
    "v_lshlrev_b32 v24, 16, v172\n" \
    "v_and_b32 v25, 0xffff0000, v172\n" \
    "v_lshlrev_b32 v26, 16, v173\n" \
    "v_and_b32 v27, 0xffff0000, v173\n" \
    "v_lshlrev_b32 v28, 16, v174\n" \
    "v_and_b32 v29, 0xffff0000, v174\n" \
    "v_lshlrev_b32 v30, 16, v175\n" \
    "v_and_b32 v31, 0xffff0000, v175\n" \
    "ds_read_b64_tr_b16 a[0:1], v86 offset:0\n" \
    "ds_read_b64_tr_b16 a[2:3], v86 offset:1536\n" \
    "ds_read_b64_tr_b16 a[4:5], v87 offset:0\n" \
    "ds_read_b64_tr_b16 a[6:7], v87 offset:1536\n" \
    "ds_read_b64_tr_b16 a[8:9], v86 offset:64\n" \
    "ds_read_b64_tr_b16 a[10:11], v86 offset:1600\n" \
    "ds_read_b64_tr_b16 a[12:13], v87 offset:64\n" \
    "ds_read_b64_tr_b16 a[14:15], v87 offset:1600\n" \
    "ds_read_b64_tr_b16 a[16:17], v86 offset:128\n" \
    "ds_read_b64_tr_b16 a[18:19], v86 offset:1664\n" \
    "ds_read_b64_tr_b16 a[20:21], v87 offset:128\n" \
    "ds_read_b64_tr_b16 a[22:23], v87 offset:1664\n" \
    "ds_read_b64_tr_b16 a[24:25], v87 offset:6912\n" \
    "ds_read_b64_tr_b16 a[26:27], v87 offset:8448\n" \
    "ds_read_b64_tr_b16 a[28:29], v86 offset:6912\n" \
    "ds_read_b64_tr_b16 a[30:31], v86 offset:8448\n" \
    "ds_read_b64_tr_b16 a[32:33], v87 offset:6976\n" \
    "ds_read_b64_tr_b16 a[34:35], v87 offset:8512\n" \
    "ds_read_b64_tr_b16 a[36:37], v86 offset:6976\n" \
    "ds_read_b64_tr_b16 a[38:39], v86 offset:8512\n" \
    "ds_read_b64_tr_b16 a[40:41], v87 offset:7040\n" \
    "ds_read_b64_tr_b16 a[42:43], v87 offset:8576\n" \
    "ds_read_b64_tr_b16 a[44:45], v86 offset:7040\n" \
    "ds_read_b64_tr_b16 a[46:47], v86 offset:8576\n" \
    "ds_read_b64_tr_b16 a[48:49], v86 offset:13824\n" \
    "ds_read_b64_tr_b16 a[50:51], v86 offset:15360\n" \
    "ds_read_b64_tr_b16 a[52:53], v87 offset:13824\n" \
    "ds_read_b64_tr_b16 a[54:55], v87 offset:15360\n" \
    "ds_read_b64_tr_b16 a[56:57], v86 offset:13888\n" \
    "ds_read_b64_tr_b16 a[58:59], v86 offset:15424\n" \
    "ds_read_b64_tr_b16 a[60:61], v87 offset:13888\n" \
    "ds_read_b64_tr_b16 a[62:63], v87 offset:15424\n" \
    "ds_read_b64_tr_b16 a[64:65], v86 offset:13952\n" \
    "ds_read_b64_tr_b16 a[66:67], v86 offset:15488\n" \
    "ds_read_b64_tr_b16 a[68:69], v87 offset:13952\n" \
    "ds_read_b64_tr_b16 a[70:71], v87 offset:15488\n" \
    "ds_read_b64_tr_b16 a[72:73], v87 offset:20736\n" \
    "ds_read_b64_tr_b16 a[74:75], v87 offset:22272\n" \
    "ds_read_b64_tr_b16 a[76:77], v86 offset:20736\n" \
    "ds_read_b64_tr_b16 a[78:79], v86 offset:22272\n" \
    "ds_read_b64_tr_b16 a[80:81], v87 offset:20800\n" \
    "ds_read_b64_tr_b16 a[82:83], v87 offset:22336\n" \
    "ds_read_b64_tr_b16 a[84:85], v86 offset:20800\n" \
    "ds_read_b64_tr_b16 a[86:87], v86 offset:22336\n" \
    "ds_read_b64_tr_b16 a[88:89], v87 offset:20864\n" \
    "ds_read_b64_tr_b16 a[90:91], v87 offset:22400\n" \
    "ds_read_b64_tr_b16 a[92:93], v86 offset:20864\n" \
    "ds_read_b64_tr_b16 a[94:95], v86 offset:22400\n" \
    "ds_read_b64_tr_b16 a[96:97], v86 offset:27648\n" \
    "ds_read_b64_tr_b16 a[98:99], v86 offset:29184\n" \
    "ds_read_b64_tr_b16 a[100:101], v87 offset:27648\n" \
    "ds_read_b64_tr_b16 a[102:103], v87 offset:29184\n" \
    "ds_read_b64_tr_b16 a[104:105], v86 offset:27712\n" \
    "ds_read_b64_tr_b16 a[106:107], v86 offset:29248\n" \
    "ds_read_b64_tr_b16 a[108:109], v87 offset:27712\n" \
    "ds_read_b64_tr_b16 a[110:111], v87 offset:29248\n" \
    "ds_read_b64_tr_b16 a[112:113], v86 offset:27776\n" \
    "ds_read_b64_tr_b16 a[114:115], v86 offset:29312\n" \
    "ds_read_b64_tr_b16 a[116:117], v87 offset:27776\n" \
    "ds_read_b64_tr_b16 a[118:119], v87 offset:29312\n" \
    "ds_read_b64_tr_b16 a[120:121], v87 offset:34560\n" \
    "ds_read_b64_tr_b16 a[122:123], v87 offset:36096\n" \
    "ds_read_b64_tr_b16 a[124:125], v86 offset:34560\n" \
    "ds_read_b64_tr_b16 a[126:127], v86 offset:36096\n" \
    "ds_read_b64_tr_b16 a[128:129], v87 offset:34624\n" \
    "ds_read_b64_tr_b16 a[130:131], v87 offset:36160\n" \
    "ds_read_b64_tr_b16 a[132:133], v86 offset:34624\n" \
    "ds_read_b64_tr_b16 a[134:135], v86 offset:36160\n" \
    "ds_read_b64_tr_b16 a[136:137], v87 offset:34688\n" \
    "ds_read_b64_tr_b16 a[138:139], v87 offset:36224\n" \
    "ds_read_b64_tr_b16 a[140:141], v86 offset:34688\n" \
    "ds_read_b64_tr_b16 a[142:143], v86 offset:36224\n" \
    "ds_read_b64_tr_b16 a[144:145], v88 offset:4608\n" \
    "ds_read_b64_tr_b16 a[146:147], v88 offset:6144\n" \
    "ds_read_b64_tr_b16 a[148:149], v89 offset:4608\n" \
    "ds_read_b64_tr_b16 a[150:151], v89 offset:6144\n" \
    "ds_read_b64_tr_b16 a[152:153], v88 offset:4672\n" \
    "ds_read_b64_tr_b16 a[154:155], v88 offset:6208\n" \
    "ds_read_b64_tr_b16 a[156:157], v89 offset:4672\n" \
    "ds_read_b64_tr_b16 a[158:159], v89 offset:6208\n" \
    "ds_read_b64_tr_b16 a[160:161], v88 offset:4736\n" \
    "ds_read_b64_tr_b16 a[162:163], v88 offset:6272\n" \
    "ds_read_b64_tr_b16 a[164:165], v89 offset:4736\n" \
    "ds_read_b64_tr_b16 a[166:167], v89 offset:6272\n" \
    "ds_read_b64_tr_b16 a[168:169], v89 offset:11520\n" \
    "ds_read_b64_tr_b16 a[170:171], v89 offset:13056\n" \
    "ds_read_b64_tr_b16 a[172:173], v88 offset:11520\n" \
    "ds_read_b64_tr_b16 a[174:175], v88 offset:13056\n" \
    "ds_read_b64_tr_b16 a[176:177], v89 offset:11584\n" \
    "ds_read_b64_tr_b16 a[178:179], v89 offset:13120\n" \
    "ds_read_b64_tr_b16 a[180:181], v88 offset:11584\n" \
    "ds_read_b64_tr_b16 a[182:183], v88 offset:13120\n" \
    "ds_read_b64_tr_b16 a[184:185], v89 offset:11648\n" \
    "ds_read_b64_tr_b16 a[186:187], v89 offset:13184\n" \
    "ds_read_b64_tr_b16 a[188:189], v88 offset:11648\n" \
    "ds_read_b64_tr_b16 a[190:191], v88 offset:13184\n" \
    "ds_read_b64_tr_b16 a[192:193], v88 offset:18432\n" \
    "ds_read_b64_tr_b16 a[194:195], v88 offset:19968\n" \
    "ds_read_b64_tr_b16 a[196:197], v89 offset:18432\n" \
    "ds_read_b64_tr_b16 a[198:199], v89 offset:19968\n" \
    "ds_read_b64_tr_b16 a[200:201], v88 offset:18496\n" \
    "ds_read_b64_tr_b16 a[202:203], v88 offset:20032\n" \
    "ds_read_b64_tr_b16 a[204:205], v89 offset:18496\n" \
    "ds_read_b64_tr_b16 a[206:207], v89 offset:20032\n" \
    "ds_read_b64_tr_b16 a[208:209], v88 offset:18560\n" \
    "ds_read_b64_tr_b16 a[210:211], v88 offset:20096\n" \
    "ds_read_b64_tr_b16 a[212:213], v89 offset:18560\n" \
    "ds_read_b64_tr_b16 a[214:215], v89 offset:20096\n" \
    "ds_read_b64_tr_b16 a[216:217], v89 offset:25344\n" \
    "ds_read_b64_tr_b16 a[218:219], v89 offset:26880\n" \
    "ds_read_b64_tr_b16 a[220:221], v88 offset:25344\n" \
    "ds_read_b64_tr_b16 a[222:223], v88 offset:26880\n" \
    "ds_read_b64_tr_b16 a[224:225], v89 offset:25408\n" \
    "ds_read_b64_tr_b16 a[226:227], v89 offset:26944\n" \
    "ds_read_b64_tr_b16 a[228:229], v88 offset:25408\n" \
    "ds_read_b64_tr_b16 a[230:231], v88 offset:26944\n" \
    "ds_read_b64_tr_b16 a[232:233], v89 offset:25472\n" \
    "ds_read_b64_tr_b16 a[234:235], v89 offset:27008\n" \
    "ds_read_b64_tr_b16 a[236:237], v88 offset:25472\n" \
    "ds_read_b64_tr_b16 a[238:239], v88 offset:27008\n" \
    "s_waitcnt vmcnt(1)\n" \
    "s_mov_b64 exec, 1\n" \
    "buffer_store_dwordx2 %0, off, s[40:43], s45 sc1\n" \
    "s_mov_b64 exec, -1\n" \
    "ds_read_b64_tr_b16 a[240:241], v90 offset:0\n" \
    "ds_read_b64_tr_b16 a[242:243], v92 offset:0\n" \
    "ds_read_b64_tr_b16 a[244:245], v91 offset:0\n" \
    "ds_read_b64_tr_b16 a[246:247], v93 offset:0\n" \
    "ds_read_b64_tr_b16 a[248:249], v90 offset:64\n" \
    "ds_read_b64_tr_b16 a[250:251], v92 offset:64\n" \
    "ds_read_b64_tr_b16 a[252:253], v91 offset:64\n" \
    "ds_read_b64_tr_b16 a[254:255], v93 offset:64\n" \
    "ds_read_b64_tr_b16 v[120:121], v90 offset:128\n" \
    "ds_read_b64_tr_b16 v[122:123], v92 offset:128\n" \
    "ds_read_b64_tr_b16 v[124:125], v91 offset:128\n" \
    "ds_read_b64_tr_b16 v[126:127], v93 offset:128\n" \
    "ds_read_b64_tr_b16 v[128:129], v85 offset:2304\n" \
    "ds_read_b64_tr_b16 v[130:131], v85 offset:3840\n" \
    "ds_read_b64_tr_b16 v[132:133], v84 offset:2304\n" \
    "ds_read_b64_tr_b16 v[134:135], v84 offset:3840\n" \
    "ds_read_b64_tr_b16 v[136:137], v85 offset:2368\n" \
    "ds_read_b64_tr_b16 v[138:139], v85 offset:3904\n" \
    "ds_read_b64_tr_b16 v[140:141], v84 offset:2368\n" \
    "ds_read_b64_tr_b16 v[142:143], v84 offset:3904\n" \
    "ds_read_b64_tr_b16 v[144:145], v85 offset:2432\n" \
    "ds_read_b64_tr_b16 v[146:147], v85 offset:3968\n" \
    "ds_read_b64_tr_b16 v[148:149], v84 offset:2432\n" \
    "ds_read_b64_tr_b16 v[150:151], v84 offset:3968\n" \
    "ds_read_b64_tr_b16 v[152:153], v84 offset:9216\n" \
    "ds_read_b64_tr_b16 v[154:155], v84 offset:10752\n" \
    "ds_read_b64_tr_b16 v[156:157], v85 offset:9216\n" \
    "ds_read_b64_tr_b16 v[158:159], v85 offset:10752\n" \
    "ds_read_b64_tr_b16 v[160:161], v84 offset:9280\n" \
    "ds_read_b64_tr_b16 v[162:163], v84 offset:10816\n" \
    "ds_read_b64_tr_b16 v[164:165], v85 offset:9280\n" \
    "ds_read_b64_tr_b16 v[166:167], v85 offset:10816\n" \
    "ds_read_b64_tr_b16 v[168:169], v84 offset:9344\n" \
    "ds_read_b64_tr_b16 v[170:171], v84 offset:10880\n" \
    "ds_read_b64_tr_b16 v[172:173], v85 offset:9344\n" \
    "ds_read_b64_tr_b16 v[174:175], v85 offset:10880\n" \
    "ds_read_b64_tr_b16 v[176:177], v85 offset:16128\n" \
    "ds_read_b64_tr_b16 v[178:179], v85 offset:17664\n" \
    "ds_read_b64_tr_b16 v[180:181], v84 offset:16128\n" \
    "ds_read_b64_tr_b16 v[182:183], v84 offset:17664\n" \
    "ds_read_b64_tr_b16 v[184:185], v85 offset:16192\n" \
    "ds_read_b64_tr_b16 v[186:187], v85 offset:17728\n" \
    "ds_read_b64_tr_b16 v[188:189], v84 offset:16192\n" \
    "ds_read_b64_tr_b16 v[190:191], v84 offset:17728\n" \
    "ds_read_b64_tr_b16 v[192:193], v85 offset:16256\n" \
    "ds_read_b64_tr_b16 v[194:195], v85 offset:17792\n" \
    "ds_read_b64_tr_b16 v[196:197], v84 offset:16256\n" \
    "ds_read_b64_tr_b16 v[198:199], v84 offset:17792\n" \
    "ds_read_b64_tr_b16 v[200:201], v84 offset:23040\n" \
    "ds_read_b64_tr_b16 v[202:203], v84 offset:24576\n" \
    "ds_read_b64_tr_b16 v[204:205], v85 offset:23040\n" \
    "ds_read_b64_tr_b16 v[206:207], v85 offset:24576\n" \
    "ds_read_b64_tr_b16 v[208:209], v84 offset:23104\n" \
    "ds_read_b64_tr_b16 v[210:211], v84 offset:24640\n" \
    "ds_read_b64_tr_b16 v[212:213], v85 offset:23104\n" \
    "ds_read_b64_tr_b16 v[214:215], v85 offset:24640\n" \
    "ds_read_b64_tr_b16 v[216:217], v84 offset:23168\n" \
    "ds_read_b64_tr_b16 v[218:219], v84 offset:24704\n" \
    "ds_read_b64_tr_b16 v[220:221], v85 offset:23168\n" \
    "ds_read_b64_tr_b16 v[222:223], v85 offset:24704\n" \
    "ds_read_b64_tr_b16 v[224:225], v85 offset:29952\n" \
    "ds_read_b64_tr_b16 v[226:227], v85 offset:31488\n" \
    "ds_read_b64_tr_b16 v[228:229], v84 offset:29952\n" \
    "ds_read_b64_tr_b16 v[230:231], v84 offset:31488\n" \
    "ds_read_b64_tr_b16 v[232:233], v85 offset:30016\n" \
    "ds_read_b64_tr_b16 v[234:235], v85 offset:31552\n" \
    "ds_read_b64_tr_b16 v[236:237], v84 offset:30016\n" \
    "ds_read_b64_tr_b16 v[238:239], v84 offset:31552\n" \
    "ds_read_b64_tr_b16 v[240:241], v85 offset:30080\n" \
    "ds_read_b64_tr_b16 v[242:243], v85 offset:31616\n" \
    "ds_read_b64_tr_b16 v[244:245], v84 offset:30080\n" \
    "ds_read_b64_tr_b16 v[246:247], v84 offset:31616\n" \
    "ds_read_b64_tr_b16 v[248:249], v110 offset:0\n" \
    "ds_read_b64_tr_b16 v[250:251], v111 offset:6912\n" \
    "ds_read_b64_tr_b16 v[252:253], v111 offset:0\n" \
    "ds_read_b64_tr_b16 v[254:255], v110 offset:6912\n" \
    "s_mov_b32 s56, s53\n" \
    "s_mov_b32 s57, 0\n" \
    "s_cmp_lg_u32 %6, 0\n" \
    "s_cbranch_scc1 TP_W0%=\n" \
    "buffer_load_dwordx2 v[96:97], v104, s[40:43], s52 offen sc1\n" \
    "s_waitcnt vmcnt(0)\n" \
    "v_cmp_ne_u64 vcc, v[96:97], %0\n" \
    "s_and_b64 vcc, vcc, s[56:57]\n" \
    "s_cbranch_scc1 TP_SPIN%=\n" \
    "TP_OK%=:\n" \
    "TP_W0%=:\n" \
    "s_barrier\n" \
    "buffer_load_dwordx4 v[32:35], v105, s[40:43], s48 offen offset:0 sc1\n" \
    "buffer_load_dwordx4 v[36:39], v105, s[40:43], s48 offen offset:1024 sc1\n" \
    "buffer_load_dwordx4 v[40:43], v105, s[40:43], s48 offen offset:2048 sc1\n" \
    "s_add_u32 s48, s48, 2359296\n" \
    "buffer_load_dwordx4 v[44:47], v105, s[40:43], s48 offen offset:0 sc1\n" \
    "buffer_load_dwordx4 v[48:51], v105, s[40:43], s48 offen offset:1024 sc1\n" \
    "buffer_load_dwordx4 v[52:55], v105, s[40:43], s48 offen offset:2048 sc1\n" \
    "s_add_u32 s48, s48, 2359296\n" \
    "buffer_load_dwordx4 v[56:59], v105, s[40:43], s48 offen offset:0 sc1\n" \
    "buffer_load_dwordx4 v[60:63], v105, s[40:43], s48 offen offset:1024 sc1\n" \
    "buffer_load_dwordx4 v[64:67], v105, s[40:43], s48 offen offset:2048 sc1\n" \
    "s_add_u32 s48, s48, 2359296\n" \
    "buffer_load_dwordx4 v[68:71], v105, s[40:43], s48 offen offset:0 sc1\n" \
    "buffer_load_dwordx4 v[72:75], v105, s[40:43], s48 offen offset:1024 sc1\n" \
    "buffer_load_dwordx4 v[76:79], v105, s[40:43], s48 offen offset:2048 sc1\n" \
    "s_add_u32 s48, s48, 2359296\n" \
    "buffer_load_dwordx4 v[80:83], v105, s[40:43], s48 offen offset:0 sc1\n" \
    "buffer_load_dwordx4 v[84:87], v105, s[40:43], s48 offen offset:1024 sc1\n" \
    "buffer_load_dwordx4 v[88:91], v105, s[40:43], s48 offen offset:2048 sc1\n" \
    "s_add_u32 s48, s48, 2359296\n" \
    "s_waitcnt vmcnt(12)\n" \
    "v_lshlrev_b32 v107, 16, v32\n" \
    "v_and_b32 v32, 0xffff0000, v32\n" \
    "v_add_f32 v8, v8, v107\n" \
    "v_add_f32 v9, v9, v32\n" \
    "v_lshlrev_b32 v107, 16, v33\n" \
    "v_and_b32 v33, 0xffff0000, v33\n" \
    "v_add_f32 v10, v10, v107\n" \
    "v_add_f32 v11, v11, v33\n" \
    "v_lshlrev_b32 v107, 16, v34\n" \
    "v_and_b32 v34, 0xffff0000, v34\n" \
    "v_add_f32 v12, v12, v107\n" \
    "v_add_f32 v13, v13, v34\n" \
    "v_lshlrev_b32 v107, 16, v35\n" \
    "v_and_b32 v35, 0xffff0000, v35\n" \
    "v_add_f32 v14, v14, v107\n" \
    "v_add_f32 v15, v15, v35\n" \
    "v_lshlrev_b32 v107, 16, v36\n" \
    "v_and_b32 v36, 0xffff0000, v36\n" \
    "v_add_f32 v16, v16, v107\n" \
    "v_add_f32 v17, v17, v36\n" \
    "v_lshlrev_b32 v107, 16, v37\n" \
    "v_and_b32 v37, 0xffff0000, v37\n" \
    "v_add_f32 v18, v18, v107\n" \
    "v_add_f32 v19, v19, v37\n" \
    "v_lshlrev_b32 v107, 16, v38\n" \
    "v_and_b32 v38, 0xffff0000, v38\n" \
    "v_add_f32 v20, v20, v107\n" \
    "v_add_f32 v21, v21, v38\n" \
    "v_lshlrev_b32 v107, 16, v39\n" \
    "v_and_b32 v39, 0xffff0000, v39\n" \
    "v_add_f32 v22, v22, v107\n" \
    "v_add_f32 v23, v23, v39\n" \
    "v_lshlrev_b32 v107, 16, v40\n" \
    "v_and_b32 v40, 0xffff0000, v40\n" \
    "v_add_f32 v24, v24, v107\n" \
    "v_add_f32 v25, v25, v40\n" \
    "v_lshlrev_b32 v107, 16, v41\n" \
    "v_and_b32 v41, 0xffff0000, v41\n" \
    "v_add_f32 v26, v26, v107\n" \
    "v_add_f32 v27, v27, v41\n" \
    "v_lshlrev_b32 v107, 16, v42\n" \
    "v_and_b32 v42, 0xffff0000, v42\n" \
    "v_add_f32 v28, v28, v107\n" \
    "v_add_f32 v29, v29, v42\n" \
    "v_lshlrev_b32 v107, 16, v43\n" \
    "v_and_b32 v43, 0xffff0000, v43\n" \
    "v_add_f32 v30, v30, v107\n" \
    "v_add_f32 v31, v31, v43\n" \
    "s_waitcnt vmcnt(9)\n" \
    "v_lshlrev_b32 v107, 16, v44\n" \
    "v_and_b32 v44, 0xffff0000, v44\n" \
    "v_add_f32 v8, v8, v107\n" \
    "v_add_f32 v9, v9, v44\n" \
    "v_lshlrev_b32 v107, 16, v45\n" \
    "v_and_b32 v45, 0xffff0000, v45\n" \
    "v_add_f32 v10, v10, v107\n" \
    "v_add_f32 v11, v11, v45\n" \
    "v_lshlrev_b32 v107, 16, v46\n" \
    "v_and_b32 v46, 0xffff0000, v46\n" \
    "v_add_f32 v12, v12, v107\n" \
    "v_add_f32 v13, v13, v46\n" \
    "v_lshlrev_b32 v107, 16, v47\n" \
    "v_and_b32 v47, 0xffff0000, v47\n" \
    "v_add_f32 v14, v14, v107\n" \
    "v_add_f32 v15, v15, v47\n" \
    "v_lshlrev_b32 v107, 16, v48\n" \
    "v_and_b32 v48, 0xffff0000, v48\n" \
    "v_add_f32 v16, v16, v107\n" \
    "v_add_f32 v17, v17, v48\n" \
    "v_lshlrev_b32 v107, 16, v49\n" \
    "v_and_b32 v49, 0xffff0000, v49\n" \
    "v_add_f32 v18, v18, v107\n" \
    "v_add_f32 v19, v19, v49\n" \
    "v_lshlrev_b32 v107, 16, v50\n" \
    "v_and_b32 v50, 0xffff0000, v50\n" \
    "v_add_f32 v20, v20, v107\n" \
    "v_add_f32 v21, v21, v50\n" \
    "v_lshlrev_b32 v107, 16, v51\n" \
    "v_and_b32 v51, 0xffff0000, v51\n" \
    "v_add_f32 v22, v22, v107\n" \
    "v_add_f32 v23, v23, v51\n" \
    "v_lshlrev_b32 v107, 16, v52\n" \
    "v_and_b32 v52, 0xffff0000, v52\n" \
    "v_add_f32 v24, v24, v107\n" \
    "v_add_f32 v25, v25, v52\n" \
    "v_lshlrev_b32 v107, 16, v53\n" \
    "v_and_b32 v53, 0xffff0000, v53\n" \
    "v_add_f32 v26, v26, v107\n" \
    "v_add_f32 v27, v27, v53\n" \
    "v_lshlrev_b32 v107, 16, v54\n" \
    "v_and_b32 v54, 0xffff0000, v54\n" \
    "v_add_f32 v28, v28, v107\n" \
    "v_add_f32 v29, v29, v54\n" \
    "v_lshlrev_b32 v107, 16, v55\n" \
    "v_and_b32 v55, 0xffff0000, v55\n" \
    "v_add_f32 v30, v30, v107\n" \
    "v_add_f32 v31, v31, v55\n" \
    "s_waitcnt vmcnt(6)\n" \
    "v_lshlrev_b32 v107, 16, v56\n" \
    "v_and_b32 v56, 0xffff0000, v56\n" \
    "v_add_f32 v8, v8, v107\n" \
    "v_add_f32 v9, v9, v56\n" \
    "v_lshlrev_b32 v107, 16, v57\n" \
    "v_and_b32 v57, 0xffff0000, v57\n" \
    "v_add_f32 v10, v10, v107\n" \
    "v_add_f32 v11, v11, v57\n" \
    "v_lshlrev_b32 v107, 16, v58\n" \
    "v_and_b32 v58, 0xffff0000, v58\n" \
    "v_add_f32 v12, v12, v107\n" \
    "v_add_f32 v13, v13, v58\n" \
    "v_lshlrev_b32 v107, 16, v59\n" \
    "v_and_b32 v59, 0xffff0000, v59\n" \
    "v_add_f32 v14, v14, v107\n" \
    "v_add_f32 v15, v15, v59\n" \
    "v_lshlrev_b32 v107, 16, v60\n" \
    "v_and_b32 v60, 0xffff0000, v60\n" \
    "v_add_f32 v16, v16, v107\n" \
    "v_add_f32 v17, v17, v60\n" \
    "v_lshlrev_b32 v107, 16, v61\n" \
    "v_and_b32 v61, 0xffff0000, v61\n" \
    "v_add_f32 v18, v18, v107\n" \
    "v_add_f32 v19, v19, v61\n" \
    "v_lshlrev_b32 v107, 16, v62\n" \
    "v_and_b32 v62, 0xffff0000, v62\n" \
    "v_add_f32 v20, v20, v107\n" \
    "v_add_f32 v21, v21, v62\n" \
    "v_lshlrev_b32 v107, 16, v63\n" \
    "v_and_b32 v63, 0xffff0000, v63\n" \
    "v_add_f32 v22, v22, v107\n" \
    "v_add_f32 v23, v23, v63\n" \
    "v_lshlrev_b32 v107, 16, v64\n" \
    "v_and_b32 v64, 0xffff0000, v64\n" \
    "v_add_f32 v24, v24, v107\n" \
    "v_add_f32 v25, v25, v64\n" \
    "v_lshlrev_b32 v107, 16, v65\n" \
    "v_and_b32 v65, 0xffff0000, v65\n" \
    "v_add_f32 v26, v26, v107\n" \
    "v_add_f32 v27, v27, v65\n" \
    "v_lshlrev_b32 v107, 16, v66\n" \
    "v_and_b32 v66, 0xffff0000, v66\n" \
    "v_add_f32 v28, v28, v107\n" \
    "v_add_f32 v29, v29, v66\n" \
    "v_lshlrev_b32 v107, 16, v67\n" \
    "v_and_b32 v67, 0xffff0000, v67\n" \
    "v_add_f32 v30, v30, v107\n" \
    "v_add_f32 v31, v31, v67\n" \
    "s_waitcnt vmcnt(3)\n" \
    "v_lshlrev_b32 v107, 16, v68\n" \
    "v_and_b32 v68, 0xffff0000, v68\n" \
    "v_add_f32 v8, v8, v107\n" \
    "v_add_f32 v9, v9, v68\n" \
    "v_lshlrev_b32 v107, 16, v69\n" \
    "v_and_b32 v69, 0xffff0000, v69\n" \
    "v_add_f32 v10, v10, v107\n" \
    "v_add_f32 v11, v11, v69\n" \
    "v_lshlrev_b32 v107, 16, v70\n" \
    "v_and_b32 v70, 0xffff0000, v70\n" \
    "v_add_f32 v12, v12, v107\n" \
    "v_add_f32 v13, v13, v70\n" \
    "v_lshlrev_b32 v107, 16, v71\n" \
    "v_and_b32 v71, 0xffff0000, v71\n" \
    "v_add_f32 v14, v14, v107\n" \
    "v_add_f32 v15, v15, v71\n" \
    "v_lshlrev_b32 v107, 16, v72\n" \
    "v_and_b32 v72, 0xffff0000, v72\n" \
    "v_add_f32 v16, v16, v107\n" \
    "v_add_f32 v17, v17, v72\n" \
    "v_lshlrev_b32 v107, 16, v73\n" \
    "v_and_b32 v73, 0xffff0000, v73\n" \
    "v_add_f32 v18, v18, v107\n" \
    "v_add_f32 v19, v19, v73\n" \
    "v_lshlrev_b32 v107, 16, v74\n" \
    "v_and_b32 v74, 0xffff0000, v74\n" \
    "v_add_f32 v20, v20, v107\n" \
    "v_add_f32 v21, v21, v74\n" \
    "v_lshlrev_b32 v107, 16, v75\n" \
    "v_and_b32 v75, 0xffff0000, v75\n" \
    "v_add_f32 v22, v22, v107\n" \
    "v_add_f32 v23, v23, v75\n" \
    "v_lshlrev_b32 v107, 16, v76\n" \
    "v_and_b32 v76, 0xffff0000, v76\n" \
    "v_add_f32 v24, v24, v107\n" \
    "v_add_f32 v25, v25, v76\n" \
    "v_lshlrev_b32 v107, 16, v77\n" \
    "v_and_b32 v77, 0xffff0000, v77\n" \
    "v_add_f32 v26, v26, v107\n" \
    "v_add_f32 v27, v27, v77\n" \
    "v_lshlrev_b32 v107, 16, v78\n" \
    "v_and_b32 v78, 0xffff0000, v78\n" \
    "v_add_f32 v28, v28, v107\n" \
    "v_add_f32 v29, v29, v78\n" \
    "v_lshlrev_b32 v107, 16, v79\n" \
    "v_and_b32 v79, 0xffff0000, v79\n" \
    "v_add_f32 v30, v30, v107\n" \
    "v_add_f32 v31, v31, v79\n" \
    "s_waitcnt vmcnt(0)\n" \
    "v_lshlrev_b32 v107, 16, v80\n" \
    "v_and_b32 v80, 0xffff0000, v80\n" \
    "v_add_f32 v8, v8, v107\n" \
    "v_add_f32 v9, v9, v80\n" \
    "v_lshlrev_b32 v107, 16, v81\n" \
    "v_and_b32 v81, 0xffff0000, v81\n" \
    "v_add_f32 v10, v10, v107\n" \
    "v_add_f32 v11, v11, v81\n" \
    "v_lshlrev_b32 v107, 16, v82\n" \
    "v_and_b32 v82, 0xffff0000, v82\n" \
    "v_add_f32 v12, v12, v107\n" \
    "v_add_f32 v13, v13, v82\n" \
    "v_lshlrev_b32 v107, 16, v83\n" \
    "v_and_b32 v83, 0xffff0000, v83\n" \
    "v_add_f32 v14, v14, v107\n" \
    "v_add_f32 v15, v15, v83\n" \
    "v_lshlrev_b32 v107, 16, v84\n" \
    "v_and_b32 v84, 0xffff0000, v84\n" \
    "v_add_f32 v16, v16, v107\n" \
    "v_add_f32 v17, v17, v84\n" \
    "v_lshlrev_b32 v107, 16, v85\n" \
    "v_and_b32 v85, 0xffff0000, v85\n" \
    "v_add_f32 v18, v18, v107\n" \
    "v_add_f32 v19, v19, v85\n" \
    "v_lshlrev_b32 v107, 16, v86\n" \
    "v_and_b32 v86, 0xffff0000, v86\n" \
    "v_add_f32 v20, v20, v107\n" \
    "v_add_f32 v21, v21, v86\n" \
    "v_lshlrev_b32 v107, 16, v87\n" \
    "v_and_b32 v87, 0xffff0000, v87\n" \
    "v_add_f32 v22, v22, v107\n" \
    "v_add_f32 v23, v23, v87\n" \
    "v_lshlrev_b32 v107, 16, v88\n" \
    "v_and_b32 v88, 0xffff0000, v88\n" \
    "v_add_f32 v24, v24, v107\n" \
    "v_add_f32 v25, v25, v88\n" \
    "v_lshlrev_b32 v107, 16, v89\n" \
    "v_and_b32 v89, 0xffff0000, v89\n" \
    "v_add_f32 v26, v26, v107\n" \
    "v_add_f32 v27, v27, v89\n" \
    "v_lshlrev_b32 v107, 16, v90\n" \
    "v_and_b32 v90, 0xffff0000, v90\n" \
    "v_add_f32 v28, v28, v107\n" \
    "v_add_f32 v29, v29, v90\n" \
    "v_lshlrev_b32 v107, 16, v91\n" \
    "v_and_b32 v91, 0xffff0000, v91\n" \
    "v_add_f32 v30, v30, v107\n" \
    "v_add_f32 v31, v31, v91\n" \
    "s_cmp_lg_u32 %6, 0\n" \
    "s_cbranch_scc1 TO_W0%=\n" \
    "buffer_load_dwordx2 v[96:97], v104, s[40:43], s54 offen sc1\n" \
    "s_waitcnt vmcnt(0)\n" \
    "v_cmp_ne_u64 vcc, v[96:97], %0\n" \
    "s_and_b64 vcc, vcc, exec\n" \
    "s_cbranch_scc1 TO_SPIN%=\n" \
    "TO_OK%=:\n" \
    "TO_W0%=:\n" \
    "s_barrier\n" \
    "buffer_load_dwordx4 v[32:35], v105, s[40:43], s49 offen offset:0 sc1\n" \
    "buffer_load_dwordx4 v[36:39], v105, s[40:43], s49 offen offset:1024 sc1\n" \
    "buffer_load_dwordx4 v[40:43], v105, s[40:43], s49 offen offset:2048 sc1\n" \
    "buffer_load_dwordx4 v[44:47], v105, s[40:43], s49 offen offset:3072 sc1\n" \
    "s_add_u32 s49, s49, 0x1000\n" \
    "buffer_load_dwordx4 v[48:51], v105, s[40:43], s49 offen offset:0 sc1\n" \
    "buffer_load_dwordx4 v[52:55], v105, s[40:43], s49 offen offset:1024 sc1\n" \
    "buffer_load_dwordx4 v[56:59], v105, s[40:43], s49 offen offset:2048 sc1\n" \
    "buffer_load_dwordx4 v[60:63], v105, s[40:43], s49 offen offset:3072 sc1\n" \
    "s_add_u32 s49, s49, 0x1000\n" \
    "buffer_load_dwordx4 v[64:67], v105, s[40:43], s49 offen offset:0 sc1\n" \
    "buffer_load_dwordx4 v[68:71], v105, s[40:43], s49 offen offset:1024 sc1\n" \
    "buffer_load_dwordx4 v[72:75], v105, s[40:43], s49 offen offset:2048 sc1\n" \
    "buffer_load_dwordx4 v[76:79], v105, s[40:43], s49 offen offset:3072 sc1\n" \
    "s_add_u32 s49, s49, 0x1000\n" \
    "buffer_load_dwordx4 v[80:83], v105, s[40:43], s49 offen offset:0 sc1\n" \
    "buffer_load_dwordx4 v[84:87], v105, s[40:43], s49 offen offset:1024 sc1\n" \
    "buffer_load_dwordx4 v[88:91], v105, s[40:43], s49 offen offset:2048 sc1\n" \
    "buffer_load_dwordx4 v[92:95], v105, s[40:43], s49 offen offset:3072 sc1\n" \
    "s_add_u32 s49, s49, 0x1000\n" \
    "buffer_load_dwordx4 v[96:99], v105, s[40:43], s49 offen offset:0 sc1\n" \
    "buffer_load_dwordx4 v[100:103], v105, s[40:43], s49 offen offset:1024 sc1\n" \
    "s_waitcnt vmcnt(17) lgkmcnt(15)\n" \
    "v_mfma_f32_16x16x32_bf16 v[8:11], a[0:3], v[32:35], v[8:11]\n" \
    "v_mfma_f32_16x16x32_bf16 v[12:15], a[4:7], v[32:35], v[12:15]\n" \
    "v_mfma_f32_16x16x32_bf16 v[16:19], a[8:11], v[32:35], v[16:19]\n" \
    "v_mfma_f32_16x16x32_bf16 v[20:23], a[12:15], v[32:35], v[20:23]\n" \
    "v_mfma_f32_16x16x32_bf16 v[24:27], a[16:19], v[32:35], v[24:27]\n" \
    "v_mfma_f32_16x16x32_bf16 v[28:31], a[20:23], v[32:35], v[28:31]\n" \
    "s_waitcnt vmcnt(16)\n" \
    "v_mfma_f32_16x16x32_bf16 v[8:11], a[24:27], v[36:39], v[8:11]\n" \
    "v_mfma_f32_16x16x32_bf16 v[12:15], a[28:31], v[36:39], v[12:15]\n" \
    "v_mfma_f32_16x16x32_bf16 v[16:19], a[32:35], v[36:39], v[16:19]\n" \
    "v_mfma_f32_16x16x32_bf16 v[20:23], a[36:39], v[36:39], v[20:23]\n" \
    "v_mfma_f32_16x16x32_bf16 v[24:27], a[40:43], v[36:39], v[24:27]\n" \
    "v_mfma_f32_16x16x32_bf16 v[28:31], a[44:47], v[36:39], v[28:31]\n" \
    "s_waitcnt vmcnt(15)\n" \
    "v_mfma_f32_16x16x32_bf16 v[8:11], a[48:51], v[40:43], v[8:11]\n" \
    "v_mfma_f32_16x16x32_bf16 v[12:15], a[52:55], v[40:43], v[12:15]\n" \
    "ds_read_b64_tr_b16 a[0:1], v110 offset:64\n" \
    "v_mfma_f32_16x16x32_bf16 v[16:19], a[56:59], v[40:43], v[16:19]\n" \
    "v_mfma_f32_16x16x32_bf16 v[20:23], a[60:63], v[40:43], v[20:23]\n" \
    "ds_read_b64_tr_b16 a[2:3], v111 offset:6976\n" \
    "v_mfma_f32_16x16x32_bf16 v[24:27], a[64:67], v[40:43], v[24:27]\n" \
    "v_mfma_f32_16x16x32_bf16 v[28:31], a[68:71], v[40:43], v[28:31]\n" \
    "ds_read_b64_tr_b16 a[4:5], v111 offset:64\n" \
    "s_waitcnt vmcnt(14)\n" \
    "v_mfma_f32_16x16x32_bf16 v[8:11], a[72:75], v[44:47], v[8:11]\n" \
    "v_mfma_f32_16x16x32_bf16 v[12:15], a[76:79], v[44:47], v[12:15]\n" \
    "ds_read_b64_tr_b16 a[6:7], v110 offset:6976\n" \
    "v_mfma_f32_16x16x32_bf16 v[16:19], a[80:83], v[44:47], v[16:19]\n" \
    "v_mfma_f32_16x16x32_bf16 v[20:23], a[84:87], v[44:47], v[20:23]\n" \
    "ds_read_b64_tr_b16 a[8:9], v110 offset:128\n" \
    "v_mfma_f32_16x16x32_bf16 v[24:27], a[88:91], v[44:47], v[24:27]\n" \
    "v_mfma_f32_16x16x32_bf16 v[28:31], a[92:95], v[44:47], v[28:31]\n" \
    "ds_read_b64_tr_b16 a[10:11], v111 offset:7040\n" \
    "s_waitcnt vmcnt(13)\n" \
    "v_mfma_f32_16x16x32_bf16 v[8:11], a[96:99], v[48:51], v[8:11]\n" \
    "v_mfma_f32_16x16x32_bf16 v[12:15], a[100:103], v[48:51], v[12:15]\n" \
    "ds_read_b64_tr_b16 a[12:13], v111 offset:128\n" \
    "v_mfma_f32_16x16x32_bf16 v[16:19], a[104:107], v[48:51], v[16:19]\n" \
    "v_mfma_f32_16x16x32_bf16 v[20:23], a[108:111], v[48:51], v[20:23]\n" \
    "ds_read_b64_tr_b16 a[14:15], v110 offset:7040\n" \
    "v_mfma_f32_16x16x32_bf16 v[24:27], a[112:115], v[48:51], v[24:27]\n" \
    "v_mfma_f32_16x16x32_bf16 v[28:31], a[116:119], v[48:51], v[28:31]\n" \
    "ds_read_b64_tr_b16 a[16:17], v112 offset:0\n" \
    "s_waitcnt vmcnt(12)\n" \
    "v_mfma_f32_16x16x32_bf16 v[8:11], a[120:123], v[52:55], v[8:11]\n" \
    "v_mfma_f32_16x16x32_bf16 v[12:15], a[124:127], v[52:55], v[12:15]\n" \
    "ds_read_b64_tr_b16 a[18:19], v114 offset:0\n" \
    "v_mfma_f32_16x16x32_bf16 v[16:19], a[128:131], v[52:55], v[16:19]\n" \
    "v_mfma_f32_16x16x32_bf16 v[20:23], a[132:135], v[52:55], v[20:23]\n" \
    "ds_read_b64_tr_b16 a[20:21], v113 offset:0\n" \
    "v_mfma_f32_16x16x32_bf16 v[24:27], a[136:139], v[52:55], v[24:27]\n" \
    "v_mfma_f32_16x16x32_bf16 v[28:31], a[140:143], v[52:55], v[28:31]\n" \
    "ds_read_b64_tr_b16 a[22:23], v115 offset:0\n" \
    "s_waitcnt vmcnt(11)\n" \
    "v_mfma_f32_16x16x32_bf16 v[8:11], a[144:147], v[56:59], v[8:11]\n" \
    "v_mfma_f32_16x16x32_bf16 v[12:15], a[148:151], v[56:59], v[12:15]\n" \
    "ds_read_b64_tr_b16 a[24:25], v112 offset:64\n" \
    "v_mfma_f32_16x16x32_bf16 v[16:19], a[152:155], v[56:59], v[16:19]\n" \
    "v_mfma_f32_16x16x32_bf16 v[20:23], a[156:159], v[56:59], v[20:23]\n" \
    "ds_read_b64_tr_b16 a[26:27], v114 offset:64\n" \
    "v_mfma_f32_16x16x32_bf16 v[24:27], a[160:163], v[56:59], v[24:27]\n" \
    "v_mfma_f32_16x16x32_bf16 v[28:31], a[164:167], v[56:59], v[28:31]\n" \
    "ds_read_b64_tr_b16 a[28:29], v113 offset:64\n" \
    "s_waitcnt vmcnt(10)\n" \
    "v_mfma_f32_16x16x32_bf16 v[8:11], a[168:171], v[60:63], v[8:11]\n" \
    "v_mfma_f32_16x16x32_bf16 v[12:15], a[172:175], v[60:63], v[12:15]\n" \
    "ds_read_b64_tr_b16 a[30:31], v115 offset:64\n" \
    "v_mfma_f32_16x16x32_bf16 v[16:19], a[176:179], v[60:63], v[16:19]\n" \
    "v_mfma_f32_16x16x32_bf16 v[20:23], a[180:183], v[60:63], v[20:23]\n" \
    "ds_read_b64_tr_b16 a[32:33], v112 offset:128\n" \
    "v_mfma_f32_16x16x32_bf16 v[24:27], a[184:187], v[60:63], v[24:27]\n" \
    "v_mfma_f32_16x16x32_bf16 v[28:31], a[188:191], v[60:63], v[28:31]\n" \
    "ds_read_b64_tr_b16 a[34:35], v114 offset:128\n" \
    "s_waitcnt vmcnt(9)\n" \
    "v_mfma_f32_16x16x32_bf16 v[8:11], a[192:195], v[64:67], v[8:11]\n" \
    "v_mfma_f32_16x16x32_bf16 v[12:15], a[196:199], v[64:67], v[12:15]\n" \
    "ds_read_b64_tr_b16 a[36:37], v113 offset:128\n" \
    "v_mfma_f32_16x16x32_bf16 v[16:19], a[200:203], v[64:67], v[16:19]\n" \
    "v_mfma_f32_16x16x32_bf16 v[20:23], a[204:207], v[64:67], v[20:23]\n" \
    "ds_read_b64_tr_b16 a[38:39], v115 offset:128\n" \
    "v_mfma_f32_16x16x32_bf16 v[24:27], a[208:211], v[64:67], v[24:27]\n" \
    "v_mfma_f32_16x16x32_bf16 v[28:31], a[212:215], v[64:67], v[28:31]\n" \
    "s_waitcnt vmcnt(8)\n" \
    "v_mfma_f32_16x16x32_bf16 v[8:11], a[216:219], v[68:71], v[8:11]\n" \
    "v_mfma_f32_16x16x32_bf16 v[12:15], a[220:223], v[68:71], v[12:15]\n" \
    "v_mfma_f32_16x16x32_bf16 v[16:19], a[224:227], v[68:71], v[16:19]\n" \
    "v_mfma_f32_16x16x32_bf16 v[20:23], a[228:231], v[68:71], v[20:23]\n" \
    "v_mfma_f32_16x16x32_bf16 v[24:27], a[232:235], v[68:71], v[24:27]\n" \
    "v_mfma_f32_16x16x32_bf16 v[28:31], a[236:239], v[68:71], v[28:31]\n" \
    "s_waitcnt vmcnt(7)\n" \
    "v_mfma_f32_16x16x32_bf16 v[8:11], a[240:243], v[72:75], v[8:11]\n" \
    "v_mfma_f32_16x16x32_bf16 v[12:15], a[244:247], v[72:75], v[12:15]\n" \
    "v_mfma_f32_16x16x32_bf16 v[16:19], a[248:251], v[72:75], v[16:19]\n" \
    "v_mfma_f32_16x16x32_bf16 v[20:23], a[252:255], v[72:75], v[20:23]\n" \
    "v_mfma_f32_16x16x32_bf16 v[24:27], v[120:123], v[72:75], v[24:27]\n" \
    "v_mfma_f32_16x16x32_bf16 v[28:31], v[124:127], v[72:75], v[28:31]\n" \
    "s_waitcnt vmcnt(6)\n" \
    "v_mfma_f32_16x16x32_bf16 v[8:11], v[128:131], v[76:79], v[8:11]\n" \
    "v_mfma_f32_16x16x32_bf16 v[12:15], v[132:135], v[76:79], v[12:15]\n" \
    "v_mfma_f32_16x16x32_bf16 v[16:19], v[136:139], v[76:79], v[16:19]\n" \
    "v_mfma_f32_16x16x32_bf16 v[20:23], v[140:143], v[76:79], v[20:23]\n" \
    "v_mfma_f32_16x16x32_bf16 v[24:27], v[144:147], v[76:79], v[24:27]\n" \
    "v_mfma_f32_16x16x32_bf16 v[28:31], v[148:151], v[76:79], v[28:31]\n" \
    "s_waitcnt vmcnt(5)\n" \
    "v_mfma_f32_16x16x32_bf16 v[8:11], v[152:155], v[80:83], v[8:11]\n" \
    "v_mfma_f32_16x16x32_bf16 v[12:15], v[156:159], v[80:83], v[12:15]\n" \
    "v_mfma_f32_16x16x32_bf16 v[16:19], v[160:163], v[80:83], v[16:19]\n" \
    "v_mfma_f32_16x16x32_bf16 v[20:23], v[164:167], v[80:83], v[20:23]\n" \
    "v_mfma_f32_16x16x32_bf16 v[24:27], v[168:171], v[80:83], v[24:27]\n" \
    "v_mfma_f32_16x16x32_bf16 v[28:31], v[172:175], v[80:83], v[28:31]\n" \
    "s_waitcnt vmcnt(4)\n" \
    "v_mfma_f32_16x16x32_bf16 v[8:11], v[176:179], v[84:87], v[8:11]\n" \
    "v_mfma_f32_16x16x32_bf16 v[12:15], v[180:183], v[84:87], v[12:15]\n" \
    "v_mfma_f32_16x16x32_bf16 v[16:19], v[184:187], v[84:87], v[16:19]\n" \
    "v_mfma_f32_16x16x32_bf16 v[20:23], v[188:191], v[84:87], v[20:23]\n" \
    "v_mfma_f32_16x16x32_bf16 v[24:27], v[192:195], v[84:87], v[24:27]\n" \
    "v_mfma_f32_16x16x32_bf16 v[28:31], v[196:199], v[84:87], v[28:31]\n" \
    "s_waitcnt vmcnt(3)\n" \
    "v_mfma_f32_16x16x32_bf16 v[8:11], v[200:203], v[88:91], v[8:11]\n" \
    "v_mfma_f32_16x16x32_bf16 v[12:15], v[204:207], v[88:91], v[12:15]\n" \
    "v_mfma_f32_16x16x32_bf16 v[16:19], v[208:211], v[88:91], v[16:19]\n" \
    "v_mfma_f32_16x16x32_bf16 v[20:23], v[212:215], v[88:91], v[20:23]\n" \
    "v_mfma_f32_16x16x32_bf16 v[24:27], v[216:219], v[88:91], v[24:27]\n" \
    "v_mfma_f32_16x16x32_bf16 v[28:31], v[220:223], v[88:91], v[28:31]\n" \
    "s_waitcnt vmcnt(2) lgkmcnt(15)\n" \
    "v_mfma_f32_16x16x32_bf16 v[8:11], v[224:227], v[92:95], v[8:11]\n" \
    "v_mfma_f32_16x16x32_bf16 v[12:15], v[228:231], v[92:95], v[12:15]\n" \
    "v_mfma_f32_16x16x32_bf16 v[16:19], v[232:235], v[92:95], v[16:19]\n" \
    "v_mfma_f32_16x16x32_bf16 v[20:23], v[236:239], v[92:95], v[20:23]\n" \
    "v_mfma_f32_16x16x32_bf16 v[24:27], v[240:243], v[92:95], v[24:27]\n" \
    "v_mfma_f32_16x16x32_bf16 v[28:31], v[244:247], v[92:95], v[28:31]\n" \
    "s_waitcnt vmcnt(1) lgkmcnt(12)\n" \
    "v_mfma_f32_16x16x32_bf16 v[8:11], v[248:251], v[96:99], v[8:11]\n" \
    "v_mfma_f32_16x16x32_bf16 v[12:15], v[252:255], v[96:99], v[12:15]\n" \
    "v_mfma_f32_16x16x32_bf16 v[16:19], a[0:3], v[96:99], v[16:19]\n" \
    "v_mfma_f32_16x16x32_bf16 v[20:23], a[4:7], v[96:99], v[20:23]\n" \
    "v_mfma_f32_16x16x32_bf16 v[24:27], a[8:11], v[96:99], v[24:27]\n" \
    "v_mfma_f32_16x16x32_bf16 v[28:31], a[12:15], v[96:99], v[28:31]\n" \
    "s_waitcnt vmcnt(0) lgkmcnt(0)\n" \
    "v_mfma_f32_16x16x32_bf16 v[8:11], a[16:19], v[100:103], v[8:11]\n" \
    "v_mfma_f32_16x16x32_bf16 v[12:15], a[20:23], v[100:103], v[12:15]\n" \
    "v_mfma_f32_16x16x32_bf16 v[16:19], a[24:27], v[100:103], v[16:19]\n" \
    "v_mfma_f32_16x16x32_bf16 v[20:23], a[28:31], v[100:103], v[20:23]\n" \
    "v_mfma_f32_16x16x32_bf16 v[24:27], a[32:35], v[100:103], v[24:27]\n" \
    "v_mfma_f32_16x16x32_bf16 v[28:31], a[36:39], v[100:103], v[28:31]\n" \
    "s_nop 7\n" \
    "v_cvt_pk_bf16_f32 v32, v8, v9\n" \
    "v_cvt_pk_bf16_f32 v33, v10, v11\n" \
    "v_cvt_pk_bf16_f32 v34, v12, v13\n" \
    "v_cvt_pk_bf16_f32 v35, v14, v15\n" \
    "v_cvt_pk_bf16_f32 v36, v16, v17\n" \
    "v_cvt_pk_bf16_f32 v37, v18, v19\n" \
    "v_cvt_pk_bf16_f32 v38, v20, v21\n" \
    "v_cvt_pk_bf16_f32 v39, v22, v23\n" \
    "v_cvt_pk_bf16_f32 v40, v24, v25\n" \
    "v_cvt_pk_bf16_f32 v41, v26, v27\n" \
    "v_cvt_pk_bf16_f32 v42, v28, v29\n" \
    "v_cvt_pk_bf16_f32 v43, v30, v31\n" \
    "s_nop 1\n" \
    "v_permlane16_swap_b32 v32, v34\n" \
    "v_permlane16_swap_b32 v33, v35\n" \
    "v_permlane16_swap_b32 v36, v38\n" \
    "v_permlane16_swap_b32 v37, v39\n" \
    "v_permlane16_swap_b32 v40, v42\n" \
    "v_permlane16_swap_b32 v41, v43\n" \
    "s_nop 0\n" \
    "buffer_store_dwordx4 v[32:35], v106, s[88:91], s47 offen offset:0 sc1\n" \
    "buffer_store_dwordx4 v[36:39], v106, s[88:91], s47 offen offset:64 sc1\n" \
    "buffer_store_dwordx4 v[40:43], v106, s[88:91], s47 offen offset:128 sc1\n" \
    "s_branch K_END%=\n" \
    "FA_SPIN%=:\n" \
    "s_mov_b32 s66, 0x40000\n" \
    "FA_L%=:\n" \
    "s_sleep 4\n" \
    "buffer_load_dwordx4 v[40:43], v39, s[40:43], s86 offen sc1\n" \
    "buffer_load_dwordx4 v[10:13], v39, s[40:43], s86 offen offset:16 sc1\n" \
    "s_waitcnt vmcnt(0)\n" \
    "v_cmp_ne_u64 s[60:61], v[40:41], %0\n" \
    "v_cmp_ne_u64 s[62:63], v[42:43], %0\n" \
    "v_cmp_ne_u64 s[64:65], v[10:11], %0\n" \
    "v_cmp_ne_u64 vcc, v[12:13], %0\n" \
    "s_or_b64 s[60:61], s[60:61], s[62:63]\n" \
    "s_or_b64 s[64:65], s[64:65], vcc\n" \
    "s_or_b64 s[60:61], s[60:61], s[64:65]\n" \
    "s_cbranch_scc0 FA_OK%=\n" \
    "s_sub_u32 s66, s66, 1\n" \
    "s_cmp_lg_u32 s66, 0\n" \
    "s_cbranch_scc1 FA_L%=\n" \
    "s_branch FA_OK%=\n" \
    "F1_SPIN%=:\n" \
    "s_mov_b32 s72, 0x40000\n" \
    "F1_L%=:\n" \
    "s_sleep 4\n" \
    "buffer_load_dwordx2 v[40:41], v39, s[40:43], s71 offen offset:2048 sc1\n" \
    "s_waitcnt vmcnt(0)\n" \
    "v_cmp_ne_u64 vcc, v[40:41], %0\n" \
    "s_cmp_lg_u64 vcc, 0\n" \
    "s_cbranch_scc0 F1_OK%=\n" \
    "s_sub_u32 s72, s72, 1\n" \
    "s_cmp_lg_u32 s72, 0\n" \
    "s_cbranch_scc1 F1_L%=\n" \
    "s_branch F1_OK%=\n" \
    "F2_CHK%=:\n" \
    "v_cmp_ne_u64 vcc, v[222:223], %0\n" \
    "s_nop 1\n" \
    "s_cmp_eq_u64 vcc, 0\n" \
    "s_cbranch_scc1 F2_OK%=\n" \
    "F2_SPIN%=:\n" \
    "s_mov_b32 s72, 0x40000\n" \
    "F2_L%=:\n" \
    "s_sleep 4\n" \
    "buffer_load_dwordx2 v[222:223], v221, s[40:43], s71 offen sc1\n" \
    "s_waitcnt vmcnt(0)\n" \
    "v_cmp_ne_u64 vcc, v[222:223], %0\n" \
    "s_cmp_lg_u64 vcc, 0\n" \
    "s_cbranch_scc0 F2_OK%=\n" \
    "s_sub_u32 s72, s72, 1\n" \
    "s_cmp_lg_u32 s72, 0\n" \
    "s_cbranch_scc1 F2_L%=\n" \
    "s_branch F2_OK%=\n" \
    "TP_SPIN%=:\n" \
    "s_mov_b32 s60, 0x20000\n" \
    "buffer_load_dwordx2 v[96:97], v104, s[40:43], s52 offen sc1\n" \
    "s_sleep 2\n" \
    "buffer_load_dwordx2 v[98:99], v104, s[40:43], s52 offen sc1\n" \
    "s_sleep 2\n" \
    "buffer_load_dwordx2 v[100:101], v104, s[40:43], s52 offen sc1\n" \
    "s_sleep 2\n" \
    "TP_L%=:\n" \
    "buffer_load_dwordx2 v[102:103], v104, s[40:43], s52 offen sc1\n" \
    "s_waitcnt vmcnt(3)\n" \
    "v_cmp_ne_u64 vcc, v[96:97], %0\n" \
    "s_and_b64 vcc, vcc, s[56:57]\n" \
    "s_cbranch_scc0 TP_DONE%=\n" \
    "s_sleep 2\n" \
    "buffer_load_dwordx2 v[96:97], v104, s[40:43], s52 offen sc1\n" \
    "s_waitcnt vmcnt(3)\n" \
    "v_cmp_ne_u64 vcc, v[98:99], %0\n" \
    "s_and_b64 vcc, vcc, s[56:57]\n" \
    "s_cbranch_scc0 TP_DONE%=\n" \
    "s_sleep 2\n" \
    "buffer_load_dwordx2 v[98:99], v104, s[40:43], s52 offen sc1\n" \
    "s_waitcnt vmcnt(3)\n" \
    "v_cmp_ne_u64 vcc, v[100:101], %0\n" \
    "s_and_b64 vcc, vcc, s[56:57]\n" \
    "s_cbranch_scc0 TP_DONE%=\n" \
    "s_sleep 2\n" \
    "buffer_load_dwordx2 v[100:101], v104, s[40:43], s52 offen sc1\n" \
    "s_waitcnt vmcnt(3)\n" \
    "v_cmp_ne_u64 vcc, v[102:103], %0\n" \
    "s_and_b64 vcc, vcc, s[56:57]\n" \
    "s_cbranch_scc0 TP_DONE%=\n" \
    "s_sleep 2\n" \
    "s_sub_u32 s60, s60, 1\n" \
    "s_cmp_lg_u32 s60, 0\n" \
    "s_cbranch_scc1 TP_L%=\n" \
    "TP_DONE%=:\n" \
    "s_branch TP_OK%=\n" \
    "TO_SPIN%=:\n" \
    "s_mov_b32 s60, 0x20000\n" \
    "buffer_load_dwordx2 v[96:97], v104, s[40:43], s54 offen sc1\n" \
    "s_sleep 2\n" \
    "buffer_load_dwordx2 v[98:99], v104, s[40:43], s54 offen sc1\n" \
    "s_sleep 2\n" \
    "buffer_load_dwordx2 v[100:101], v104, s[40:43], s54 offen sc1\n" \
    "s_sleep 2\n" \
    "TO_L%=:\n" \
    "buffer_load_dwordx2 v[102:103], v104, s[40:43], s54 offen sc1\n" \
    "s_waitcnt vmcnt(3)\n" \
    "v_cmp_ne_u64 vcc, v[96:97], %0\n" \
    "s_and_b64 vcc, vcc, exec\n" \
    "s_cbranch_scc0 TO_DONE%=\n" \
    "s_sleep 2\n" \
    "buffer_load_dwordx2 v[96:97], v104, s[40:43], s54 offen sc1\n" \
    "s_waitcnt vmcnt(3)\n" \
    "v_cmp_ne_u64 vcc, v[98:99], %0\n" \
    "s_and_b64 vcc, vcc, exec\n" \
    "s_cbranch_scc0 TO_DONE%=\n" \
    "s_sleep 2\n" \
    "buffer_load_dwordx2 v[98:99], v104, s[40:43], s54 offen sc1\n" \
    "s_waitcnt vmcnt(3)\n" \
    "v_cmp_ne_u64 vcc, v[100:101], %0\n" \
    "s_and_b64 vcc, vcc, exec\n" \
    "s_cbranch_scc0 TO_DONE%=\n" \
    "s_sleep 2\n" \
    "buffer_load_dwordx2 v[100:101], v104, s[40:43], s54 offen sc1\n" \
    "s_waitcnt vmcnt(3)\n" \
    "v_cmp_ne_u64 vcc, v[102:103], %0\n" \
    "s_and_b64 vcc, vcc, exec\n" \
    "s_cbranch_scc0 TO_DONE%=\n" \
    "s_sleep 2\n" \
    "s_sub_u32 s60, s60, 1\n" \
    "s_cmp_lg_u32 s60, 0\n" \
    "s_cbranch_scc1 TO_L%=\n" \
    "TO_DONE%=:\n" \
    "s_branch TO_OK%=\n" \
    "T_IDLE%=:\n" \
    "s_waitcnt vmcnt(0)\n" \
    "s_mov_b64 exec, 1\n" \
    "buffer_store_dwordx2 %0, off, s[40:43], s45 sc1\n" \
    "s_mov_b64 exec, -1\n" \
    "s_branch K_END%=\n" \
    "T_EXACT%=:\n" \
    "s_waitcnt vmcnt(0)\n" \
    "s_cmp_lt_u32 %6, 2\n" \
    "s_cselect_b64 vcc, -1, 0\n" \
    "v_lshrrev_b32 v40, 4, v8\n" \
    "v_and_b32 v84, 31, v40\n" \
    "v_and_b32 v85, 27, v40\n" \
    "v_cmp_eq_u32 s[60:61], 18, v85\n" \
    "v_mov_b32 v85, 19\n" \
    "v_lshrrev_b32 v86, 5, v40\n" \
    "v_cndmask_b32 v85, v85, 18, s[60:61]\n" \
    "v_cmp_gt_u32 s[60:61], 18, v84\n" \
    "v_mul_u32_u24 v86, 320, v86\n" \
    "s_nop 0\n" \
    "v_cndmask_b32 v84, v85, v84, s[60:61]\n" \
    "v_lshl_add_u32 v84, v84, 4, v86\n" \
    "v_mov_b32 v97, 0\n" \
    "v_mov_b32 v93, 0\n" \
    "v_accvgpr_read_b32 v88, a192\n" \
    "v_accvgpr_read_b32 v85, a200\n" \
    "v_cndmask_b32 v88, v85, v88, vcc\n" \
    "v_accvgpr_read_b32 v89, a193\n" \
    "v_accvgpr_read_b32 v85, a201\n" \
    "v_cndmask_b32 v89, v85, v89, vcc\n" \
    "v_accvgpr_read_b32 v90, a194\n" \
    "v_accvgpr_read_b32 v85, a202\n" \
    "v_cndmask_b32 v90, v85, v90, vcc\n" \
    "v_accvgpr_read_b32 v91, a195\n" \
    "v_accvgpr_read_b32 v85, a203\n" \
    "v_cndmask_b32 v91, v85, v91, vcc\n" \
    "v_accvgpr_read_b32 v92, a208\n" \
    "v_accvgpr_read_b32 v85, a212\n" \
    "v_cndmask_b32 v92, v85, v92, vcc\n" \
    "s_mov_b32 s60, s51\n" \
    "s_mov_b32 s61, 64\n" \
    "v_mov_b32 v87, 0xff7fffff\n" \
    "T_EX00%=:\n" \
    "buffer_load_dwordx4 v[100:103], v8, s[40:43], s60 offen sc1\n" \
    "buffer_load_dword v96, v10, s[40:43], s60 offen offset:2304 sc1\n" \
    "s_waitcnt vmcnt(0)\n" \
    "v_mfma_f32_32x32x8_bf16 v[176:191], v[96:97], v[92:93], 0\n" \
    "v_mfma_f32_32x32x16_bf16 v[176:191], v[100:103], v[88:91], v[176:191]\n" \
    "s_nop 15\n" \
    "v_max3_f32 v87, v87, v176, v177\n" \
    "v_max3_f32 v87, v87, v178, v179\n" \
    "v_max3_f32 v87, v87, v180, v181\n" \
    "v_max3_f32 v87, v87, v182, v183\n" \
    "v_max3_f32 v87, v87, v184, v185\n" \
    "v_max3_f32 v87, v87, v186, v187\n" \
    "v_max3_f32 v87, v87, v188, v189\n" \
    "v_max3_f32 v87, v87, v190, v191\n" \
    "s_add_u32 s60, s60, 2560\n" \
    "s_sub_u32 s61, s61, 1\n" \
    "s_cmp_lg_u32 s61, 0\n" \
    "s_cbranch_scc1 T_EX00%=\n" \
    "v_mov_b32 v85, v87\n" \
    "s_nop 1\n" \
    "v_permlane32_swap_b32 v87, v85\n" \
    "s_nop 1\n" \
    "v_max_f32 v87, v87, v85\n" \
    "s_mov_b32 s60, s51\n" \
    "s_mov_b32 s61, 64\n" \
    "v_mov_b32 v192, 0\n" \
    "v_mov_b32 v193, 0\n" \
    "v_mov_b32 v194, 0\n" \
    "v_mov_b32 v195, 0\n" \
    "v_mov_b32 v196, 0\n" \
    "v_mov_b32 v197, 0\n" \
    "v_mov_b32 v198, 0\n" \
    "v_mov_b32 v199, 0\n" \
    "v_mov_b32 v200, 0\n" \
    "v_mov_b32 v201, 0\n" \
    "v_mov_b32 v202, 0\n" \
    "v_mov_b32 v203, 0\n" \
    "v_mov_b32 v204, 0\n" \
    "v_mov_b32 v205, 0\n" \
    "v_mov_b32 v206, 0\n" \
    "v_mov_b32 v207, 0\n" \
    "T_EX01%=:\n" \
    "buffer_load_dwordx4 v[100:103], v8, s[40:43], s60 offen sc1\n" \
    "buffer_load_dword v96, v10, s[40:43], s60 offen offset:2304 sc1\n" \
    "buffer_load_dwordx4 v[104:107], v84, s[40:43], s60 offen offset:1024 sc1\n" \
    "buffer_load_dwordx4 v[108:111], v84, s[40:43], s60 offen offset:1664 sc1\n" \
    "s_waitcnt vmcnt(0)\n" \
    "v_mfma_f32_32x32x8_bf16 v[176:191], v[96:97], v[92:93], 0\n" \
    "v_mfma_f32_32x32x16_bf16 v[176:191], v[100:103], v[88:91], v[176:191]\n" \
    "s_nop 15\n" \
    "v_sub_f32 v176, v176, v87\n" \
    "v_sub_f32 v177, v177, v87\n" \
    "v_sub_f32 v178, v178, v87\n" \
    "v_sub_f32 v179, v179, v87\n" \
    "v_sub_f32 v180, v180, v87\n" \
    "v_sub_f32 v181, v181, v87\n" \
    "v_sub_f32 v182, v182, v87\n" \
    "v_sub_f32 v183, v183, v87\n" \
    "v_sub_f32 v184, v184, v87\n" \
    "v_sub_f32 v185, v185, v87\n" \
    "v_sub_f32 v186, v186, v87\n" \
    "v_sub_f32 v187, v187, v87\n" \
    "v_sub_f32 v188, v188, v87\n" \
    "v_sub_f32 v189, v189, v87\n" \
    "v_sub_f32 v190, v190, v87\n" \
    "v_sub_f32 v191, v191, v87\n" \
    "v_exp_f32 v176, v176\n" \
    "v_exp_f32 v177, v177\n" \
    "v_exp_f32 v178, v178\n" \
    "v_exp_f32 v179, v179\n" \
    "v_exp_f32 v180, v180\n" \
    "v_exp_f32 v181, v181\n" \
    "v_exp_f32 v182, v182\n" \
    "v_exp_f32 v183, v183\n" \
    "v_exp_f32 v184, v184\n" \
    "v_exp_f32 v185, v185\n" \
    "v_exp_f32 v186, v186\n" \
    "v_exp_f32 v187, v187\n" \
    "v_exp_f32 v188, v188\n" \
    "v_exp_f32 v189, v189\n" \
    "v_exp_f32 v190, v190\n" \
    "v_exp_f32 v191, v191\n" \
    "s_nop 0\n" \
    "v_cvt_pk_bf16_f32 v112, v176, v177\n" \
    "v_cvt_pk_bf16_f32 v113, v178, v179\n" \
    "v_cvt_pk_bf16_f32 v114, v180, v181\n" \
    "v_cvt_pk_bf16_f32 v115, v182, v183\n" \
    "v_cvt_pk_bf16_f32 v116, v184, v185\n" \
    "v_cvt_pk_bf16_f32 v117, v186, v187\n" \
    "v_cvt_pk_bf16_f32 v118, v188, v189\n" \
    "v_cvt_pk_bf16_f32 v119, v190, v191\n" \
    "s_nop 1\n" \
    "v_mfma_f32_32x32x16_bf16 v[192:207], v[104:107], v[112:115], v[192:207]\n" \
    "v_mfma_f32_32x32x16_bf16 v[192:207], v[108:111], v[116:119], v[192:207]\n" \
    "s_add_u32 s60, s60, 2560\n" \
    "s_sub_u32 s61, s61, 1\n" \
    "s_cmp_lg_u32 s61, 0\n" \
    "s_cbranch_scc1 T_EX01%=\n" \
    "s_nop 15\n" \
    "v_mov_b32 v16, v192\n" \
    "v_mov_b32 v17, v193\n" \
    "v_mov_b32 v18, v194\n" \
    "v_mov_b32 v19, v195\n" \
    "v_mov_b32 v20, v196\n" \
    "v_mov_b32 v21, v197\n" \
    "v_mov_b32 v22, v198\n" \
    "v_mov_b32 v23, v199\n" \
    "v_mov_b32 v24, v200\n" \
    "v_mov_b32 v25, v201\n" \
    "v_mov_b32 v26, v202\n" \
    "v_accvgpr_read_b32 v88, a196\n" \
    "v_accvgpr_read_b32 v85, a204\n" \
    "v_cndmask_b32 v88, v85, v88, vcc\n" \
    "v_accvgpr_read_b32 v89, a197\n" \
    "v_accvgpr_read_b32 v85, a205\n" \
    "v_cndmask_b32 v89, v85, v89, vcc\n" \
    "v_accvgpr_read_b32 v90, a198\n" \
    "v_accvgpr_read_b32 v85, a206\n" \
    "v_cndmask_b32 v90, v85, v90, vcc\n" \
    "v_accvgpr_read_b32 v91, a199\n" \
    "v_accvgpr_read_b32 v85, a207\n" \
    "v_cndmask_b32 v91, v85, v91, vcc\n" \
    "v_accvgpr_read_b32 v92, a210\n" \
    "v_accvgpr_read_b32 v85, a214\n" \
    "v_cndmask_b32 v92, v85, v92, vcc\n" \
    "s_mov_b32 s60, s51\n" \
    "s_mov_b32 s61, 64\n" \
    "v_mov_b32 v87, 0xff7fffff\n" \
    "T_EX10%=:\n" \
    "buffer_load_dwordx4 v[100:103], v8, s[40:43], s60 offen sc1\n" \
    "buffer_load_dword v96, v10, s[40:43], s60 offen offset:2304 sc1\n" \
    "s_waitcnt vmcnt(0)\n" \
    "v_mfma_f32_32x32x8_bf16 v[176:191], v[96:97], v[92:93], 0\n" \
    "v_mfma_f32_32x32x16_bf16 v[176:191], v[100:103], v[88:91], v[176:191]\n" \
    "s_nop 15\n" \
    "v_max3_f32 v87, v87, v176, v177\n" \
    "v_max3_f32 v87, v87, v178, v179\n" \
    "v_max3_f32 v87, v87, v180, v181\n" \
    "v_max3_f32 v87, v87, v182, v183\n" \
    "v_max3_f32 v87, v87, v184, v185\n" \
    "v_max3_f32 v87, v87, v186, v187\n" \
    "v_max3_f32 v87, v87, v188, v189\n" \
    "v_max3_f32 v87, v87, v190, v191\n" \
    "s_add_u32 s60, s60, 2560\n" \
    "s_sub_u32 s61, s61, 1\n" \
    "s_cmp_lg_u32 s61, 0\n" \
    "s_cbranch_scc1 T_EX10%=\n" \
    "v_mov_b32 v85, v87\n" \
    "s_nop 1\n" \
    "v_permlane32_swap_b32 v87, v85\n" \
    "s_nop 1\n" \
    "v_max_f32 v87, v87, v85\n" \
    "s_mov_b32 s60, s51\n" \
    "s_mov_b32 s61, 64\n" \
    "v_mov_b32 v192, 0\n" \
    "v_mov_b32 v193, 0\n" \
    "v_mov_b32 v194, 0\n" \
    "v_mov_b32 v195, 0\n" \
    "v_mov_b32 v196, 0\n" \
    "v_mov_b32 v197, 0\n" \
    "v_mov_b32 v198, 0\n" \
    "v_mov_b32 v199, 0\n" \
    "v_mov_b32 v200, 0\n" \
    "v_mov_b32 v201, 0\n" \
    "v_mov_b32 v202, 0\n" \
    "v_mov_b32 v203, 0\n" \
    "v_mov_b32 v204, 0\n" \
    "v_mov_b32 v205, 0\n" \
    "v_mov_b32 v206, 0\n" \
    "v_mov_b32 v207, 0\n" \
    "T_EX11%=:\n" \
    "buffer_load_dwordx4 v[100:103], v8, s[40:43], s60 offen sc1\n" \
    "buffer_load_dword v96, v10, s[40:43], s60 offen offset:2304 sc1\n" \
    "buffer_load_dwordx4 v[104:107], v84, s[40:43], s60 offen offset:1024 sc1\n" \
    "buffer_load_dwordx4 v[108:111], v84, s[40:43], s60 offen offset:1664 sc1\n" \
    "s_waitcnt vmcnt(0)\n" \
    "v_mfma_f32_32x32x8_bf16 v[176:191], v[96:97], v[92:93], 0\n" \
    "v_mfma_f32_32x32x16_bf16 v[176:191], v[100:103], v[88:91], v[176:191]\n" \
    "s_nop 15\n" \
    "v_sub_f32 v176, v176, v87\n" \
    "v_sub_f32 v177, v177, v87\n" \
    "v_sub_f32 v178, v178, v87\n" \
    "v_sub_f32 v179, v179, v87\n" \
    "v_sub_f32 v180, v180, v87\n" \
    "v_sub_f32 v181, v181, v87\n" \
    "v_sub_f32 v182, v182, v87\n" \
    "v_sub_f32 v183, v183, v87\n" \
    "v_sub_f32 v184, v184, v87\n" \
    "v_sub_f32 v185, v185, v87\n" \
    "v_sub_f32 v186, v186, v87\n" \
    "v_sub_f32 v187, v187, v87\n" \
    "v_sub_f32 v188, v188, v87\n" \
    "v_sub_f32 v189, v189, v87\n" \
    "v_sub_f32 v190, v190, v87\n" \
    "v_sub_f32 v191, v191, v87\n" \
    "v_exp_f32 v176, v176\n" \
    "v_exp_f32 v177, v177\n" \
    "v_exp_f32 v178, v178\n" \
    "v_exp_f32 v179, v179\n" \
    "v_exp_f32 v180, v180\n" \
    "v_exp_f32 v181, v181\n" \
    "v_exp_f32 v182, v182\n" \
    "v_exp_f32 v183, v183\n" \
    "v_exp_f32 v184, v184\n" \
    "v_exp_f32 v185, v185\n" \
    "v_exp_f32 v186, v186\n" \
    "v_exp_f32 v187, v187\n" \
    "v_exp_f32 v188, v188\n" \
    "v_exp_f32 v189, v189\n" \
    "v_exp_f32 v190, v190\n" \
    "v_exp_f32 v191, v191\n" \
    "s_nop 0\n" \
    "v_cvt_pk_bf16_f32 v112, v176, v177\n" \
    "v_cvt_pk_bf16_f32 v113, v178, v179\n" \
    "v_cvt_pk_bf16_f32 v114, v180, v181\n" \
    "v_cvt_pk_bf16_f32 v115, v182, v183\n" \
    "v_cvt_pk_bf16_f32 v116, v184, v185\n" \
    "v_cvt_pk_bf16_f32 v117, v186, v187\n" \
    "v_cvt_pk_bf16_f32 v118, v188, v189\n" \
    "v_cvt_pk_bf16_f32 v119, v190, v191\n" \
    "s_nop 1\n" \
    "v_mfma_f32_32x32x16_bf16 v[192:207], v[104:107], v[112:115], v[192:207]\n" \
    "v_mfma_f32_32x32x16_bf16 v[192:207], v[108:111], v[116:119], v[192:207]\n" \
    "s_add_u32 s60, s60, 2560\n" \
    "s_sub_u32 s61, s61, 1\n" \
    "s_cmp_lg_u32 s61, 0\n" \
    "s_cbranch_scc1 T_EX11%=\n" \
    "s_nop 15\n" \
    "v_mov_b32 v28, v192\n" \
    "v_mov_b32 v29, v193\n" \
    "v_mov_b32 v30, v194\n" \
    "v_mov_b32 v31, v195\n" \
    "v_mov_b32 v32, v196\n" \
    "v_mov_b32 v33, v197\n" \
    "v_mov_b32 v34, v198\n" \
    "v_mov_b32 v35, v199\n" \
    "v_mov_b32 v36, v200\n" \
    "v_mov_b32 v37, v201\n" \
    "v_mov_b32 v38, v202\n" \
    "s_branch T_NORM%=\n" \
    "K_END%=:\n" \
    "FUSED_CODE_END:\n"
#define MAIN_ASM_CLOB "v8", "v9", "v10", "v11", "v12", "v13", "v14", "v15", "v16", "v17", "v18", "v19", "v20", "v21", "v22", "v23", "v24", "v25", "v26", "v27", "v28", "v29", "v30", "v31", "v32", "v33", "v34", "v35", "v36", "v37", "v38", "v39", "v40", "v41", "v42", "v43", "v44", "v45", "v46", "v47", "v48", "v49", "v50", "v51", "v52", "v53", "v54", "v55", "v56", "v57", "v58", "v59", "v60", "v61", "v62", "v63", "v64", "v65", "v66", "v67", "v68", "v69", "v70", "v71", "v72", "v73", "v74", "v75", "v76", "v77", "v78", "v79", "v80", "v81", "v82", "v83", "v84", "v85", "v86", "v87", "v88", "v89", "v90", "v91", "v92", "v93", "v94", "v95", "v96", "v97", "v98", "v99", "v100", "v101", "v102", "v103", "v104", "v105", "v106", "v107", "v108", "v109", "v110", "v111", "v112", "v113", "v114", "v115", "v116", "v117", "v118", "v119", "v120", "v121", "v122", "v123", "v124", "v125", "v126", "v127", "v128", "v129", "v130", "v131", "v132", "v133", "v134", "v135", "v136", "v137", "v138", "v139", "v140", "v141", "v142", "v143", "v144", "v145", "v146", "v147", "v148", "v149", "v150", "v151", "v152", "v153", "v154", "v155", "v156", "v157", "v158", "v159", "v160", "v161", "v162", "v163", "v164", "v165", "v166", "v167", "v168", "v169", "v170", "v171", "v172", "v173", "v174", "v175", "v176", "v177", "v178", "v179", "v180", "v181", "v182", "v183", "v184", "v185", "v186", "v187", "v188", "v189", "v190", "v191", "v192", "v193", "v194", "v195", "v196", "v197", "v198", "v199", "v200", "v201", "v202", "v203", "v204", "v205", "v206", "v207", "v208", "v209", "v210", "v211", "v212", "v213", "v214", "v215", "v216", "v217", "v218", "v219", "v220", "v221", "v222", "v223", "v224", "v225", "v226", "v227", "v228", "v229", "v230", "v231", "v232", "v233", "v234", "v235", "v236", "v237", "v238", "v239", "v240", "v241", "v242", "v243", "v244", "v245", "v246", "v247", "v248", "v249", "v250", "v251", "v252", "v253", "v254", "v255", "a0", "a1", "a2", "a3", "a4", "a5", "a6", "a7", "a8", "a9", "a10", "a11", "a12", "a13", "a14", "a15", "a16", "a17", "a18", "a19", "a20", "a21", "a22", "a23", "a24", "a25", "a26", "a27", "a28", "a29", "a30", "a31", "a32", "a33", "a34", "a35", "a36", "a37", "a38", "a39", "a40", "a41", "a42", "a43", "a44", "a45", "a46", "a47", "a48", "a49", "a50", "a51", "a52", "a53", "a54", "a55", "a56", "a57", "a58", "a59", "a60", "a61", "a62", "a63", "a64", "a65", "a66", "a67", "a68", "a69", "a70", "a71", "a72", "a73", "a74", "a75", "a76", "a77", "a78", "a79", "a80", "a81", "a82", "a83", "a84", "a85", "a86", "a87", "a88", "a89", "a90", "a91", "a92", "a93", "a94", "a95", "a96", "a97", "a98", "a99", "a100", "a101", "a102", "a103", "a104", "a105", "a106", "a107", "a108", "a109", "a110", "a111", "a112", "a113", "a114", "a115", "a116", "a117", "a118", "a119", "a120", "a121", "a122", "a123", "a124", "a125", "a126", "a127", "a128", "a129", "a130", "a131", "a132", "a133", "a134", "a135", "a136", "a137", "a138", "a139", "a140", "a141", "a142", "a143", "a144", "a145", "a146", "a147", "a148", "a149", "a150", "a151", "a152", "a153", "a154", "a155", "a156", "a157", "a158", "a159", "a160", "a161", "a162", "a163", "a164", "a165", "a166", "a167", "a168", "a169", "a170", "a171", "a172", "a173", "a174", "a175", "a176", "a177", "a178", "a179", "a180", "a181", "a182", "a183", "a184", "a185", "a186", "a187", "a188", "a189", "a190", "a191", "a192", "a193", "a194", "a195", "a196", "a197", "a198", "a199", "a200", "a201", "a202", "a203", "a204", "a205", "a206", "a207", "a208", "a209", "a210", "a211", "a212", "a213", "a214", "a215", "a216", "a217", "a218", "a219", "a220", "a221", "a222", "a223", "a224", "a225", "a226", "a227", "a228", "a229", "a230", "a231", "a232", "a233", "a234", "a235", "a236", "a237", "a238", "a239", "a240", "a241", "a242", "a243", "a244", "a245", "a246", "a247", "a248", "a249", "a250", "a251", "a252", "a253", "a254", "a255", "s36", "s37", "s38", "s39", "s40", "s41", "s42", "s43", "s44", "s45", "s46", "s47", "s48", "s49", "s50", "s51", "s52", "s53", "s54", "s55", "s56", "s57", "s58", "s59", "s60", "s61", "s62", "s63", "s64", "s65", "s66", "s67", "s68", "s69", "s70", "s71", "s72", "s73", "s74", "s75", "s76", "s77", "s78", "s79", "s80", "s81", "s82", "s83", "s84", "s85", "s86", "s87", "s88", "s89", "s90", "s91", "s92", "s93", "s94", "s95", "s96", "s97", "s98", "s99", "scc", "vcc", "memory"
#define AI __attribute__((always_inline))
#define PPX 4
constexpr unsigned RS1 = 36864;
typedef unsigned long long u64;
DI u64 pack2(unsigned lo, unsigned hi) { return (u64)lo | ((u64)hi << 32); }
DI u64 spack(unsigned lo, unsigned hi) { return pack2(rfl(lo), rfl(hi)); }
constexpr unsigned OOB = 0xF0000000u;
constexpr unsigned STG2 = 45056;
constexpr unsigned FL_EPOCH = (unsigned)FL_OFF, FL_F2 = FL_EPOCH + 256, FL_F1 = FL_F2 + 2048, FL_FA = FL_EPOCH + 8192, FL_P = FL_EPOCH + 24576, FL_O = FL_EPOCH + 32768;

DI u64 gu_piece(const u16* wg, const u16* wu, int c) {
  const int j = c / 1152, rem = c - 1152 * j;
  return (rem < 576 ? (u64)wg + rem * 9216 : (u64)wu + (rem - 576) * 9216) + j * 2304;
}
__global__ __launch_bounds__(256, 1) void fused_kernel(const u16* __restrict__ x, const u16* __restrict__ wqkv, const u16* __restrict__ wo, const u16* __restrict__ wg,
                                                       const u16* __restrict__ wu, const u16* __restrict__ wd, char* __restrict__ ws, u16* __restrict__ out) {
  constexpr int KID = 2;
  extern __shared__ __attribute__((aligned(16))) char lds[];
  const unsigned lds0 = (unsigned)(uintptr_t)lds;
  const int blk = blockIdx.x, xg = blk & 7, rr = blk >> 3;
  const int lane = threadIdx.x & 63, w = rfl(threadIdx.x >> 6);
  const int panel = (xg % (8 / PPX)) * PPX + (rr % PPX), cslot = (xg / (8 / PPX)) * (32 / PPX) + rr / PPX, tile0 = panel * 8 + 2 * w;
  const int npre = cslot < 22 ? 1 : (cslot < 26 ? 2 : 4), ngu = cslot < 22 ? 5 : (cslot < 26 ? 4 : 3), NP = npre + 2 * ngu;
  const int pre0 = cslot < 22 ? cslot : (cslot < 26 ? 2 * cslot - 22 : 4 * cslot - 74);
  const unsigned c0 = NP == 11 ? RS1 : 2 * RS1;
  const unsigned stg = lds0 + (NP == 11 && w >= 2 ? 3 * RS1 + (w - 2) * 18432 : w * 18432);
  unsigned stampv = 0;
  const int gw = rfl(blk * 4 + w);
  asm volatile(HEAD_ASM_TEXT : "+v"(stampv) : "s"((u64)x), "s"(spack(tile0, stg)), "s"(gw), "s"((u64)ws),
               "s"((u64)wqkv + (gw < 864 ? gw : 0) * 2304), "s"(gu_piece(wg, wu, gw)), "s"(gu_piece(wg, wu, 1024 + gw)) : HEAD_ASM_CLOB);
  const int q = lane, e = q - npre, job = e >> 1, pi = cslot >> 1;
  const int rank = cslot < 22 ? (job == 0 ? 0 : 16 * job - 3) + pi : (cslot < 26 ? (job == 0 ? 11 : 16 * job + 8) + pi - 11 : 26 + 16 * job + pi - 13);
#define PERM 3
  const int j5 = cslot < 26 ? job : job + 1;
  const int rank2 = cslot < 22 ? (job == 0 ? 0 : 16 * job - 3) + (pi + PERM * job) % 11 : (j5 == 0 ? pi : 16 * j5 + 8 + (pi - 11 + 2 * j5) % 5);
  const int hb = 2 * rank2 + (cslot & 1);
  const int st = pre0 + q, col = st < 18 ? 1152 + 32 * st : 32 * (st - 18);
  const bool pre = q < npre;
  u64 tag = pre ? (u64)wqkv + col * 2 : ((e & 1) ? (u64)wu : (u64)wg) + hb * 64;
  const unsigned size = q < NP ? (pre ? 3456u * 576u - col * 2 : 9216u * 576u - hb * 64) : 0u;
  const unsigned tcol = st < 18 ? 32 * st : (col >= 576 ? (col - 576) | 0x8000 : col);
  const int kh = w >> 1, head = 4 * xg + (rr >> 3), qt0 = (rr & 7) * 8 + 4 * (w & 1);
  const int ntile = rr % 6, cmb = 5 * xg + (rr < 30 ? rr / 6 : 0), split = cmb >> 3, gp = cmb & 7;
  const int npairs = rr < 30 ? (split < 2 ? 15 : 14) : 0, mt0 = 16 * gp + 4 * w;
  if (q >= 16) {
    const int k = q - 16, c = (k - 1) * 1024 + gw, d = (k - 6) * 1024 + gw;
    tag = (u64)x + gw * 2304;
    if (k == 0) { if (gw < 864) tag = (u64)wqkv + gw * 2304; }
    else if (k < 6) { if (c < 4608) tag = gu_piece(wg, wu, c); }
    else if (k < 9) { if (d < 2304) tag = (u64)wd + d * 2304; else if (d < 2592) tag = (u64)wo + (d - 2304) * 2304; }
  }
  u64 tsp = pack2(size, pre ? 3456u : 9216u);
  {
    const int mt = 16 * xg + 4 * (rr / 6) + w;
    const unsigned mainoff = (head >> 1) * 1024 + (head & 1) * 512, tailoff = (16 + (head >> 4)) * 1024 + ((head >> 2) & 3) * 256 + (head & 3) * 4;
    if (q == 40) tsp = pack2((unsigned)O_OFF + (qt0 + 2 * kh) * 36 * 1024 + mainoff, FL_O + (((rr & 7) * 4 + 2 * (w & 1) + kh) * 32 + head) * 8);
    if (q == 41) tsp = pack2(FL_P + ((gp * 4 + w) * 32 + (rr < 30 ? split * 6 + ntile : rr)) * 8, rr < 24 ? (unsigned)(mt * 16 * 1152 + ntile * 192) : OOB);
    if (q == 42) tsp = pack2((unsigned)P_OFF + (mt * 18 + 3 * ntile) * 1024, (unsigned)O_OFF + mt * 18 * 1024);
    if (q == 43) tsp = pack2(tailoff - mainoff, (unsigned)KV_OFF + head * 64 * KVT);
    if (q == 44) tsp = pack2(FL_P + (xg * 4 + rr / 6) * 256, 0x01041041u << ntile);
    if (q == 45) tsp = pack2(FL_O + (xg * 4 + rr / 6) * 256, 0);
    if (q == 46) tsp = (u64)out;
  }
  ST(0);
  asm volatile(MAIN_ASM_TEXT
               : "+v"(tag), "+v"(stampv)
               : "s"((u64)x), "s"((u64)ws), "s"((u64)wd), "s"((u64)wo), "s"(w), "s"(rfl(lds0)), "s"(spack(tile0, stg)), "s"(spack(npre, ngu - 1)), "s"(spack(c0, pre0)),
                 "s"(spack((unsigned)Q_OFF + (head * 64 + qt0) * QT, (unsigned)KV_OFF + head * 64 * KVT)),
                 "s"(spack((unsigned)KV_OFF + (head * 64 + 32 * kh) * KVT, (unsigned)H_OFF + mt0 * HSTRIDE + 2048 * (split < 5 ? split : 0))),
                 "s"(spack(npairs, split * 64 * 1152 + ntile * 192)),
                 "s"(spack(ntile * 192 | (gp << 16) | (xg << 20) | (rr < 24 ? 0x80000000u : 0u), npairs ? (unsigned)P_OFF + ((split * 128 + mt0) * 18 + 3 * ntile) * 1024 : OOB)),
                 "s"(spack(FL_FA + blk * 8, FL_F2 + (panel * 32 + cslot) * 8)),
                 "v"(tsp), "v"(pack2((unsigned)H_OFF + 2 * tile0 * HSTRIDE + hb * 1024, tcol))
               : MAIN_ASM_CLOB);
  ST(3);
}

extern "C" void kernel_launch(void* const* d_in, const int* in_sizes, int n_in, void* d_out, int out_size, void* d_ws, size_t ws_size, hipStream_t stream) {
  (void)hipFuncSetAttribute((const void*)fused_kernel, hipFuncAttributeMaxDynamicSharedMemorySize, 163840);
  fused_kernel<<<256, 256, 163840, stream>>>((const u16*)d_in[0], (const u16*)d_in[1], (const u16*)d_in[2], (const u16*)d_in[4], (const u16*)d_in[5], (const u16*)d_in[6],
                                             (char*)d_ws, (u16*)d_out);
}
```
